```python
import torch

def _softmax(x, axis=-1):
    return torch.softmax(x, dim=axis)
def _gelu(x):
    return torch.nn.functional.gelu(x, approximate="tanh")
def _silu(x):
    return torch.nn.functional.silu(x)
def _sigmoid(x):
    return torch.sigmoid(x)
def _layernorm(x, axis=-1, eps=1e-5):
    m = x.mean(dim=axis, keepdim=True)
    v = x.var(dim=axis, keepdim=True, unbiased=False)
    return (x - m) / torch.sqrt(v + eps)
def _scatter_add(tgt, idx, src, axis=0):
    return tgt.clone().index_add_(axis, idx, src)
def _segment_sum(src, seg, n):
    out = torch.zeros((n,) + src.shape[1:], dtype=src.dtype)
    return out.index_add_(0, seg, src)
def _pad(x, pw):
    flat = tuple(v for pair in reversed(pw) for v in pair)
    return torch.nn.functional.pad(x, flat)

kernel_name = 'recurrent_scan__sliding_local_dilated__dense_activation_silu__prompt'

def setup_inputs():

    _w = (lambda a: (lambda t: t.to(torch.bfloat16) if t.is_floating_point() else t)(torch.as_tensor(a)))
    def _A_setup():
        B, S, D = 1, 2048, 640
        return {
            "x": _w((lambda *s: torch.randn(*s, dtype=torch.bfloat16) * 0.1)(B, S, D)),
            "wqkv": _w((lambda *s: torch.randn(*s, dtype=torch.bfloat16) * 0.2)(D, 3 * D)),
            "wo": _w((lambda *s: torch.randn(*s, dtype=torch.bfloat16) * 0.4)(D, D)),
        }
    _A_in = _A_setup()

    def _B_setup():
        B, S, D, F = 1, 2048, 640, 1728
        return {
            "x": _w((lambda *s: torch.randn(*s, dtype=torch.bfloat16) * 0.1)(B, S, D)),
            "w1": _w((lambda *s: torch.randn(*s, dtype=torch.bfloat16) * 3.2)(D, F) * (D ** -0.5)),
            "w2": _w((lambda *s: torch.randn(*s, dtype=torch.bfloat16) * 3.2)(F, D) * (F ** -0.5)),
        }
    _B_in = _B_setup()

    return {
        "A_x": _A_in["x"],
        "A_wqkv": _A_in["wqkv"],
        "A_wo": _A_in["wo"],
        "B_x": _B_in["x"],
        "B_w1": _B_in["w1"],
        "B_w2": _B_in["w2"],
    }

def reference(A_x, A_wqkv, A_wo, B_x, B_w1, B_w2):
    def _A_ref(x, wqkv, wo):
        B, S, D = x.shape
        H = 32; Dh = D // H
        W, dil = 8, 2
        qkv = (lambda x, *d: x.permute(*d))((x @ wqkv).reshape(B, S, 3, H, Dh), 2, 0, 3, 1, 4)
        q, k, v = qkv[0], qkv[1], qkv[2]
        scale = Dh ** -0.5
        pos = torch.arange(S)
        off = torch.arange(W) * dil
        idx = pos[:, None] - off[None, :]
        mask = idx >= 0
        idx_c = torch.where(mask, idx, 0).reshape(-1)
        kg = k[:, :, idx_c, :].reshape(B, H, S, W, Dh)
        vg = v[:, :, idx_c, :].reshape(B, H, S, W, Dh)
        logits = torch.einsum("bhsd,bhswd->bhsw", q, kg) * scale
        logits = torch.where(mask[None, None, :, :], logits, -1e9)
        attn = _softmax(logits, axis=-1)
        o = torch.einsum("bhsw,bhswd->bhsd", attn, vg)
        return (lambda x, *d: x.permute(*d))(o, 0, 2, 1, 3).reshape(B, S, D) @ wo

    def _B_ref(x, w1, w2):
        h = torch.einsum("bsd,df->bsf", x, w1)
        h = _silu(h)
        out = torch.einsum("bsf,fd->bsd", h, w2)
        return out

    x = A_x
    xn = _layernorm(x, axis=-1)
    s = _A_ref(xn, A_wqkv, A_wo)
    g = _silu(_B_ref(xn, B_w1, B_w2))
    return x + s * g
```

```cpp
#include <hip/hip_runtime.h>
#include <hip/hip_bf16.h>
#include <stdint.h>
typedef unsigned short u16;
__device__ __forceinline__ float bf2f(u16 b){ return __builtin_bit_cast(float,(unsigned)b<<16); }
__device__ __forceinline__ u16 f2bf(float f){ unsigned u=__builtin_bit_cast(unsigned,f); u+=0x7FFFu+((u>>16)&1u); return (u16)(u>>16);}
__device__ __forceinline__ float rbf(float f){ return bf2f(f2bf(f)); }
#define MIMIC 0
#define RB(x) (x)
constexpr int S=2048,D=640,F=1728,H=32,DH=20;

__global__ void k_ln(const u16* x, float* xn){
  int row=blockIdx.x; int t=threadIdx.x;
  float v[10]; float s=0;
  for(int i=0;i<10;i++){ v[i]=bf2f(x[row*D+t+64*i]); s+=v[i]; }
  for(int o=32;o;o>>=1) s+=__shfl_xor(s,o);
  float mean=s/D; float q=0;
  for(int i=0;i<10;i++){ float d=v[i]-mean; q+=d*d; }
  for(int o=32;o;o>>=1) q+=__shfl_xor(q,o);
  float var=q/D;
  float m=RB(mean); float vb=RB(var); float sd=RB(sqrtf(RB(vb+1e-5f)));
  for(int i=0;i<10;i++){ float d=RB(v[i]-m); xn[row*D+t+64*i]=RB(d/sd); }
}
__global__ void k_gemm(const float* A,const u16* B,float* C,int M,int N,int K,int act){
  __shared__ float sa[16][17], sb[16][17];
  int tx=threadIdx.x, ty=threadIdx.y; int r=blockIdx.y*16+ty, c=blockIdx.x*16+tx; float acc=0;
  for(int k0=0;k0<K;k0+=16){
    sa[ty][tx]=A[(size_t)r*K+k0+tx]; sb[ty][tx]=bf2f(B[(size_t)(k0+ty)*N+c]); __syncthreads();
    for(int k=0;k<16;k++) acc+=sa[ty][k]*sb[k][tx]; __syncthreads();
  }
  acc=RB(acc);
  if(act){ acc=RB(acc/(1.f+expf(-acc))); }
  C[(size_t)r*N+c]=acc;
}
__global__ void k_attn(const float* qkv,float* o){
  int idx=blockIdx.x*blockDim.x+threadIdx.x; if(idx>=S*H) return; int s=idx/H,h=idx%H;
  const float* q=qkv+(size_t)s*3*D+h*DH; float lg[8]; float mx=-3e38f; const float scale=RB(0.22360679774997896f);
  for(int w=0;w<8;w++){ int p=s-2*w; if(p>=0){ const float* k=qkv+(size_t)p*3*D+D+h*DH; float a=0; for(int d=0;d<DH;d++) a+=q[d]*k[d]; a=RB(a); a=RB(a*0.22360679774997896f); lg[w]=a;} else lg[w]=-1e9f; mx=fmaxf(mx,lg[w]); }
  float sum=0; for(int w=0;w<8;w++){ lg[w]=expf(lg[w]-mx); sum+=lg[w]; }
  for(int w=0;w<8;w++) lg[w]=RB(lg[w]/sum);
  for(int d=0;d<DH;d++){ float a=0; for(int w=0;w<8;w++){ int p=s-2*w; if(p<0)p=0; a+=lg[w]*qkv[(size_t)p*3*D+2*D+h*DH+d]; } o[(size_t)s*D+h*DH+d]=RB(a);}
}
__global__ void k_fin(const u16* x,const float* s,const float* g,u16* out){
  int i=blockIdx.x*blockDim.x+threadIdx.x; float v=bf2f(x[i])+RB(s[i]*g[i]); out[i]=f2bf(v);
}
extern "C" void kernel_launch(void* const* d_in, const int* in_sizes, int n_in, void* d_out, int out_size, void* d_ws, size_t ws_size, hipStream_t stream){
  const u16* x=(const u16*)d_in[0]; const u16* wqkv=(const u16*)d_in[1]; const u16* wo=(const u16*)d_in[2]; const u16* w1=(const u16*)d_in[4]; const u16* w2=(const u16*)d_in[5];
  float* ws=(float*)d_ws; float* xn=ws; float* qkv=xn+S*D; float* h=qkv+S*3*D; float* o=h+S*F; float* s=o+S*D; float* g=s+S*D;
  k_ln<<<S,64,0,stream>>>(x,xn);
  k_gemm<<<dim3(3*D/16,S/16),dim3(16,16),0,stream>>>(xn,wqkv,qkv,S,3*D,D,0);
  k_gemm<<<dim3(F/16,S/16),dim3(16,16),0,stream>>>(xn,w1,h,S,F,D,1);
  k_attn<<<S*H/256,256,0,stream>>>(qkv,o);
  k_gemm<<<dim3(D/16,S/16),dim3(16,16),0,stream>>>(o,wo,s,S,D,D,0);
  k_gemm<<<dim3(D/16,S/16),dim3(16,16),0,stream>>>(h,w2,g,S,D,F,1);
  k_fin<<<S*D/256,256,0,stream>>>(x,s,g,(u16*)d_out);
}
```

```cpp
#include <hip/hip_runtime.h>
#include <stdint.h>
#include <type_traits>

#define GAS __attribute__((address_space(1)))
#define LAS __attribute__((address_space(3)))
typedef unsigned short u16;
typedef unsigned long long u64;
typedef __bf16 v2bf __attribute__((ext_vector_type(2)));
typedef __bf16 v4bf __attribute__((ext_vector_type(4)));
typedef __bf16 v8bf __attribute__((ext_vector_type(8)));
typedef float f32x2 __attribute__((ext_vector_type(2)));
typedef float f32x4 __attribute__((ext_vector_type(4)));
typedef unsigned v4u __attribute__((ext_vector_type(4)));
typedef unsigned v2u __attribute__((ext_vector_type(2)));
typedef int v4i __attribute__((ext_vector_type(4)));

namespace {
constexpr int SEQ = 2048, DM = 640, FF = 1728;
constexpr int NCT = 148;
constexpr int H_BYTES = 64 * NCT * 1024;
constexpr size_t CTRL_OFF = 16u << 20;
constexpr int EPOCH_OFF = 0, FLAG_OFF = 8192, STAT_OFF = 65536;
constexpr int A_BYTES = 144 * 128, B_BYTES = 64 * 512, ST_BYTES = A_BYTES + B_BYTES;
constexpr int CS_OFF = 3 * ST_BYTES, RS_OFF = CS_OFF + 1024;
constexpr int IMG_LD = 488;
constexpr int LDS_BYTES = 163840;
constexpr int OOB = 0x7FF00000;
constexpr unsigned SPIN_CAP = 1u << 18;
#define STAMP(i)

__device__ __forceinline__ v4i mkrs(const void* p, int bytes) {
  u64 a = (u64)p; v4i r; r.x = (int)(unsigned)a; r.y = (int)((unsigned)(a >> 32) & 0xFFFFu); r.z = bytes; r.w = 0x00020000; return r;
}
__device__ __forceinline__ void dma16(v4i rs, unsigned lds, int voff, int soff) {
  asm volatile("s_mov_b32 m0, %0\n\ts_nop 0\n\tbuffer_load_dwordx4 %1, %2, %3 offen lds" ::"s"(lds), "v"(voff), "s"(rs), "s"(soff));
}
#define SC1A " sc1"
template <int IMM> __device__ __forceinline__ v4u ld128_sc1(v4i rs, int voff, int soff) {
  v4u r; asm volatile("buffer_load_dwordx4 %0, %1, %2, %3 offen offset:%4" SC1A : "=v"(r) : "v"(voff), "s"(rs), "s"(soff), "i"(IMM)); return r;
}
template <int IMM> __device__ __forceinline__ v4u ld128(v4i rs, int voff, int soff) {
  v4u r; asm volatile("buffer_load_dwordx4 %0, %1, %2, %3 offen offset:%4" : "=v"(r) : "v"(voff), "s"(rs), "s"(soff), "i"(IMM)); return r;
}
__device__ __forceinline__ v2u ld64(v4i rs, int voff, int soff) {
  v2u r; asm volatile("buffer_load_dwordx2 %0, %1, %2, %3 offen" : "=v"(r) : "v"(voff), "s"(rs), "s"(soff)); return r;
}
__device__ __forceinline__ v2u ld64_sc1(v4i rs, int voff, int soff) {
  v2u r; asm volatile("buffer_load_dwordx2 %0, %1, %2, %3 offen sc1" : "=v"(r) : "v"(voff), "s"(rs), "s"(soff)); return r;
}
__device__ __forceinline__ v2u ld64_sc1_wait(v4i rs, int voff) {
  v2u r; asm volatile("buffer_load_dwordx2 %0, %1, %2, 0 offen sc1\n\ts_waitcnt vmcnt(0)" : "=&v"(r) : "v"(voff), "s"(rs) : "memory"); return r;
}
__device__ __forceinline__ void st128_sc1(v4i rs, int voff, int soff, v4u v) {
  asm volatile("buffer_store_dwordx4 %0, %1, %2, %3 offen sc1\n\ts_nop 1" ::"v"(v), "v"(voff), "s"(rs), "s"(soff) : "memory");
}
__device__ __forceinline__ void st64_sc1(v4i rs, int voff, v2u v) {
  asm volatile("buffer_store_dwordx2 %0, %1, %2, 0 offen sc1" ::"v"(v), "v"(voff), "s"(rs) : "memory");
}
template <int N> __device__ __forceinline__ void waitbar() { asm volatile("s_waitcnt vmcnt(%0) lgkmcnt(0)\n\ts_barrier" ::"i"(N) : "memory"); }
__device__ __forceinline__ void ldsbar() { asm volatile("s_waitcnt lgkmcnt(0)\n\ts_barrier" ::: "memory"); }
__device__ __forceinline__ float bflo(unsigned u) { return __builtin_bit_cast(float, u << 16); }
__device__ __forceinline__ float bfhi(unsigned u) { return __builtin_bit_cast(float, u & 0xFFFF0000u); }
__device__ __forceinline__ unsigned pk2(float a, float b) {
  v2bf v; v.x = (__bf16)a; v.y = (__bf16)b; return __builtin_bit_cast(unsigned, v);
}
__device__ __forceinline__ float rbf(float a) { return (float)(__bf16)a; }
__device__ __forceinline__ float dot2(unsigned a, unsigned b, float c) {
  return __builtin_amdgcn_fdot2_f32_bf16(__builtin_bit_cast(v2bf, a), __builtin_bit_cast(v2bf, b), c, false);
}
__device__ __forceinline__ f32x4 mfma(v8bf a, v8bf b, f32x4 c) { return __builtin_amdgcn_mfma_f32_16x16x32_bf16(a, b, c, 0, 0, 0); }
__device__ __forceinline__ v8bf trfrag(LAS char* p, int second) {
  v4bf lo = __builtin_amdgcn_ds_read_tr16_b64_v4bf16((LAS v4bf*)p);
  v4bf hi = __builtin_amdgcn_ds_read_tr16_b64_v4bf16((LAS v4bf*)(p + second));
  return __builtin_shufflevector(lo, hi, 0, 1, 2, 3, 4, 5, 6, 7);
}
__device__ __forceinline__ float row16_sum(float v) {
  v += __builtin_bit_cast(float, __builtin_amdgcn_update_dpp(0, __builtin_bit_cast(int, v), 0x128, 0xF, 0xF, false));
  v += __builtin_bit_cast(float, __builtin_amdgcn_update_dpp(0, __builtin_bit_cast(int, v), 0x124, 0xF, 0xF, false));
  v += __builtin_bit_cast(float, __builtin_amdgcn_update_dpp(0, __builtin_bit_cast(int, v), 0x122, 0xF, 0xF, false));
  v += __builtin_bit_cast(float, __builtin_amdgcn_update_dpp(0, __builtin_bit_cast(int, v), 0x121, 0xF, 0xF, false));
  return v;
}
__device__ __forceinline__ float silu_f(float y) { return y * __builtin_amdgcn_rcpf(1.0f + __builtin_amdgcn_exp2f(y * -1.4426950408889634f)); }

struct P1 {
  v4i rsA, rsB, rsH, rsC;
  int avoff, asvoff, bvoff, ldb2;
  int w, wm, lane, grp;
  int tstart, ftile0;
  int isffn, htile0;
  unsigned tag;
};

__device__ __forceinline__ void p1_issue(const P1& c, unsigned lds0, int st, int slot) {
  const unsigned base = lds0 + slot * ST_BYTES;
  if (!c.isffn && c.w < 2) dma16(c.rsA, base + c.w * 1024, c.asvoff, st * 128);
#pragma unroll
  for (int i = 0; i < 2; i++) dma16(c.rsA, base + (2 + c.w + 8 * i) * 1024, c.avoff, st * 128 + i * 64 * (DM * 2));
#pragma unroll
  for (int i = 0; i < 4; i++) dma16(c.rsB, base + A_BYTES + (c.w + 8 * i) * 1024, c.bvoff, (st * 64 + i * 16) * c.ldb2);
}

template <int NT, int NOWN, int NFOR>
__device__ __forceinline__ void phase1(const P1& c, LAS char* smem, unsigned lds0, char* ws) {
  constexpr int NB = NT + NFOR, NX = NOWN + NFOR;
  const int lane = c.lane, r = lane & 15, g = lane >> 4, q = r >> 2, p = lane & 3;
  int aad[2];
#pragma unroll
  for (int ks = 0; ks < 2; ks++) aad[ks] = r * 128 + (((4 * ks + g) ^ (r >> 1)) << 4);
  const int amain = (1 + c.wm * 4) * 2048;
  const int z = q | ((g & 1) << 2);
  int tile[NB], bad[NB];
#pragma unroll
  for (int n = 0; n < NB; n++) {
    if (n < NT) { int t = n + 2 * c.wm; t = t >= NT ? t - NT : t; tile[n] = c.tstart + t; }
    else tile[n] = c.ftile0 + 2 * (n - NT);
    bad[n] = A_BYTES + (8 * g + q) * 512 + (((tile[n] & 7) ^ z) << 5) + (tile[n] >> 3) * 256 + p * 8;
  }
  f32x4 acc[4][NT], accx[NX];
#pragma unroll
  for (int m = 0; m < 4; m++)
#pragma unroll
    for (int n = 0; n < NT; n++) acc[m][n] = f32x4{0, 0, 0, 0};
#pragma unroll
  for (int n = 0; n < NX; n++) accx[n] = f32x4{0, 0, 0, 0};
  v8bf af[2][4], as[2], bf[2][NB];
  const bool ones = (r == 0) || c.isffn;
  auto ldfr = [&](int buf, int slot, int ks) {
    LAS char* base = smem + slot * ST_BYTES;
#pragma unroll
    for (int m = 0; m < 4; m++) af[buf][m] = *(LAS v8bf*)(base + aad[ks] + amain + m * 2048);
    as[buf] = *(LAS v8bf*)(base + aad[ks]);
#pragma unroll
    for (int n = 0; n < NB; n++) bf[buf][n] = trfrag(base + bad[n] + ks * 16384, 2048);
  };
  auto mma = [&](int buf) {
#pragma unroll
    for (int m = 0; m < 4; m++)
#pragma unroll
      for (int n = 0; n < NT; n++) acc[m][n] = mfma(bf[buf][n], af[buf][m], acc[m][n]);
    v4u s = __builtin_bit_cast(v4u, as[buf]);
#pragma unroll
    for (int e = 0; e < 4; e++) s[e] = ones ? 0x3F803F80u : s[e];
    v8bf sv = __builtin_bit_cast(v8bf, s);
#pragma unroll
    for (int n = 0; n < NX; n++) accx[n] = mfma(bf[buf][n < NOWN ? n : NT + n - NOWN], sv, accx[n]);
  };
  waitbar<12>();
  STAMP(1);
  ldfr(0, 0, 0);
  int slot = 0;
  for (int st = 0; st < 10; st++) {
    ldfr(1, slot, 1);
    mma(0);
    __builtin_amdgcn_sched_barrier(0);
    const int nslot = slot == 2 ? 0 : slot + 1;
    if (st < 9) {
      if (st < 8) waitbar<6>(); else waitbar<0>();
      __builtin_amdgcn_sched_barrier(0);
      if (st < 7) p1_issue(c, lds0, st + 3, slot);
      ldfr(0, nslot, 0);
    }
    mma(1);
    __builtin_amdgcn_sched_barrier(0);
    slot = nslot;
  }
  STAMP(2);
  if (r == 0) {
#pragma unroll
    for (int n = 0; n < NX; n++) *(LAS f32x4*)(smem + CS_OFF + (tile[n < NOWN ? n : NT + n - NOWN] * 16 + 4 * g) * 4) = accx[n];
  }
  if (c.w < 3) {
    const int t = c.w * 64 + lane, tt = t < 144 ? t : 143;
    v2u gr;
    for (unsigned spins = 0;; spins++) {
      gr = ld64_sc1_wait(c.rsC, STAT_OFF + c.grp * 2048 + tt * 8);
      if (__all(gr.y == c.tag) || spins > SPIN_CAP) break;
      __builtin_amdgcn_s_sleep(1);
    }
    f32x2 ms; ms.x = bflo(gr.x); ms.y = __builtin_amdgcn_rcpf(bfhi(gr.x));
    *(LAS f32x2*)(smem + RS_OFF + tt * 8) = ms;
  }
  ldsbar();
  STAMP(8);
  f32x2 rs[4];
#pragma unroll
  for (int m = 0; m < 4; m++) rs[m] = *(LAS f32x2*)(smem + RS_OFF + (16 + c.wm * 64 + m * 16 + r) * 8);
  if (c.isffn) {
#pragma unroll
    for (int n = 0; n < NT; n++) {
      const f32x4 cs = *(LAS f32x4*)(smem + CS_OFF + (tile[n] * 16 + 4 * g) * 4);
#pragma unroll
      for (int mp = 0; mp < 2; mp++) {
        v4u o;
#pragma unroll
        for (int h = 0; h < 2; h++) {
          const int m = 2 * mp + h;
          float y[4];
#pragma unroll
          for (int j = 0; j < 4; j++) y[j] = silu_f(rbf((acc[m][n][j] - rs[m].x * cs[j]) * rs[m].y));
          o[2 * h] = pk2(y[0], y[1]); o[2 * h + 1] = pk2(y[2], y[3]);
        }
        st128_sc1(c.rsH, lane * 16, ((c.grp * 4 + c.wm * 2 + mp) * NCT + c.htile0 + tile[n]) * 1024, o);
      }
    }
  } else {
#pragma unroll
    for (int n = 0; n < NT; n++) {
      const f32x4 cs = *(LAS f32x4*)(smem + CS_OFF + (tile[n] * 16 + 4 * g) * 4);
#pragma unroll
      for (int m = 0; m < 4; m++) {
        float y[4];
#pragma unroll
        for (int j = 0; j < 4; j++) y[j] = (acc[m][n][j] - rs[m].x * cs[j]) * rs[m].y;
        v2u o; o.x = pk2(y[0], y[1]); o.y = pk2(y[2], y[3]);
        *(LAS v2u*)(smem + (16 + c.wm * 64 + m * 16 + r) * IMG_LD + tile[n] * 32 + g * 8) = o;
      }
    }
    const f32x2 rh = *(LAS f32x2*)(smem + RS_OFF + r * 8);
#pragma unroll
    for (int n = 0; n < NX; n++) {
      const int t = tile[n < NOWN ? n : NT + n - NOWN];
      const f32x4 cs = *(LAS f32x4*)(smem + CS_OFF + (t * 16 + 4 * g) * 4);
      float y[4];
#pragma unroll
      for (int j = 0; j < 4; j++) y[j] = (accx[n][j] - rh.x * cs[j]) * rh.y;
      v2u o; o.x = pk2(y[0], y[1]); o.y = pk2(y[2], y[3]);
      *(LAS v2u*)(smem + r * IMG_LD + t * 32 + g * 8) = o;
    }
  }
}

template <int S> struct P3W { static constexpr int v = S == 0 ? 8 : S == 1 ? 13 : S <= 6 ? 18 : S == 7 ? 13 : S == 8 ? 4 : 0; };

__global__ __launch_bounds__(512, 2) void fused_kernel(const u16* __restrict__ x, const u16* __restrict__ wqkv, const u16* __restrict__ wo,
                                                       const u16* __restrict__ w1, const u16* __restrict__ w2, u16* __restrict__ out,
                                                       char* __restrict__ ws) {
  extern __shared__ __attribute__((aligned(16))) char smem_[];
  LAS char* smem = (LAS char*)smem_;
  const unsigned lds0 = (unsigned)(size_t)smem;
  const int tid = threadIdx.x, lane = tid & 63, w = __builtin_amdgcn_readfirstlane(tid >> 6);
  const int bid = blockIdx.x, xcd = bid & 7, idx = bid >> 3, half = xcd & 1, j8 = idx & 7;
  const int grp = (xcd >> 1) * 4 + (idx >> 3), mem = half * 8 + j8;
  const int isffn = j8 >> 2, sub = half * 4 + (j8 & 3);
  STAMP(0);
  P1 c;
  c.rsA = mkrs(x, SEQ * DM * 2);
  c.rsB = isffn ? mkrs(w1, DM * FF * 2) : mkrs(wqkv, DM * 3 * DM * 2);
  c.rsH = mkrs(ws, H_BYTES);
  c.rsC = mkrs(ws + CTRL_OFF, 1 << 20);
  c.ldb2 = isffn ? FF * 2 : 3 * DM * 2;
  c.w = w; c.lane = lane; c.grp = grp; c.isffn = isffn;
  c.wm = w >> 2;
  const int wn = w < 4 ? w : 7 - w;
  const int ftile0 = sub < 4 ? sub * 14 : 56 + (sub - 4) * 13;
  const int ntile = isffn ? (sub < 4 ? 14 : 13) : 15;
  c.htile0 = 40 + ftile0;
  v2u ep = ld64_sc1(c.rsC, EPOCH_OFF + grp * 256, 0);
  v4u xs[5];
  const int sslot = w * 4 + (lane >> 4);
  const bool dostat = isffn && w < 5;
  if (dostat) {
    int rho = sub * 18 + (sslot < 18 ? sslot : 17);
    int row = grp * 128 - 16 + rho; row = row < 0 ? 0 : row;
    const int vo = row * (DM * 2) + (lane & 15) * 16;
    xs[0] = ld128<0>(c.rsA, vo, 0); xs[1] = ld128<256>(c.rsA, vo, 0); xs[2] = ld128<512>(c.rsA, vo, 0);
    xs[3] = ld128<768>(c.rsA, vo, 0); xs[4] = ld128<1024>(c.rsA, vo, 0);
  }
  {
    const int ar = lane >> 3, sw = ((lane & 7) ^ ((4 * w + (lane >> 4)) & 7)) << 4;
    c.avoff = (grp * 128 + 8 * w + ar) * (DM * 2) + sw;
    int hr = grp * 128 - 16 + 8 * w + ar; hr = hr < 0 ? 0 : hr;
    c.asvoff = hr * (DM * 2) + sw;
    const int kk = 2 * w + (lane >> 5), zz = (kk & 3) | (((kk >> 3) & 1) << 2), pc = lane & 31, seg = (pc >> 1) ^ zz;
    int col;
    if (isffn) col = (ftile0 + seg) * 16;
    else { const int part = seg >= 10 ? 2 : (seg >= 5 ? 1 : 0); col = part * DM + sub * 80 + (seg - 5 * part) * 16; }
    c.bvoff = seg < ntile ? kk * c.ldb2 + col * 2 + (pc & 1) * 16 : OOB;
  }
  p1_issue(c, lds0, 0, 0); p1_issue(c, lds0, 1, 1); p1_issue(c, lds0, 2, 2);
  asm volatile("s_waitcnt vmcnt(18)" : "+v"(ep), "+v"(xs[0]), "+v"(xs[1]), "+v"(xs[2]), "+v"(xs[3]), "+v"(xs[4])::"memory");
  u64 tag64;
  {
    const u64 e = ((u64)(unsigned)__builtin_amdgcn_readfirstlane(ep.y) << 32) | (unsigned)__builtin_amdgcn_readfirstlane(ep.x);
    tag64 = e * 0x5851F42D4C957F2Dull + 0x14057B7EF767814Full;
  }
  c.tag = (unsigned)tag64;
  if (dostat) {
    float s = 0;
#pragma unroll
    for (int i = 0; i < 5; i++)
#pragma unroll
      for (int e = 0; e < 4; e++) s += bflo(xs[i][e]) + bfhi(xs[i][e]);
    const float mean = row16_sum(s) * (1.0f / DM);
    float v = 0;
#pragma unroll
    for (int i = 0; i < 5; i++)
#pragma unroll
      for (int e = 0; e < 4; e++) { const float a = bflo(xs[i][e]) - mean, b = bfhi(xs[i][e]) - mean; v += a * a + b * b; }
    const float var = row16_sum(v) * (1.0f / DM);
    const float sd = __builtin_sqrtf(rbf(rbf(var) + 1e-5f));
    v2u gr; gr.x = pk2(mean, sd); gr.y = c.tag;
    if ((lane & 15) == 0 && sslot < 18) st64_sc1(c.rsC, STAT_OFF + grp * 2048 + (sub * 18 + sslot) * 8, gr);
  }
  {
    const int n4 = ntile - 12;
    c.tstart = wn <= n4 ? 4 * wn : 4 * n4 + 3 * (wn - n4);
    if (!isffn) c.ftile0 = 1 + 6 * c.wm;
    else { const int fi = (wn - n4) * 2 + c.wm; c.ftile0 = 1 + 2 * (n4 == 2 ? fi : (fi & 1)); }
    if (wn < n4) phase1<4, 1, 0>(c, smem, lds0, ws);
    else if (!isffn) phase1<3, 2, 3>(c, smem, lds0, ws);
    else phase1<3, 2, 1>(c, smem, lds0, ws);
  }
  STAMP(9);
  if (!isffn) {
    ldsbar();
    STAMP(10);
    const int hh = w >> 1, row = (w & 1) * 64 + lane;
    LAS char* qp = smem + (row + 16) * IMG_LD + hh * 40;
    unsigned qv[10];
#pragma unroll
    for (int i = 0; i < 5; i++) { const v2u t = *(LAS v2u*)(qp + i * 8); qv[2 * i] = t.x; qv[2 * i + 1] = t.y; }
    const int pos = grp * 128 + row;
    float lg[8];
#pragma unroll
    for (int k = 0; k < 8; k++) {
      LAS char* kp = qp + 160 - 2 * k * IMG_LD;
      float a0 = 0, a1 = 0;
#pragma unroll
      for (int i = 0; i < 5; i++) { const v2u t = *(LAS v2u*)(kp + i * 8); a0 = dot2(qv[2 * i], t.x, a0); a1 = dot2(qv[2 * i + 1], t.y, a1); }
      const float l = rbf(rbf(a0 + a1) * 0.22360679774997896f);
      lg[k] = pos - 2 * k >= 0 ? l : -1e9f;
    }
    float mx = lg[0];
#pragma unroll
    for (int k = 1; k < 8; k++) mx = fmaxf(mx, lg[k]);
    float sum = 0;
#pragma unroll
    for (int k = 0; k < 8; k++) { lg[k] = __builtin_amdgcn_exp2f((lg[k] - mx) * 1.4426950408889634f); sum += lg[k]; }
    const float inv = __builtin_amdgcn_rcpf(sum);
    float o[20];
#pragma unroll
    for (int d = 0; d < 20; d++) o[d] = 0;
#pragma unroll
    for (int k = 0; k < 8; k++) {
      const unsigned plo = pk2(lg[k] * inv, 0.f), phi = plo << 16;
      LAS char* vp = qp + 320 - 2 * k * IMG_LD;
#pragma unroll
      for (int i = 0; i < 5; i++) {
        const v2u t = *(LAS v2u*)(vp + i * 8);
        o[4 * i] = dot2(t.x, plo, o[4 * i]); o[4 * i + 1] = dot2(t.x, phi, o[4 * i + 1]);
        o[4 * i + 2] = dot2(t.y, plo, o[4 * i + 2]); o[4 * i + 3] = dot2(t.y, phi, o[4 * i + 3]);
      }
    }
#pragma unroll
    for (int i = 0; i < 5; i++) { v2u t; t.x = pk2(o[4 * i], o[4 * i + 1]); t.y = pk2(o[4 * i + 2], o[4 * i + 3]); *(LAS v2u*)(qp + i * 8) = t; }
    STAMP(11);
    ldsbar();
    const int r = lane & 15, g = lane >> 4;
#pragma unroll
    for (int i = 0; i < 3; i++) {
      const int b = w + 8 * i;
      if (b < 20) {
        const int rp = b / 5, t = b - 5 * rp;
        LAS char* sp = smem + (16 + rp * 32 + r) * IMG_LD + t * 32 + g * 8;
        const v2u lo = *(LAS v2u*)sp, hi = *(LAS v2u*)(sp + 16 * IMG_LD);
        v4u v; v.x = lo.x; v.y = lo.y; v.z = hi.x; v.w = hi.y;
        st128_sc1(c.rsH, lane * 16, ((grp * 4 + rp) * NCT + sub * 5 + t) * 1024, v);
      }
    }
  }
  STAMP(3);
  waitbar<0>();
  if (tid == 0) { v2u t; t.x = (unsigned)tag64; t.y = (unsigned)(tag64 >> 32); st64_sc1(c.rsC, FLAG_OFF + grp * 128 + mem * 8, t); }
  STAMP(4);
  const int rh = j8 >> 2, c3 = half * 4 + (j8 & 3);
  const int r = lane & 15, g = lane >> 4;
  const v4i rsW = w < 2 ? mkrs(wo, DM * DM * 2) : mkrs(w2, FF * DM * 2);
  const int krow0 = w < 2 ? 320 * w : 288 * (w - 2);
  const int u0 = w < 2 ? 10 * w : 20 + 9 * (w - 2);
  const bool ten = w < 2;
  v2u xr[3];
#pragma unroll
  for (int i = 0; i < 3; i++) {
    const int tl = w + 8 * i, tt = tl < 20 ? tl : 19, m = tt / 5, n = tt - 5 * m;
    xr[i] = ld64(c.rsA, (grp * 128 + rh * 64 + m * 16 + r) * (DM * 2) + (c3 * 80 + n * 16 + g * 4) * 2, 0);
  }
  int dvo[5];
#pragma unroll
  for (int i = 0; i < 5; i++) { const int pc = 64 * i + lane, rr = pc / 10, cc = pc - 10 * rr; dvo[i] = rr * (DM * 2) + c3 * 160 + cc * 16; }
  const unsigned ring = lds0 + w * 20480;
  auto dmaB = [&](int s) {
    const int so = (s == 9 && !ten) ? OOB : (krow0 + 32 * s) * (DM * 2);
#pragma unroll
    for (int i = 0; i < 5; i++) dma16(rsW, ring + (s & 3) * 5120 + i * 1024, dvo[i], so);
  };
  dmaB(0); dmaB(1); dmaB(2); dmaB(3);
  {
    unsigned need;
    if (w < 2) need = w == 0 ? 0x000Fu : 0x0F00u;
    else {
      const int t0 = 18 * (w - 2), t1 = t0 + 17;
      const int f0 = t0 < 56 ? t0 / 14 : 4 + (t0 - 56) / 13, f1 = t1 < 56 ? t1 / 14 : 4 + (t1 - 56) / 13;
      need = 0;
      for (int f = f0; f <= f1; f++) need |= 1u << ((f >> 2) * 8 + 4 + (f & 3));
    }
    const bool mine = (need >> (lane & 15)) & 1;
    for (unsigned spins = 0;; spins++) {
      const v2u f = ld64_sc1_wait(c.rsC, FLAG_OFF + grp * 128 + (lane & 15) * 8);
      const bool ok = !mine || (f.x == (unsigned)tag64 && f.y == (unsigned)(tag64 >> 32));
      if (__all(ok) || spins > SPIN_CAP) break;
      __builtin_amdgcn_s_sleep(1);
    }
  }
  STAMP(5);
  v4u A[4][4];
  const int hso = ((grp * 4 + rh * 2) * NCT + 2 * u0) * 1024;
  auto ldA = [&](int s) {
    const int so = (s == 9 && !ten) ? OOB : hso + s * 2048;
    const int so2 = (s == 9 && !ten) ? OOB : hso + s * 2048 + NCT * 1024;
    A[s & 3][0] = ld128_sc1<0>(c.rsH, lane * 16, so); A[s & 3][1] = ld128_sc1<1024>(c.rsH, lane * 16, so);
    A[s & 3][2] = ld128_sc1<0>(c.rsH, lane * 16, so2); A[s & 3][3] = ld128_sc1<1024>(c.rsH, lane * 16, so2);
  };
  ldA(0); ldA(1); ldA(2);
  v8bf bb[2][5];
  LAS char* bbase = smem + w * 20480 + (4 * g + (r >> 2)) * 160 + (lane & 3) * 8;
  auto rdB = [&](int s) {
#pragma unroll
    for (int n = 0; n < 5; n++) bb[s & 1][n] = trfrag(bbase + (s & 3) * 5120 + n * 32, 16 * 160);
  };
  rdB(0);
  f32x4 acc[4][5];
#pragma unroll
  for (int m = 0; m < 4; m++)
#pragma unroll
    for (int n = 0; n < 5; n++) acc[m][n] = f32x4{0, 0, 0, 0};
  auto step = [&](auto S) {
    constexpr int s = decltype(S)::value;
    asm volatile("s_waitcnt vmcnt(%4) lgkmcnt(0)" : "+v"(A[s & 3][0]), "+v"(A[s & 3][1]), "+v"(A[s & 3][2]), "+v"(A[s & 3][3]) : "i"(P3W<s>::v) : "memory");
    if constexpr (s + 4 < 10) dmaB(s + 4);
    if constexpr (s + 3 < 10) ldA(s + 3);
    if constexpr (s + 1 < 10) rdB(s + 1);
    if (s == 9 && !ten) return;
    v8bf a[4];
#pragma unroll
    for (int h = 0; h < 2; h++) {
      const v4u l0 = A[s & 3][2 * h], l1 = A[s & 3][2 * h + 1];
      a[2 * h] = __builtin_bit_cast(v8bf, __builtin_shufflevector(l0, l1, 0, 1, 4, 5));
      a[2 * h + 1] = __builtin_bit_cast(v8bf, __builtin_shufflevector(l0, l1, 2, 3, 6, 7));
    }
#pragma unroll
    for (int m = 0; m < 4; m++)
#pragma unroll
      for (int n = 0; n < 5; n++) acc[m][n] = mfma(bb[s & 1][n], a[m], acc[m][n]);
    __builtin_amdgcn_sched_barrier(0);
  };
  step(std::integral_constant<int, 0>{}); step(std::integral_constant<int, 1>{}); step(std::integral_constant<int, 2>{});
  step(std::integral_constant<int, 3>{}); step(std::integral_constant<int, 4>{}); step(std::integral_constant<int, 5>{});
  step(std::integral_constant<int, 6>{}); step(std::integral_constant<int, 7>{}); step(std::integral_constant<int, 8>{});
  step(std::integral_constant<int, 9>{});
  STAMP(6);
  asm volatile("s_waitcnt vmcnt(0) lgkmcnt(0)" : "+v"(xr[0]), "+v"(xr[1]), "+v"(xr[2])::"memory");
#pragma unroll
  for (int m = 0; m < 4; m++)
#pragma unroll
    for (int n = 0; n < 5; n++) *(LAS f32x4*)(smem + w * 20480 + (m * 5 + n) * 1024 + lane * 16) = acc[m][n];
  STAMP(12);
  ldsbar();
  STAMP(13);
  if (tid == 0 && mem == 0) { v2u t; t.x = (unsigned)tag64; t.y = (unsigned)(tag64 >> 32); st64_sc1(c.rsC, EPOCH_OFF + grp * 256, t); }
#pragma unroll
  for (int i = 0; i < 3; i++) {
    const int tl = w + 8 * i;
    if (tl < 20) {
      const int m = tl / 5, n = tl - 5 * m;
      LAS char* pp = smem + tl * 1024 + lane * 16;
      const f32x4 s = *(LAS f32x4*)pp + *(LAS f32x4*)(pp + 20480);
      f32x4 f = *(LAS f32x4*)(pp + 2 * 20480);
#pragma unroll
      for (int k = 3; k < 8; k++) f += *(LAS f32x4*)(pp + k * 20480);
      float y[4];
#pragma unroll
      for (int j = 0; j < 4; j++) {
        const float xv = (j & 1) ? bfhi(xr[i][j >> 1]) : bflo(xr[i][j >> 1]);
        y[j] = xv + rbf(rbf(s[j]) * rbf(silu_f(rbf(f[j]))));
      }
      v2u o; o.x = pk2(y[0], y[1]); o.y = pk2(y[2], y[3]);
      *(GAS v2u*)((GAS char*)out + (size_t)(grp * 128 + rh * 64 + m * 16 + r) * (DM * 2) + (c3 * 80 + n * 16 + g * 4) * 2) = o;
    }
  }
  STAMP(7);
}
}

extern "C" void kernel_launch(void* const* d_in, const int* in_sizes, int n_in, void* d_out, int out_size, void* d_ws, size_t ws_size,
                              hipStream_t stream) {
  (void)hipFuncSetAttribute((const void*)fused_kernel, hipFuncAttributeMaxDynamicSharedMemorySize, LDS_BYTES);
  fused_kernel<<<256, 512, LDS_BYTES, stream>>>((const u16*)d_in[0], (const u16*)d_in[1], (const u16*)d_in[2], (const u16*)d_in[4],
                                                (const u16*)d_in[5], (u16*)d_out, (char*)d_ws);
}
```

```cpp
#include <hip/hip_runtime.h>
#include <stdint.h>
#include <type_traits>

#define GAS __attribute__((address_space(1)))
#define LAS __attribute__((address_space(3)))
typedef unsigned short u16;
typedef unsigned long long u64;
typedef __bf16 v2bf __attribute__((ext_vector_type(2)));
typedef __bf16 v4bf __attribute__((ext_vector_type(4)));
typedef __bf16 v8bf __attribute__((ext_vector_type(8)));
typedef float f32x2 __attribute__((ext_vector_type(2)));
typedef float f32x4 __attribute__((ext_vector_type(4)));
typedef unsigned v4u __attribute__((ext_vector_type(4)));
typedef unsigned v2u __attribute__((ext_vector_type(2)));
typedef int v4i __attribute__((ext_vector_type(4)));

namespace {
constexpr int SEQ = 2048, DM = 640, FF = 1728;
constexpr int NCT = 148;
constexpr int H_BYTES = 64 * NCT * 1024;
constexpr size_t CTRL_OFF = 16u << 20;
constexpr int EPOCH_OFF = 0, FLAG_OFF = 8192, STAT_OFF = 65536;
constexpr int A_BYTES = 144 * 128, B_BYTES = 64 * 512, ST_BYTES = A_BYTES + B_BYTES;
constexpr int CS_OFF = 3 * ST_BYTES, RS_OFF = CS_OFF + 1024;
constexpr int IMG_LD = 488;
constexpr int LDS_BYTES = 163840;
constexpr int OOB = 0x7FF00000;
constexpr unsigned SPIN_CAP = 1u << 18;
#define STAMP(i)

__device__ __forceinline__ v4i mkrs(const void* p, int bytes) {
  u64 a = (u64)p; v4i r; r.x = (int)(unsigned)a; r.y = (int)((unsigned)(a >> 32) & 0xFFFFu); r.z = bytes; r.w = 0x00020000; return r;
}
__device__ __forceinline__ void dma16(v4i rs, unsigned lds, int voff, int soff) {
  asm volatile("s_mov_b32 m0, %0\n\ts_nop 0\n\tbuffer_load_dwordx4 %1, %2, %3 offen lds" ::"s"(lds), "v"(voff), "s"(rs), "s"(soff));
}
#define SC1A " sc1"
template <int IMM> __device__ __forceinline__ v4u ld128_sc1(v4i rs, int voff, int soff) {
  v4u r; asm volatile("buffer_load_dwordx4 %0, %1, %2, %3 offen offset:%4" SC1A : "=v"(r) : "v"(voff), "s"(rs), "s"(soff), "i"(IMM)); return r;
}
template <int IMM> __device__ __forceinline__ v4u ld128(v4i rs, int voff, int soff) {
  v4u r; asm volatile("buffer_load_dwordx4 %0, %1, %2, %3 offen offset:%4" : "=v"(r) : "v"(voff), "s"(rs), "s"(soff), "i"(IMM)); return r;
}
__device__ __forceinline__ v2u ld64(v4i rs, int voff, int soff) {
  v2u r; asm volatile("buffer_load_dwordx2 %0, %1, %2, %3 offen" : "=v"(r) : "v"(voff), "s"(rs), "s"(soff)); return r;
}
__device__ __forceinline__ v2u ld64_sc1(v4i rs, int voff, int soff) {
  v2u r; asm volatile("buffer_load_dwordx2 %0, %1, %2, %3 offen sc1" : "=v"(r) : "v"(voff), "s"(rs), "s"(soff)); return r;
}
__device__ __forceinline__ v2u ld64_sc1_wait(v4i rs, int voff) {
  v2u r; asm volatile("buffer_load_dwordx2 %0, %1, %2, 0 offen sc1\n\ts_waitcnt vmcnt(0)" : "=&v"(r) : "v"(voff), "s"(rs) : "memory"); return r;
}
__device__ __forceinline__ void st128_sc1(v4i rs, int voff, int soff, v4u v) {
  asm volatile("buffer_store_dwordx4 %0, %1, %2, %3 offen sc1\n\ts_nop 1" ::"v"(v), "v"(voff), "s"(rs), "s"(soff) : "memory");
}
__device__ __forceinline__ void st64_sc1(v4i rs, int voff, v2u v) {
  asm volatile("buffer_store_dwordx2 %0, %1, %2, 0 offen sc1" ::"v"(v), "v"(voff), "s"(rs) : "memory");
}
template <int N> __device__ __forceinline__ void waitbar() { asm volatile("s_waitcnt vmcnt(%0) lgkmcnt(0)\n\ts_barrier" ::"i"(N) : "memory"); }
__device__ __forceinline__ void ldsbar() { asm volatile("s_waitcnt lgkmcnt(0)\n\ts_barrier" ::: "memory"); }
__device__ __forceinline__ float bflo(unsigned u) { return __builtin_bit_cast(float, u << 16); }
__device__ __forceinline__ float bfhi(unsigned u) { return __builtin_bit_cast(float, u & 0xFFFF0000u); }
__device__ __forceinline__ unsigned pk2(float a, float b) {
  v2bf v; v.x = (__bf16)a; v.y = (__bf16)b; return __builtin_bit_cast(unsigned, v);
}
__device__ __forceinline__ float rbf(float a) { return (float)(__bf16)a; }
__device__ __forceinline__ float dot2(unsigned a, unsigned b, float c) {
  return __builtin_amdgcn_fdot2_f32_bf16(__builtin_bit_cast(v2bf, a), __builtin_bit_cast(v2bf, b), c, false);
}
__device__ __forceinline__ f32x4 mfma(v8bf a, v8bf b, f32x4 c) { return __builtin_amdgcn_mfma_f32_16x16x32_bf16(a, b, c, 0, 0, 0); }
__device__ __forceinline__ v8bf trfrag(LAS char* p, int second) {
  v4bf lo = __builtin_amdgcn_ds_read_tr16_b64_v4bf16((LAS v4bf*)p);
  v4bf hi = __builtin_amdgcn_ds_read_tr16_b64_v4bf16((LAS v4bf*)(p + second));
  return __builtin_shufflevector(lo, hi, 0, 1, 2, 3, 4, 5, 6, 7);
}
__device__ __forceinline__ float row16_sum(float v) {
  v += __builtin_bit_cast(float, __builtin_amdgcn_update_dpp(0, __builtin_bit_cast(int, v), 0x128, 0xF, 0xF, false));
  v += __builtin_bit_cast(float, __builtin_amdgcn_update_dpp(0, __builtin_bit_cast(int, v), 0x124, 0xF, 0xF, false));
  v += __builtin_bit_cast(float, __builtin_amdgcn_update_dpp(0, __builtin_bit_cast(int, v), 0x122, 0xF, 0xF, false));
  v += __builtin_bit_cast(float, __builtin_amdgcn_update_dpp(0, __builtin_bit_cast(int, v), 0x121, 0xF, 0xF, false));
  return v;
}
__device__ __forceinline__ float silu_f(float y) { return y * __builtin_amdgcn_rcpf(1.0f + __builtin_amdgcn_exp2f(y * -1.4426950408889634f)); }

struct P1 {
  v4i rsA, rsB, rsH, rsC;
  int avoff, asvoff, bvoff, ldb2;
  int w, wm, lane, grp;
  int tstart, ftile0;
  int isffn, htile0;
  unsigned tag;
};

__device__ __forceinline__ void p1_issue(const P1& c, unsigned lds0, int st, int slot) {
  const unsigned base = lds0 + slot * ST_BYTES;
  if (!c.isffn && c.w < 2) dma16(c.rsA, base + c.w * 1024, c.asvoff, st * 128);
#pragma unroll
  for (int i = 0; i < 2; i++) dma16(c.rsA, base + (2 + c.w + 8 * i) * 1024, c.avoff, st * 128 + i * 64 * (DM * 2));
#pragma unroll
  for (int i = 0; i < 4; i++) dma16(c.rsB, base + A_BYTES + (c.w + 8 * i) * 1024, c.bvoff, (st * 64 + i * 16) * c.ldb2);
}

template <int NT, int NOWN, int NFOR>
__device__ __forceinline__ void phase1(const P1& c, LAS char* smem, unsigned lds0, char* ws) {
  constexpr int NB = NT + NFOR, NX = NOWN + NFOR;
  const int lane = c.lane, r = lane & 15, g = lane >> 4, q = r >> 2, p = lane & 3;
  int aad[2];
#pragma unroll
  for (int ks = 0; ks < 2; ks++) aad[ks] = r * 128 + (((4 * ks + g) ^ (r >> 1)) << 4);
  const int amain = (1 + c.wm * 4) * 2048;
  const int z = q | ((g & 1) << 2);
  int tile[NB], bad[NB];
#pragma unroll
  for (int n = 0; n < NB; n++) {
    if (n < NT) { int t = n + 2 * c.wm; t = t >= NT ? t - NT : t; tile[n] = c.tstart + t; }
    else tile[n] = c.ftile0 + 2 * (n - NT);
    bad[n] = A_BYTES + (8 * g + q) * 512 + (((tile[n] & 7) ^ z) << 5) + (tile[n] >> 3) * 256 + p * 8;
  }
  f32x4 acc[4][NT], accx[NX];
#pragma unroll
  for (int m = 0; m < 4; m++)
#pragma unroll
    for (int n = 0; n < NT; n++) acc[m][n] = f32x4{0, 0, 0, 0};
#pragma unroll
  for (int n = 0; n < NX; n++) accx[n] = f32x4{0, 0, 0, 0};
  v8bf af[2][4], as[2], bf[2][NB];
  const bool ones = (r == 0) || c.isffn;
  auto ldfr = [&](int buf, int slot, int ks) {
    LAS char* base = smem + slot * ST_BYTES;
#pragma unroll
    for (int m = 0; m < 4; m++) af[buf][m] = *(LAS v8bf*)(base + aad[ks] + amain + m * 2048);
    as[buf] = *(LAS v8bf*)(base + aad[ks]);
#pragma unroll
    for (int n = 0; n < NB; n++) bf[buf][n] = trfrag(base + bad[n] + ks * 16384, 2048);
  };
  auto mma = [&](int buf) {
#pragma unroll
    for (int m = 0; m < 4; m++)
#pragma unroll
      for (int n = 0; n < NT; n++) acc[m][n] = mfma(bf[buf][n], af[buf][m], acc[m][n]);
    v4u s = __builtin_bit_cast(v4u, as[buf]);
#pragma unroll
    for (int e = 0; e < 4; e++) s[e] = ones ? 0x3F803F80u : s[e];
    v8bf sv = __builtin_bit_cast(v8bf, s);
#pragma unroll
    for (int n = 0; n < NX; n++) accx[n] = mfma(bf[buf][n < NOWN ? n : NT + n - NOWN], sv, accx[n]);
  };
  waitbar<13>();
  STAMP(1);
  ldfr(0, 0, 0);
  int slot = 0;
  for (int st = 0; st < 10; st++) {
    ldfr(1, slot, 1);
    mma(0);
    __builtin_amdgcn_sched_barrier(0);
    const int nslot = slot == 2 ? 0 : slot + 1;
    if (st < 9) {
      if (st < 2) waitbar<7>(); else if (st < 8) waitbar<6>(); else waitbar<0>();
      __builtin_amdgcn_sched_barrier(0);
      if (st < 7) p1_issue(c, lds0, st + 3, slot);
      ldfr(0, nslot, 0);
    }
    mma(1);
    __builtin_amdgcn_sched_barrier(0);
    slot = nslot;
  }
  STAMP(2);
  if (r == 0) {
#pragma unroll
    for (int n = 0; n < NX; n++) *(LAS f32x4*)(smem + CS_OFF + (tile[n < NOWN ? n : NT + n - NOWN] * 16 + 4 * g) * 4) = accx[n];
  }
  if (c.w < 3) {
    const int t = c.w * 64 + lane, tt = t < 144 ? t : 143;
    v2u gr;
    for (unsigned spins = 0;; spins++) {
      gr = ld64_sc1_wait(c.rsC, STAT_OFF + c.grp * 2048 + tt * 8);
      if (__all(gr.y == c.tag) || spins > SPIN_CAP) break;
      __builtin_amdgcn_s_sleep(1);
    }
    f32x2 ms; ms.x = bflo(gr.x); ms.y = __builtin_amdgcn_rcpf(bfhi(gr.x));
    *(LAS f32x2*)(smem + RS_OFF + tt * 8) = ms;
  }
  ldsbar();
  STAMP(8);
  f32x2 rs[4];
#pragma unroll
  for (int m = 0; m < 4; m++) rs[m] = *(LAS f32x2*)(smem + RS_OFF + (16 + c.wm * 64 + m * 16 + r) * 8);
  if (c.isffn) {
#pragma unroll
    for (int n = 0; n < NT; n++) {
      const f32x4 cs = *(LAS f32x4*)(smem + CS_OFF + (tile[n] * 16 + 4 * g) * 4);
#pragma unroll
      for (int mp = 0; mp < 2; mp++) {
        v4u o;
#pragma unroll
        for (int h = 0; h < 2; h++) {
          const int m = 2 * mp + h;
          float y[4];
#pragma unroll
          for (int j = 0; j < 4; j++) y[j] = silu_f(rbf((acc[m][n][j] - rs[m].x * cs[j]) * rs[m].y));
          o[2 * h] = pk2(y[0], y[1]); o[2 * h + 1] = pk2(y[2], y[3]);
        }
        st128_sc1(c.rsH, lane * 16, ((c.grp * 4 + c.wm * 2 + mp) * NCT + c.htile0 + tile[n]) * 1024, o);
      }
    }
  } else {
#pragma unroll
    for (int n = 0; n < NT; n++) {
      const f32x4 cs = *(LAS f32x4*)(smem + CS_OFF + (tile[n] * 16 + 4 * g) * 4);
#pragma unroll
      for (int m = 0; m < 4; m++) {
        float y[4];
#pragma unroll
        for (int j = 0; j < 4; j++) y[j] = (acc[m][n][j] - rs[m].x * cs[j]) * rs[m].y;
        v2u o; o.x = pk2(y[0], y[1]); o.y = pk2(y[2], y[3]);
        *(LAS v2u*)(smem + (16 + c.wm * 64 + m * 16 + r) * IMG_LD + tile[n] * 32 + g * 8) = o;
      }
    }
    const f32x2 rh = *(LAS f32x2*)(smem + RS_OFF + r * 8);
#pragma unroll
    for (int n = 0; n < NX; n++) {
      const int t = tile[n < NOWN ? n : NT + n - NOWN];
      const f32x4 cs = *(LAS f32x4*)(smem + CS_OFF + (t * 16 + 4 * g) * 4);
      float y[4];
#pragma unroll
      for (int j = 0; j < 4; j++) y[j] = (accx[n][j] - rh.x * cs[j]) * rh.y;
      v2u o; o.x = pk2(y[0], y[1]); o.y = pk2(y[2], y[3]);
      *(LAS v2u*)(smem + r * IMG_LD + t * 32 + g * 8) = o;
    }
  }
}

template <int S> struct P3W { static constexpr int v = S == 0 ? 8 : S == 1 ? 13 : S <= 6 ? 18 : S == 7 ? 13 : S == 8 ? 4 : 0; };

__global__ __launch_bounds__(512, 2) void fused_kernel(const u16* __restrict__ x, const u16* __restrict__ wqkv, const u16* __restrict__ wo,
                                                       const u16* __restrict__ w1, const u16* __restrict__ w2, u16* __restrict__ out,
                                                       char* __restrict__ ws) {
  extern __shared__ __attribute__((aligned(16))) char smem_[];
  LAS char* smem = (LAS char*)smem_;
  const unsigned lds0 = (unsigned)(size_t)smem;
  const int tid = threadIdx.x, lane = tid & 63, w = __builtin_amdgcn_readfirstlane(tid >> 6);
  const int bid = blockIdx.x, xcd = bid & 7, idx = bid >> 3, half = xcd & 1, j8 = idx & 7;
  const int grp = (xcd >> 1) * 4 + (idx >> 3), mem = half * 8 + j8;
  const int isffn = j8 >> 2, sub = half * 4 + (j8 & 3);
  STAMP(0);
  u64 pc0; unsigned codelen;
  asm volatile("s_getpc_b64 %0\n.Lcode_begin_mark:\n\ts_mov_b32 %1, .Lcode_end_mark-.Lcode_begin_mark" : "=s"(pc0), "=s"(codelen));
  P1 c;
  c.rsA = mkrs(x, SEQ * DM * 2);
  c.rsB = isffn ? mkrs(w1, DM * FF * 2) : mkrs(wqkv, DM * 3 * DM * 2);
  c.rsH = mkrs(ws, H_BYTES);
  c.rsC = mkrs(ws + CTRL_OFF, 1 << 20);
  c.ldb2 = isffn ? FF * 2 : 3 * DM * 2;
  c.w = w; c.lane = lane; c.grp = grp; c.isffn = isffn;
  c.wm = w >> 2;
  const int wn = w < 4 ? w : 7 - w;
  const int ftile0 = sub < 4 ? sub * 14 : 56 + (sub - 4) * 13;
  const int ntile = isffn ? (sub < 4 ? 14 : 13) : 15;
  c.htile0 = 40 + ftile0;
  v2u ep = ld64_sc1(c.rsC, EPOCH_OFF + grp * 256, 0);
  v4u xs[5];
  const int sslot = w * 4 + (lane >> 4);
  const bool dostat = isffn && w < 5;
  if (dostat) {
    int rho = sub * 18 + (sslot < 18 ? sslot : 17);
    int row = grp * 128 - 16 + rho; row = row < 0 ? 0 : row;
    const int vo = row * (DM * 2) + (lane & 15) * 16;
    xs[0] = ld128<0>(c.rsA, vo, 0); xs[1] = ld128<256>(c.rsA, vo, 0); xs[2] = ld128<512>(c.rsA, vo, 0);
    xs[3] = ld128<768>(c.rsA, vo, 0); xs[4] = ld128<1024>(c.rsA, vo, 0);
  }
  {
    const int ar = lane >> 3, sw = ((lane & 7) ^ ((4 * w + (lane >> 4)) & 7)) << 4;
    c.avoff = (grp * 128 + 8 * w + ar) * (DM * 2) + sw;
    int hr = grp * 128 - 16 + 8 * w + ar; hr = hr < 0 ? 0 : hr;
    c.asvoff = hr * (DM * 2) + sw;
    const int kk = 2 * w + (lane >> 5), zz = (kk & 3) | (((kk >> 3) & 1) << 2), pc = lane & 31, seg = (pc >> 1) ^ zz;
    int col;
    if (isffn) col = (ftile0 + seg) * 16;
    else { const int part = seg >= 10 ? 2 : (seg >= 5 ? 1 : 0); col = part * DM + sub * 80 + (seg - 5 * part) * 16; }
    c.bvoff = seg < ntile ? kk * c.ldb2 + col * 2 + (pc & 1) * 16 : OOB;
  }
  p1_issue(c, lds0, 0, 0); p1_issue(c, lds0, 1, 1); p1_issue(c, lds0, 2, 2);
  unsigned pfd;
  {
    v4i rp; int first;
    const int gw = (bid - 8) * 8 + w;
    if (bid < 8) { rp = mkrs((const void*)pc0, (int)codelen); first = w; }
    else if (gw < 300) { rp = mkrs(wqkv, DM * 3 * DM * 2); first = gw; }
    else if (gw < 570) { rp = mkrs(w1, DM * FF * 2); first = gw - 300; }
    else if (gw < 890) { rp = c.rsA; first = gw - 570; }
    else if (gw < 990) { rp = mkrs(wo, DM * DM * 2); first = gw - 890; }
    else if (gw < 1260) { rp = mkrs(w2, DM * FF * 2); first = gw - 990; }
    else { rp = mkrs(out, SEQ * DM * 2); first = gw - 1260; }
    asm volatile("buffer_load_dword %0, %1, %2, %3 offen" : "=v"(pfd) : "v"(lane * 128), "s"(rp), "s"(first * 8192));
  }
  asm volatile("s_waitcnt vmcnt(19)" : "+v"(ep), "+v"(xs[0]), "+v"(xs[1]), "+v"(xs[2]), "+v"(xs[3]), "+v"(xs[4])::"memory");
  u64 tag64;
  {
    const u64 e = ((u64)(unsigned)__builtin_amdgcn_readfirstlane(ep.y) << 32) | (unsigned)__builtin_amdgcn_readfirstlane(ep.x);
    tag64 = e * 0x5851F42D4C957F2Dull + 0x14057B7EF767814Full;
  }
  c.tag = (unsigned)tag64;
  if (dostat) {
    float s = 0;
#pragma unroll
    for (int i = 0; i < 5; i++)
#pragma unroll
      for (int e = 0; e < 4; e++) s += bflo(xs[i][e]) + bfhi(xs[i][e]);
    const float mean = row16_sum(s) * (1.0f / DM);
    float v = 0;
#pragma unroll
    for (int i = 0; i < 5; i++)
#pragma unroll
      for (int e = 0; e < 4; e++) { const float a = bflo(xs[i][e]) - mean, b = bfhi(xs[i][e]) - mean; v += a * a + b * b; }
    const float var = row16_sum(v) * (1.0f / DM);
    const float sd = __builtin_sqrtf(rbf(rbf(var) + 1e-5f));
    v2u gr; gr.x = pk2(mean, sd); gr.y = c.tag;
    if ((lane & 15) == 0 && sslot < 18) st64_sc1(c.rsC, STAT_OFF + grp * 2048 + (sub * 18 + sslot) * 8, gr);
  }
  {
    const int n4 = ntile - 12;
    c.tstart = wn <= n4 ? 4 * wn : 4 * n4 + 3 * (wn - n4);
    if (!isffn) c.ftile0 = 1 + 6 * c.wm;
    else { const int fi = (wn - n4) * 2 + c.wm; c.ftile0 = 1 + 2 * (n4 == 2 ? fi : (fi & 1)); }
    if (wn < n4) phase1<4, 1, 0>(c, smem, lds0, ws);
    else if (!isffn) phase1<3, 2, 3>(c, smem, lds0, ws);
    else phase1<3, 2, 1>(c, smem, lds0, ws);
  }
  asm volatile("" ::"v"(pfd));
  STAMP(9);
  if (!isffn) {
    ldsbar();
    STAMP(10);
    const int hh = w >> 1, row = (w & 1) * 64 + lane;
    LAS char* qp = smem + (row + 16) * IMG_LD + hh * 40;
    unsigned qv[10];
#pragma unroll
    for (int i = 0; i < 5; i++) { const v2u t = *(LAS v2u*)(qp + i * 8); qv[2 * i] = t.x; qv[2 * i + 1] = t.y; }
    const int pos = grp * 128 + row;
    float lg[8];
#pragma unroll
    for (int k = 0; k < 8; k++) {
      LAS char* kp = qp + 160 - 2 * k * IMG_LD;
      float a0 = 0, a1 = 0;
#pragma unroll
      for (int i = 0; i < 5; i++) { const v2u t = *(LAS v2u*)(kp + i * 8); a0 = dot2(qv[2 * i], t.x, a0); a1 = dot2(qv[2 * i + 1], t.y, a1); }
      const float l = rbf(rbf(a0 + a1) * 0.22360679774997896f);
      lg[k] = pos - 2 * k >= 0 ? l : -1e9f;
    }
    float mx = lg[0];
#pragma unroll
    for (int k = 1; k < 8; k++) mx = fmaxf(mx, lg[k]);
    float sum = 0;
#pragma unroll
    for (int k = 0; k < 8; k++) { lg[k] = __builtin_amdgcn_exp2f((lg[k] - mx) * 1.4426950408889634f); sum += lg[k]; }
    const float inv = __builtin_amdgcn_rcpf(sum);
    float o[20];
#pragma unroll
    for (int d = 0; d < 20; d++) o[d] = 0;
#pragma unroll
    for (int k = 0; k < 8; k++) {
      const unsigned plo = pk2(lg[k] * inv, 0.f), phi = plo << 16;
      LAS char* vp = qp + 320 - 2 * k * IMG_LD;
#pragma unroll
      for (int i = 0; i < 5; i++) {
        const v2u t = *(LAS v2u*)(vp + i * 8);
        o[4 * i] = dot2(t.x, plo, o[4 * i]); o[4 * i + 1] = dot2(t.x, phi, o[4 * i + 1]);
        o[4 * i + 2] = dot2(t.y, plo, o[4 * i + 2]); o[4 * i + 3] = dot2(t.y, phi, o[4 * i + 3]);
      }
    }
#pragma unroll
    for (int i = 0; i < 5; i++) { v2u t; t.x = pk2(o[4 * i], o[4 * i + 1]); t.y = pk2(o[4 * i + 2], o[4 * i + 3]); *(LAS v2u*)(qp + i * 8) = t; }
    STAMP(11);
    ldsbar();
    const int r = lane & 15, g = lane >> 4;
#pragma unroll
    for (int i = 0; i < 3; i++) {
      const int b = w + 8 * i;
      if (b < 20) {
        const int rp = b / 5, t = b - 5 * rp;
        LAS char* sp = smem + (16 + rp * 32 + r) * IMG_LD + t * 32 + g * 8;
        const v2u lo = *(LAS v2u*)sp, hi = *(LAS v2u*)(sp + 16 * IMG_LD);
        v4u v; v.x = lo.x; v.y = lo.y; v.z = hi.x; v.w = hi.y;
        st128_sc1(c.rsH, lane * 16, ((grp * 4 + rp) * NCT + sub * 5 + t) * 1024, v);
      }
    }
  }
  STAMP(3);
  waitbar<0>();
  if (tid == 0) { v2u t; t.x = (unsigned)tag64; t.y = (unsigned)(tag64 >> 32); st64_sc1(c.rsC, FLAG_OFF + grp * 128 + mem * 8, t); }
  STAMP(4);
  const int rh = j8 >> 2, c3 = half * 4 + (j8 & 3);
  const int r = lane & 15, g = lane >> 4;
  const v4i rsW = w < 2 ? mkrs(wo, DM * DM * 2) : mkrs(w2, FF * DM * 2);
  const int krow0 = w < 2 ? 320 * w : 288 * (w - 2);
  const int u0 = w < 2 ? 10 * w : 20 + 9 * (w - 2);
  const bool ten = w < 2;
  v2u xr[3];
#pragma unroll
  for (int i = 0; i < 3; i++) {
    const int tl = w + 8 * i, tt = tl < 20 ? tl : 19, m = tt / 5, n = tt - 5 * m;
    xr[i] = ld64(c.rsA, (grp * 128 + rh * 64 + m * 16 + r) * (DM * 2) + (c3 * 80 + n * 16 + g * 4) * 2, 0);
  }
  int dvo[5];
#pragma unroll
  for (int i = 0; i < 5; i++) { const int pc = 64 * i + lane, rr = pc / 10, cc = pc - 10 * rr; dvo[i] = rr * (DM * 2) + c3 * 160 + cc * 16; }
  const unsigned ring = lds0 + w * 20480;
  auto dmaB = [&](int s) {
    const int so = (s == 9 && !ten) ? OOB : (krow0 + 32 * s) * (DM * 2);
#pragma unroll
    for (int i = 0; i < 5; i++) dma16(rsW, ring + (s & 3) * 5120 + i * 1024, dvo[i], so);
  };
  dmaB(0); dmaB(1); dmaB(2); dmaB(3);
  {
    unsigned need;
    if (w < 2) need = w == 0 ? 0x000Fu : 0x0F00u;
    else {
      const int t0 = 18 * (w - 2), t1 = t0 + 17;
      const int f0 = t0 < 56 ? t0 / 14 : 4 + (t0 - 56) / 13, f1 = t1 < 56 ? t1 / 14 : 4 + (t1 - 56) / 13;
      need = 0;
      for (int f = f0; f <= f1; f++) need |= 1u << ((f >> 2) * 8 + 4 + (f & 3));
    }
    const bool mine = (need >> (lane & 15)) & 1;
    for (unsigned spins = 0;; spins++) {
      const v2u f = ld64_sc1_wait(c.rsC, FLAG_OFF + grp * 128 + (lane & 15) * 8);
      const bool ok = !mine || (f.x == (unsigned)tag64 && f.y == (unsigned)(tag64 >> 32));
      if (__all(ok) || spins > SPIN_CAP) break;
      __builtin_amdgcn_s_sleep(1);
    }
  }
  STAMP(5);
  v4u A[4][4];
  const int hso = ((grp * 4 + rh * 2) * NCT + 2 * u0) * 1024;
  auto ldA = [&](int s) {
    const int so = (s == 9 && !ten) ? OOB : hso + s * 2048;
    const int so2 = (s == 9 && !ten) ? OOB : hso + s * 2048 + NCT * 1024;
    A[s & 3][0] = ld128_sc1<0>(c.rsH, lane * 16, so); A[s & 3][1] = ld128_sc1<1024>(c.rsH, lane * 16, so);
    A[s & 3][2] = ld128_sc1<0>(c.rsH, lane * 16, so2); A[s & 3][3] = ld128_sc1<1024>(c.rsH, lane * 16, so2);
  };
  ldA(0); ldA(1); ldA(2);
  v8bf bb[2][5];
  LAS char* bbase = smem + w * 20480 + (4 * g + (r >> 2)) * 160 + (lane & 3) * 8;
  auto rdB = [&](int s) {
#pragma unroll
    for (int n = 0; n < 5; n++) bb[s & 1][n] = trfrag(bbase + (s & 3) * 5120 + n * 32, 16 * 160);
  };
  rdB(0);
  f32x4 acc[4][5];
#pragma unroll
  for (int m = 0; m < 4; m++)
#pragma unroll
    for (int n = 0; n < 5; n++) acc[m][n] = f32x4{0, 0, 0, 0};
  auto step = [&](auto S) {
    constexpr int s = decltype(S)::value;
    asm volatile("s_waitcnt vmcnt(%4) lgkmcnt(0)" : "+v"(A[s & 3][0]), "+v"(A[s & 3][1]), "+v"(A[s & 3][2]), "+v"(A[s & 3][3]) : "i"(P3W<s>::v) : "memory");
    if constexpr (s + 4 < 10) dmaB(s + 4);
    if constexpr (s + 3 < 10) ldA(s + 3);
    if constexpr (s + 1 < 10) rdB(s + 1);
    if (s == 9 && !ten) return;
    v8bf a[4];
#pragma unroll
    for (int h = 0; h < 2; h++) {
      const v4u l0 = A[s & 3][2 * h], l1 = A[s & 3][2 * h + 1];
      a[2 * h] = __builtin_bit_cast(v8bf, __builtin_shufflevector(l0, l1, 0, 1, 4, 5));
      a[2 * h + 1] = __builtin_bit_cast(v8bf, __builtin_shufflevector(l0, l1, 2, 3, 6, 7));
    }
#pragma unroll
    for (int m = 0; m < 4; m++)
#pragma unroll
      for (int n = 0; n < 5; n++) acc[m][n] = mfma(bb[s & 1][n], a[m], acc[m][n]);
    __builtin_amdgcn_sched_barrier(0);
  };
  step(std::integral_constant<int, 0>{}); step(std::integral_constant<int, 1>{}); step(std::integral_constant<int, 2>{});
  step(std::integral_constant<int, 3>{}); step(std::integral_constant<int, 4>{}); step(std::integral_constant<int, 5>{});
  step(std::integral_constant<int, 6>{}); step(std::integral_constant<int, 7>{}); step(std::integral_constant<int, 8>{});
  step(std::integral_constant<int, 9>{});
  STAMP(6);
  asm volatile("s_waitcnt vmcnt(0) lgkmcnt(0)" : "+v"(xr[0]), "+v"(xr[1]), "+v"(xr[2])::"memory");
#pragma unroll
  for (int m = 0; m < 4; m++)
#pragma unroll
    for (int n = 0; n < 5; n++) *(LAS f32x4*)(smem + w * 20480 + (m * 5 + n) * 1024 + lane * 16) = acc[m][n];
  STAMP(12);
  ldsbar();
  STAMP(13);
  if (tid == 0 && mem == 0) { v2u t; t.x = (unsigned)tag64; t.y = (unsigned)(tag64 >> 32); st64_sc1(c.rsC, EPOCH_OFF + grp * 256, t); }
#pragma unroll
  for (int i = 0; i < 3; i++) {
    const int tl = w + 8 * i;
    if (tl < 20) {
      const int m = tl / 5, n = tl - 5 * m;
      LAS char* pp = smem + tl * 1024 + lane * 16;
      const f32x4 s = *(LAS f32x4*)pp + *(LAS f32x4*)(pp + 20480);
      f32x4 f = *(LAS f32x4*)(pp + 2 * 20480);
#pragma unroll
      for (int k = 3; k < 8; k++) f += *(LAS f32x4*)(pp + k * 20480);
      float y[4];
#pragma unroll
      for (int j = 0; j < 4; j++) {
        const float xv = (j & 1) ? bfhi(xr[i][j >> 1]) : bflo(xr[i][j >> 1]);
        y[j] = xv + rbf(rbf(s[j]) * rbf(silu_f(rbf(f[j]))));
      }
      v2u o; o.x = pk2(y[0], y[1]); o.y = pk2(y[2], y[3]);
      *(GAS v2u*)((GAS char*)out + (size_t)(grp * 128 + rh * 64 + m * 16 + r) * (DM * 2) + (c3 * 80 + n * 16 + g * 4) * 2) = o;
    }
  }
  STAMP(7);
  asm volatile(".Lcode_end_mark:");
}
}

extern "C" void kernel_launch(void* const* d_in, const int* in_sizes, int n_in, void* d_out, int out_size, void* d_ws, size_t ws_size,
                              hipStream_t stream) {
  (void)hipFuncSetAttribute((const void*)fused_kernel, hipFuncAttributeMaxDynamicSharedMemorySize, LDS_BYTES);
  fused_kernel<<<256, 512, LDS_BYTES, stream>>>((const u16*)d_in[0], (const u16*)d_in[1], (const u16*)d_in[2], (const u16*)d_in[4],
                                                (const u16*)d_in[5], (u16*)d_out, (char*)d_ws);
}
```

```cpp
#include <hip/hip_runtime.h>
#include <stdint.h>
#include <type_traits>
#include <utility>

#define GAS __attribute__((address_space(1)))
#define LAS __attribute__((address_space(3)))
typedef unsigned short u16;
typedef unsigned long long u64;
typedef __bf16 v2bf __attribute__((ext_vector_type(2)));
typedef __bf16 v4bf __attribute__((ext_vector_type(4)));
typedef __bf16 v8bf __attribute__((ext_vector_type(8)));
typedef float f32x2 __attribute__((ext_vector_type(2)));
typedef float f32x4 __attribute__((ext_vector_type(4)));
typedef unsigned v4u __attribute__((ext_vector_type(4)));
typedef unsigned v2u __attribute__((ext_vector_type(2)));
typedef int v4i __attribute__((ext_vector_type(4)));

namespace {
constexpr int SEQ = 2048, DM = 640, FF = 1728;
constexpr int NCT = 148;
constexpr int H_BYTES = 64 * NCT * 1024;
constexpr size_t CTRL_OFF = 16u << 20;
constexpr int EPOCH_OFF = 0, TEPOCH_OFF = 4096, FLAG_OFF = 8192, TFLAG_OFF = 32768, STAT_OFF = 65536;
constexpr size_t BT_OFF = 12u << 20;
constexpr int BT_SLICE = 74 * 5120;
constexpr int A_BYTES = 144 * 128, B_BYTES = 64 * 512, ST_BYTES = A_BYTES + B_BYTES;
constexpr int CS_OFF = 3 * ST_BYTES, RS_OFF = CS_OFF + 1024;
constexpr int DUMP_OFF = CS_OFF + 4096;
constexpr int IMG_LD = 488;
constexpr int RING_OFF = 71680;
constexpr int LDS_BYTES = 163840;
constexpr int OOB = 0x7FF00000;
constexpr unsigned SPIN_CAP = 1u << 16;
#define POLL_SLEEP 8
#define STAMP(i)
#define SB __builtin_amdgcn_sched_barrier(0)
#define LDPOL2 " sc1"

__device__ __forceinline__ v4i mkrs(const void* p, int bytes) {
  u64 a = (u64)p; v4i r; r.x = (int)(unsigned)a; r.y = (int)((unsigned)(a >> 32) & 0xFFFFu); r.z = bytes; r.w = 0x00020000; return r;
}
__device__ __forceinline__ void dma16(v4i rs, unsigned lds, int voff, int soff) {
  asm volatile("s_mov_b32 m0, %0\n\ts_nop 0\n\tbuffer_load_dwordx4 %1, %2, %3 offen lds" ::"s"(lds), "v"(voff), "s"(rs), "s"(soff));
}
__device__ __forceinline__ void dma16_sc1(v4i rs, unsigned lds, int voff, int soff) {
  asm volatile("s_mov_b32 m0, %0\n\ts_nop 0\n\tbuffer_load_dwordx4 %1, %2, %3 offen" LDPOL2 " lds" ::"s"(lds), "v"(voff), "s"(rs), "s"(soff));
}
__device__ __forceinline__ void dma16_50(v4i rs, unsigned lds, int voff, int soff) {
  asm volatile("s_mov_b32 m0, %0\n\ts_mov_b32 exec_hi, 0x3ffff\n\tbuffer_load_dwordx4 %1, %2, %3 offen lds\n\ts_mov_b32 exec_hi, -1" ::"s"(lds), "v"(voff), "s"(rs), "s"(soff));
}
#define LDPOL " sc1"
#define STPOL " sc1"
template <int IMM> __device__ __forceinline__ v4u ld128_sc1(v4i rs, int voff, int soff) {
  v4u r; asm volatile("buffer_load_dwordx4 %0, %1, %2, %3 offen offset:%4" LDPOL : "=v"(r) : "v"(voff), "s"(rs), "s"(soff), "i"(IMM)); return r;
}
template <int IMM> __device__ __forceinline__ v4u ld128(v4i rs, int voff, int soff) {
  v4u r; asm volatile("buffer_load_dwordx4 %0, %1, %2, %3 offen offset:%4" : "=v"(r) : "v"(voff), "s"(rs), "s"(soff), "i"(IMM)); return r;
}
template <int IMM> __device__ __forceinline__ unsigned ld16(v4i rs, int voff, int soff) {
  unsigned r; asm volatile("buffer_load_ushort %0, %1, %2, %3 offen offset:%4" : "=v"(r) : "v"(voff), "s"(rs), "s"(soff), "i"(IMM)); return r;
}
__device__ __forceinline__ v2u ld64(v4i rs, int voff, int soff) {
  v2u r; asm volatile("buffer_load_dwordx2 %0, %1, %2, %3 offen" : "=v"(r) : "v"(voff), "s"(rs), "s"(soff)); return r;
}
__device__ __forceinline__ v2u ld64_sc1(v4i rs, int voff, int soff) {
  v2u r; asm volatile("buffer_load_dwordx2 %0, %1, %2, %3 offen sc1" : "=v"(r) : "v"(voff), "s"(rs), "s"(soff)); return r;
}
__device__ __forceinline__ v2u ld64_sc1_wait(v4i rs, int voff) {
  v2u r; asm volatile("buffer_load_dwordx2 %0, %1, %2, 0 offen sc1\n\ts_waitcnt vmcnt(0)" : "=&v"(r) : "v"(voff), "s"(rs) : "memory"); return r;
}
__device__ __forceinline__ void st128_sc1(v4i rs, int voff, int soff, v4u v) {
  asm volatile("buffer_store_dwordx4 %0, %1, %2, %3 offen" STPOL "\n\ts_nop 1" ::"v"(v), "v"(voff), "s"(rs), "s"(soff) : "memory");
}
__device__ __forceinline__ void st64_sc1(v4i rs, int voff, v2u v) {
  asm volatile("buffer_store_dwordx2 %0, %1, %2, 0 offen sc1" ::"v"(v), "v"(voff), "s"(rs) : "memory");
}
__device__ __forceinline__ void st32_sc1(v4i rs, int voff, unsigned v) {
  asm volatile("buffer_store_dword %0, %1, %2, 0 offen sc1" ::"v"(v), "v"(voff), "s"(rs) : "memory");
}
template <int N> __device__ __forceinline__ void waitbar() { asm volatile("s_waitcnt vmcnt(%0) lgkmcnt(0)\n\ts_barrier" ::"i"(N) : "memory"); }
__device__ __forceinline__ void ldsbar() { asm volatile("s_waitcnt lgkmcnt(0)\n\ts_barrier" ::: "memory"); }
__device__ __forceinline__ void drain() { asm volatile("s_waitcnt vmcnt(0)" ::: "memory"); }
__device__ __forceinline__ float bflo(unsigned u) { return __builtin_bit_cast(float, u << 16); }
__device__ __forceinline__ float bfhi(unsigned u) { return __builtin_bit_cast(float, u & 0xFFFF0000u); }
__device__ __forceinline__ unsigned pk2(float a, float b) {
  v2bf v; v.x = (__bf16)a; v.y = (__bf16)b; return __builtin_bit_cast(unsigned, v);
}
__device__ __forceinline__ float rbf(float a) { return (float)(__bf16)a; }
__device__ __forceinline__ float dot2(unsigned a, unsigned b, float c) {
  return __builtin_amdgcn_fdot2_f32_bf16(__builtin_bit_cast(v2bf, a), __builtin_bit_cast(v2bf, b), c, false);
}
__device__ __forceinline__ f32x4 mfma(v8bf a, v8bf b, f32x4 c) { return __builtin_amdgcn_mfma_f32_16x16x32_bf16(a, b, c, 0, 0, 0); }
__device__ __forceinline__ v8bf trfrag(LAS char* p, int second) {
  v4bf lo = __builtin_amdgcn_ds_read_tr16_b64_v4bf16((LAS v4bf*)p);
  v4bf hi = __builtin_amdgcn_ds_read_tr16_b64_v4bf16((LAS v4bf*)(p + second));
  return __builtin_shufflevector(lo, hi, 0, 1, 2, 3, 4, 5, 6, 7);
}
__device__ __forceinline__ float row8_sum(float v) {
  v += __builtin_bit_cast(float, __builtin_amdgcn_update_dpp(0, __builtin_bit_cast(int, v), 0xB1, 0xF, 0xF, false));
  v += __builtin_bit_cast(float, __builtin_amdgcn_update_dpp(0, __builtin_bit_cast(int, v), 0x4E, 0xF, 0xF, false));
  v += __builtin_bit_cast(float, __builtin_amdgcn_update_dpp(0, __builtin_bit_cast(int, v), 0x141, 0xF, 0xF, false));
  return v;
}
__device__ __forceinline__ float silu_f(float y) { return y * __builtin_amdgcn_rcpf(1.0f + __builtin_amdgcn_exp2f(y * -1.4426950408889634f)); }
template <class F, int... I> __device__ __forceinline__ void static_for_impl(F&& f, std::integer_sequence<int, I...>) { (f(std::integral_constant<int, I>{}), ...); }
template <int N, class F> __device__ __forceinline__ void static_for(F&& f) { static_for_impl(f, std::make_integer_sequence<int, N>{}); }

struct P1 {
  v4i rsA, rsB, rsH, rsC, rsT, rsPf;
  int avoff, asvoff, bvoff, ldb2;
  int pfso, tfoff;
  int w, wm, lane, grp, mem, c3, rot;
  int tstart, ftile0;
  int isffn, htile0;
  unsigned tag, ttag;
};
__device__ __forceinline__ int h_u0(int hw) { return 14 * hw - ((hw + 1) >> 2); }
__device__ __forceinline__ int h_nk(int hw) { return 14 - (hw >> 1); }
__device__ __forceinline__ int h_k(int hw, int rot, int p) {
  const int nk = h_nk(hw); int k = p + rot; k = k >= nk ? k - nk : k; return h_u0(hw) + k;
}

__device__ __forceinline__ void p1_issue(const P1& c, unsigned lds0, int st, int slot) {
  const unsigned base = lds0 + slot * ST_BYTES;
  if (!c.isffn && c.w < 2) dma16(c.rsA, base + c.w * 1024, c.asvoff, st * 128);
#pragma unroll
  for (int i = 0; i < 2; i++) dma16(c.rsA, base + (2 + c.w + 8 * i) * 1024, c.avoff, st * 128 + i * 64 * (DM * 2));
#pragma unroll
  for (int i = 0; i < 4; i++) dma16(c.rsB, base + A_BYTES + (c.w + 8 * i) * 1024, c.bvoff, (st * 64 + i * 16) * c.ldb2);
}
template <int B0, int B1> __device__ __forceinline__ void tail_issue(const P1& c, unsigned lds0) {
  int w = c.w;
  asm volatile("" : "+s"(w));
#pragma unroll
  for (int i = 0; i < (B1 - B0 + 7) / 8; i++) {
    int b = B0 + w + 8 * i; b = b < B1 ? b : B1 - 1;
    const int hw = b / 20, e = b - 20 * hw, sq = e / 5;
    dma16_sc1(c.rsT, lds0 + RING_OFF + b * 1024, c.lane * 16, (20 + h_k(hw, c.rot, h_nk(hw) - 4 + sq)) * 5120 + (e - 5 * sq) * 1024);
  }
}

template <int NT, int NOWN, int NFOR>
__device__ __forceinline__ void phase1(P1& c, LAS char* smem, unsigned lds0, char* ws) {
  constexpr int NB = NT + NFOR, NX = NOWN + NFOR;
  const int lane = c.lane, r = lane & 15, g = lane >> 4, q = r >> 2, p = lane & 3;
  int aad[2];
#pragma unroll
  for (int ks = 0; ks < 2; ks++) aad[ks] = r * 128 + (((4 * ks + g) ^ (r >> 1)) << 4);
  const int amain = (1 + c.wm * 4) * 2048;
  const int z = q | ((g & 1) << 2);
  int tile[NB], bad[NB];
#pragma unroll
  for (int n = 0; n < NB; n++) {
    if (n < NT) { int t = n + 2 * c.wm; t = t >= NT ? t - NT : t; tile[n] = c.tstart + t; }
    else tile[n] = c.ftile0 + 2 * (n - NT);
    bad[n] = A_BYTES + (8 * g + q) * 512 + (((tile[n] & 7) ^ z) << 5) + (tile[n] >> 3) * 256 + p * 8;
  }
  f32x4 acc[4][NT], accx[NX];
#pragma unroll
  for (int m = 0; m < 4; m++)
#pragma unroll
    for (int n = 0; n < NT; n++) acc[m][n] = f32x4{0, 0, 0, 0};
#pragma unroll
  for (int n = 0; n < NX; n++) accx[n] = f32x4{0, 0, 0, 0};
  v8bf af[2][4], as[2], bf[2][NB];
  const bool ones = (r == 0) || c.isffn;
  auto ldfr = [&](int buf, int slot, int ks) {
    LAS char* base = smem + slot * ST_BYTES;
#pragma unroll
    for (int m = 0; m < 4; m++) af[buf][m] = *(LAS v8bf*)(base + aad[ks] + amain + m * 2048);
#pragma unroll
    for (int n = 0; n < NB; n++) bf[buf][n] = trfrag(base + bad[n] + ks * 16384, 2048);
    as[buf] = *(LAS v8bf*)(base + aad[ks]);
  };
  auto mma = [&](int buf) {
#pragma unroll
    for (int m = 0; m < 4; m++)
#pragma unroll
      for (int n = 0; n < NT; n++) acc[m][n] = mfma(bf[buf][n], af[buf][m], acc[m][n]);
    v4u s = __builtin_bit_cast(v4u, as[buf]);
#pragma unroll
    for (int e = 0; e < 4; e++) s[e] = ones ? 0x3F803F80u : s[e];
    v8bf sv = __builtin_bit_cast(v8bf, s);
#pragma unroll
    for (int n = 0; n < NX; n++) accx[n] = mfma(bf[buf][n < NOWN ? n : NT + n - NOWN], sv, accx[n]);
#pragma unroll
    for (int i = 0; i < 4 * NT + NX; i++) {
      __builtin_amdgcn_sched_group_barrier(0x8, 1, 0); __builtin_amdgcn_sched_group_barrier(0x100, 1, 0); __builtin_amdgcn_sched_group_barrier(0x2, 1, 0);
    }
    SB;
  };
  waitbar<12>();
  STAMP(1);
  unsigned pfd = 0;
  auto issue = [&](int st, int slot) {
    if (st < 7) {
      p1_issue(c, lds0, st + 3, slot);
      if (st == 0) asm volatile("buffer_load_dword %0, %1, %2, %3 offen" : "=v"(pfd) : "v"(lane * 128), "s"(c.rsPf), "s"(c.pfso));
      if (st == 2) st32_sc1(c.rsC, c.tfoff, c.ttag);
    } else if (st == 7) {
      if (!c.isffn) {
        for (unsigned spins = 0;; spins++) {
          const v2u gr = ld64_sc1_wait(c.rsC, STAT_OFF + c.grp * 2048 + (144 + c.c3) * 8);
          c.ttag = __builtin_amdgcn_readfirstlane(gr.x);
          if (__builtin_amdgcn_readfirstlane(gr.y) == c.tag || spins > SPIN_CAP) break;
          __builtin_amdgcn_s_sleep(POLL_SLEEP);
        }
      }
      const int off = lane < 37 ? TFLAG_OFF + c.c3 * 512 + lane * 8 : OOB;
      for (unsigned spins = 0;; spins++) {
        const v2u f = ld64_sc1_wait(c.rsC, off);
        if (__all(lane >= 37 || (f.x == c.ttag && f.y == c.ttag)) || spins > SPIN_CAP) break;
        __builtin_amdgcn_s_sleep(POLL_SLEEP);
      }
      tail_issue<0, 30>(c, lds0);
    } else tail_issue<30, 80>(c, lds0);
  };
  ldfr(0, 0, 0);
  int slot = 0;
#pragma nounroll
  for (int st = 0; st < 9; st++) {
    ldfr(1, slot, 1);
    mma(0);
    const int nslot = slot == 2 ? 0 : slot + 1;
    waitbar<6>();
    SB;
    if (c.w < 4) issue(st, slot);
    SB;
    ldfr(0, nslot, 0);
    mma(1);
    if (c.w >= 4) issue(st, slot);
    SB;
    slot = nslot;
  }
  ldfr(1, 0, 1);
  mma(0);
  mma(1);
  STAMP(2);
#pragma unroll
  for (int n = 0; n < NX; n++)
    *(LAS f32x4*)(smem + (r == 0 ? CS_OFF + (tile[n < NOWN ? n : NT + n - NOWN] * 16 + 4 * g) * 4 : DUMP_OFF + lane * 16)) = accx[n];
  if (c.w < 3) {
    const int t = c.w * 64 + lane, tt = t < 144 ? t : 143;
    v2u gr;
    for (unsigned spins = 0;; spins++) {
      gr = ld64_sc1_wait(c.rsC, STAT_OFF + c.grp * 2048 + tt * 8);
      if (__all(gr.y == c.tag) || spins > SPIN_CAP) break;
      __builtin_amdgcn_s_sleep(1);
    }
    f32x2 ms; ms.x = bflo(gr.x); ms.y = bfhi(gr.x);
    *(LAS f32x2*)(smem + RS_OFF + tt * 8) = ms;
  }
  waitbar<0>();
  asm volatile("" ::"v"(pfd));
  STAMP(8);
  f32x2 rs[4];
#pragma unroll
  for (int m = 0; m < 4; m++) rs[m] = *(LAS f32x2*)(smem + RS_OFF + (16 + c.wm * 64 + m * 16 + r) * 8);
  if (c.isffn) {
    float c1[4];
#pragma unroll
    for (int m = 0; m < 4; m++) c1[m] = __builtin_amdgcn_rcpf(rs[m].y) * -1.4426950408889634f;
#pragma unroll
    for (int n = 0; n < NT; n++) {
      const f32x4 cs = *(LAS f32x4*)(smem + CS_OFF + (tile[n] * 16 + 4 * g) * 4);
#pragma unroll
      for (int mp = 0; mp < 2; mp++) {
        v4u o;
#pragma unroll
        for (int h = 0; h < 2; h++) {
          const int m = 2 * mp + h;
          const f32x4 t = acc[m][n] - rs[m].x * cs;
          const f32x4 zz = t * c1[m];
          f32x4 e;
#pragma unroll
          for (int j = 0; j < 4; j++) e[j] = __builtin_amdgcn_exp2f(zz[j]);
          const f32x4 d = e * rs[m].y + rs[m].y;
          f32x4 rr;
#pragma unroll
          for (int j = 0; j < 4; j++) rr[j] = __builtin_amdgcn_rcpf(d[j]);
          const f32x4 y = t * rr;
          o[2 * h] = pk2(y[0], y[1]); o[2 * h + 1] = pk2(y[2], y[3]);
        }
        st128_sc1(c.rsH, lane * 16, ((c.grp * 4 + c.wm * 2 + mp) * NCT + c.htile0 + tile[n]) * 1024, o);
      }
    }
    drain();
    st32_sc1(c.rsC, lane == 0 ? FLAG_OFF + c.grp * 512 + (c.mem * 8 + c.w) * 4 : OOB, c.tag);
  } else {
    float ri[4];
#pragma unroll
    for (int m = 0; m < 4; m++) ri[m] = __builtin_amdgcn_rcpf(rs[m].y);
#pragma unroll
    for (int n = 0; n < NT; n++) {
      const f32x4 cs = *(LAS f32x4*)(smem + CS_OFF + (tile[n] * 16 + 4 * g) * 4);
#pragma unroll
      for (int m = 0; m < 4; m++) {
        const f32x4 y = (acc[m][n] - rs[m].x * cs) * ri[m];
        v2u o; o.x = pk2(y[0], y[1]); o.y = pk2(y[2], y[3]);
        *(LAS v2u*)(smem + (16 + c.wm * 64 + m * 16 + r) * IMG_LD + tile[n] * 32 + g * 8) = o;
      }
    }
    const f32x2 rh = *(LAS f32x2*)(smem + RS_OFF + r * 8);
    const float rih = __builtin_amdgcn_rcpf(rh.y);
#pragma unroll
    for (int n = 0; n < NX; n++) {
      const int t = tile[n < NOWN ? n : NT + n - NOWN];
      const f32x4 cs = *(LAS f32x4*)(smem + CS_OFF + (t * 16 + 4 * g) * 4);
      const f32x4 y = (accx[n] - rh.x * cs) * rih;
      v2u o; o.x = pk2(y[0], y[1]); o.y = pk2(y[2], y[3]);
      *(LAS v2u*)(smem + r * IMG_LD + t * 32 + g * 8) = o;
    }
    ldsbar();
  }
}

__device__ __forceinline__ void wait_flags(const P1& c, u64 need_lo, u64 need_hi) {
  const bool n0 = (need_lo >> c.lane) & 1;
  const int off = n0 ? FLAG_OFF + c.grp * 512 + c.lane * 8 : OOB;
  for (unsigned spins = 0;; spins++) {
    const v2u f = ld64_sc1_wait(c.rsC, off);
    const bool ok = !n0 || (f.x == c.tag && f.y == c.tag);
    if (__all(ok) || spins > SPIN_CAP) break;
    __builtin_amdgcn_s_sleep(POLL_SLEEP);
  }
}
__device__ __forceinline__ void mma20(f32x4 (&acc)[4][5], const v4u (&b)[5], const v4u (&A)[4]) {
  v4u a[4];
#pragma unroll
  for (int h = 0; h < 2; h++) {
    const v4u l0 = A[2 * h], l1 = A[2 * h + 1];
    a[2 * h] = __builtin_shufflevector(l0, l1, 0, 1, 4, 5);
    a[2 * h + 1] = __builtin_shufflevector(l0, l1, 2, 3, 6, 7);
  }
  asm volatile("s_nop 1\n\t"
               "v_mfma_f32_16x16x32_bf16 %0, %20, %25, %0\n\t"
               "v_mfma_f32_16x16x32_bf16 %1, %21, %25, %1\n\t"
               "v_mfma_f32_16x16x32_bf16 %2, %22, %25, %2\n\t"
               "v_mfma_f32_16x16x32_bf16 %3, %23, %25, %3\n\t"
               "v_mfma_f32_16x16x32_bf16 %4, %24, %25, %4\n\t"
               "v_mfma_f32_16x16x32_bf16 %5, %20, %26, %5\n\t"
               "v_mfma_f32_16x16x32_bf16 %6, %21, %26, %6\n\t"
               "v_mfma_f32_16x16x32_bf16 %7, %22, %26, %7\n\t"
               "v_mfma_f32_16x16x32_bf16 %8, %23, %26, %8\n\t"
               "v_mfma_f32_16x16x32_bf16 %9, %24, %26, %9\n\t"
               "v_mfma_f32_16x16x32_bf16 %10, %20, %27, %10\n\t"
               "v_mfma_f32_16x16x32_bf16 %11, %21, %27, %11\n\t"
               "v_mfma_f32_16x16x32_bf16 %12, %22, %27, %12\n\t"
               "v_mfma_f32_16x16x32_bf16 %13, %23, %27, %13\n\t"
               "v_mfma_f32_16x16x32_bf16 %14, %24, %27, %14\n\t"
               "v_mfma_f32_16x16x32_bf16 %15, %20, %28, %15\n\t"
               "v_mfma_f32_16x16x32_bf16 %16, %21, %28, %16\n\t"
               "v_mfma_f32_16x16x32_bf16 %17, %22, %28, %17\n\t"
               "v_mfma_f32_16x16x32_bf16 %18, %23, %28, %18\n\t"
               "v_mfma_f32_16x16x32_bf16 %19, %24, %28, %19\n\t"
               : "+v"(acc[0][0]), "+v"(acc[0][1]), "+v"(acc[0][2]), "+v"(acc[0][3]), "+v"(acc[0][4]), "+v"(acc[1][0]), "+v"(acc[1][1]), "+v"(acc[1][2]), "+v"(acc[1][3]), "+v"(acc[1][4]), "+v"(acc[2][0]), "+v"(acc[2][1]), "+v"(acc[2][2]), "+v"(acc[2][3]), "+v"(acc[2][4]), "+v"(acc[3][0]), "+v"(acc[3][1]), "+v"(acc[3][2]), "+v"(acc[3][3]), "+v"(acc[3][4])
               : "v"(b[0]), "v"(b[1]), "v"(b[2]), "v"(b[3]), "v"(b[4]), "v"(a[0]), "v"(a[1]), "v"(a[2]), "v"(a[3]));
}
__device__ __forceinline__ void ldA4(v4u (&A)[4], v4i rsH, int voff, int so) {
  const int so2 = so == OOB ? OOB : so + NCT * 1024;
  A[0] = ld128_sc1<0>(rsH, voff, so); A[1] = ld128_sc1<1024>(rsH, voff, so);
  A[2] = ld128_sc1<0>(rsH, voff, so2); A[3] = ld128_sc1<1024>(rsH, voff, so2);
}
#define WAITA(A, N) asm volatile("s_waitcnt vmcnt(%4) lgkmcnt(0)" : "+v"((A)[0]), "+v"((A)[1]), "+v"((A)[2]), "+v"((A)[3]) : "i"(N) : "memory")
constexpr int hwait(int s) {
  auto cnt = [](int t) { return (t + 4 < 14 ? 4 : 0) + (t + 3 < 10 ? 5 : 0); };
  int n = 0;
  if (s < 3) { n = 4 * (3 - s); for (int t = 0; t < s; t++) n += cnt(t); }
  else if (s < 10) { for (int t = s - 2; t < s; t++) n += cnt(t); }
  else { n = (s - 4 + 3 < 10 ? 5 : 0); for (int t = s - 3; t < s; t++) n += cnt(t); }
  return n;
}
__device__ __forceinline__ void ldB5(v4u (&B)[5], v4i rsT, int voff, int so) {
  const int so2 = so == OOB ? OOB : so + 4096;
  B[0] = ld128_sc1<0>(rsT, voff, so); B[1] = ld128_sc1<1024>(rsT, voff, so); B[2] = ld128_sc1<2048>(rsT, voff, so);
  B[3] = ld128_sc1<3072>(rsT, voff, so); B[4] = ld128_sc1<0>(rsT, voff, so2);
}
#define TIE5(B) asm volatile("" : "+v"((B)[0]), "+v"((B)[1]), "+v"((B)[2]), "+v"((B)[3]), "+v"((B)[4]))

__global__ __launch_bounds__(512, 2) void fused_kernel(const u16* __restrict__ x, const u16* __restrict__ wqkv, const u16* __restrict__ wo,
                                                       const u16* __restrict__ w1, const u16* __restrict__ w2, u16* __restrict__ out,
                                                       char* __restrict__ ws) {
  extern __shared__ __attribute__((aligned(16))) char smem_[];
  LAS char* smem = (LAS char*)smem_;
  const unsigned lds0 = (unsigned)(size_t)smem;
  const int tid = threadIdx.x, lane = tid & 63, w = __builtin_amdgcn_readfirstlane(tid >> 6);
  const int bid = blockIdx.x, xcd = bid & 7, idx = bid >> 3, half = xcd & 1, j8 = idx & 7;
  const int grp = (xcd >> 1) * 4 + (idx >> 3), mem = half * 8 + j8;
  const int isffn = j8 >> 2, sub = half * 4 + (j8 & 3);
  STAMP(0);
  u64 pc0; unsigned codelen, cpf = 0;
  asm volatile("s_getpc_b64 %0\n.Lcode_begin_mark:\n\ts_mov_b32 %1, .Lcode_end_mark-.Lcode_begin_mark" : "=s"(pc0), "=s"(codelen));
  if (bid < 8) {
    const v4i rp = mkrs((const void*)pc0, (int)codelen);
    asm volatile("buffer_load_dword %0, %1, %2, %3 offen" : "=v"(cpf) : "v"(lane * 128), "s"(rp), "s"(w * 8192));
  }
  P1 c;
  c.rsA = mkrs(x, SEQ * DM * 2);
  c.rsB = isffn ? mkrs(w1, DM * FF * 2) : mkrs(wqkv, DM * 3 * DM * 2);
  c.rsH = mkrs(ws, H_BYTES);
  c.rsC = mkrs(ws + CTRL_OFF, 1 << 20);
  c.rsT = mkrs(ws + BT_OFF + (size_t)sub * BT_SLICE, BT_SLICE);
  c.ldb2 = isffn ? FF * 2 : 3 * DM * 2;
  c.w = w; c.lane = lane; c.grp = grp; c.isffn = isffn; c.mem = mem; c.c3 = sub;
  c.rot = ((sub + grp + 2 * isffn) & 3) * 7 >> 1;
  c.wm = w >> 2;
  const int wn = w < 4 ? w : 7 - w;
  const int ftile0 = sub < 4 ? sub * 14 : 56 + (sub - 4) * 13;
  const int ntile = isffn ? (sub < 4 ? 14 : 13) : 15;
  c.htile0 = 40 + ftile0;
  v2u ep = ld64_sc1(c.rsC, EPOCH_OFF + grp * 256, 0);
  v2u tep = ld64_sc1(c.rsC, isffn ? TEPOCH_OFF + sub * 256 : OOB, 0);
  unsigned pre[40];
  v4u xs[10];
  const int sslot = w * 8 + (lane >> 3);
  const int kap = (7 - w) * 16 + grp;
  const bool dostat = isffn && w < 3, dotile = isffn && w >= 3 && kap < 74;
  if (dostat) {
    int rho = sub * 18 + (sslot < 18 ? sslot : 17);
    int row = grp * 128 - 16 + rho; row = row < 0 ? 0 : row;
    const int vo = row * (DM * 2) + (lane & 7) * 16;
    static_for<10>([&](auto I) { constexpr int i = decltype(I)::value; xs[i] = ld128<128 * i>(c.rsA, vo, 0); });
  } else if (dotile) {
    const v4i rw = mkrs(kap < 20 ? wo : w2, kap < 20 ? DM * DM * 2 : DM * FF * 2);
    int kq = kap;
    if (kap >= 20) asm volatile("s_sub_u32 %0, %0, 20" : "+s"(kq)::"scc");
    const int so = kq * 32 * (DM * 2);
    const int vo = (lane >> 4) * 4 * (DM * 2) + (sub * 80 + (lane & 15)) * 2;
    static_for<40>([&](auto I) { constexpr int i = decltype(I)::value, n = i >> 3, j = i & 7;
      pre[i] = ld16<(j & 3) * (DM * 2) + n * 32>(rw, vo, so + (j >> 2) * 16 * (DM * 2)); });
  }
  {
    const int ar = lane >> 3, sw = ((lane & 7) ^ ((4 * w + (lane >> 4)) & 7)) << 4;
    c.avoff = (grp * 128 + 8 * w + ar) * (DM * 2) + sw;
    int hr = grp * 128 - 16 + 8 * w + ar; hr = hr < 0 ? 0 : hr;
    c.asvoff = hr * (DM * 2) + sw;
    const int kk = 2 * w + (lane >> 5), zz = (kk & 3) | (((kk >> 3) & 1) << 2), pc = lane & 31, seg = (pc >> 1) ^ zz;
    int col;
    if (isffn) col = (ftile0 + seg) * 16;
    else { const int part = seg >= 10 ? 2 : (seg >= 5 ? 1 : 0); col = part * DM + sub * 80 + (seg - 5 * part) * 16; }
    c.bvoff = seg < ntile ? kk * c.ldb2 + col * 2 + (pc & 1) * 16 : OOB;
  }
  p1_issue(c, lds0, 0, 0); p1_issue(c, lds0, 1, 1); p1_issue(c, lds0, 2, 2);
  {
    const int gw = bid * 8 + w;
    if (gw < 300) { c.rsPf = mkrs(wqkv, DM * 3 * DM * 2); c.pfso = gw * 8192; }
    else if (gw < 570) { c.rsPf = mkrs(w1, DM * FF * 2); c.pfso = (gw - 300) * 8192; }
    else { c.rsPf = c.rsA; c.pfso = (gw - 570) * 8192; }
  }
  asm volatile("s_waitcnt vmcnt(18)" : "+v"(ep), "+v"(tep), "+v"(cpf)::"memory");
  u64 tag64, ttag64;
  {
    const u64 e = ((u64)(unsigned)__builtin_amdgcn_readfirstlane(ep.y) << 32) | (unsigned)__builtin_amdgcn_readfirstlane(ep.x);
    tag64 = e * 0x5851F42D4C957F2Dull + 0x14057B7EF767814Full;
    const u64 t = ((u64)(unsigned)__builtin_amdgcn_readfirstlane(tep.y) << 32) | (unsigned)__builtin_amdgcn_readfirstlane(tep.x);
    ttag64 = t * 0x5851F42D4C957F2Dull + 0x14057B7EF767814Full;
  }
  c.tag = (unsigned)tag64; c.ttag = (unsigned)ttag64;
  c.tfoff = (dotile && lane == 0) ? TFLAG_OFF + sub * 512 + kap * 4 : OOB;
  if (dostat) {
    asm volatile("" : "+v"(xs[0]), "+v"(xs[1]), "+v"(xs[2]), "+v"(xs[3]), "+v"(xs[4]), "+v"(xs[5]), "+v"(xs[6]), "+v"(xs[7]), "+v"(xs[8]), "+v"(xs[9]));
    float s = 0;
#pragma unroll
    for (int i = 0; i < 40; i++) s += bflo(xs[i >> 2][i & 3]) + bfhi(xs[i >> 2][i & 3]);
    const float mean = row8_sum(s) * (1.0f / DM);
    float v = 0;
#pragma unroll
    for (int i = 0; i < 40; i++) { const float a = bflo(xs[i >> 2][i & 3]) - mean, b = bfhi(xs[i >> 2][i & 3]) - mean; v += a * a + b * b; }
    const float var = row8_sum(v) * (1.0f / DM);
    const float sd = __builtin_sqrtf(rbf(rbf(var) + 1e-5f));
    v2u gr; gr.x = sslot == 18 ? c.ttag : pk2(mean, sd); gr.y = c.tag;
    st64_sc1(c.rsC, ((lane & 7) == 0 && sslot <= 18) ? STAT_OFF + grp * 2048 + (sslot == 18 ? 144 + sub : sub * 18 + sslot) * 8 : OOB, gr);
  } else if (dotile) {
#pragma unroll
    for (int i = 0; i < 40; i += 10)
      asm volatile("" : "+v"(pre[i]), "+v"(pre[i + 1]), "+v"(pre[i + 2]), "+v"(pre[i + 3]), "+v"(pre[i + 4]), "+v"(pre[i + 5]), "+v"(pre[i + 6]), "+v"(pre[i + 7]), "+v"(pre[i + 8]), "+v"(pre[i + 9]));
#pragma unroll
    for (int n = 0; n < 5; n++) {
      v4u v;
#pragma unroll
      for (int e = 0; e < 4; e++) v[e] = pre[8 * n + 2 * e] | (pre[8 * n + 2 * e + 1] << 16);
      st128_sc1(c.rsT, lane * 16, (kap * 5 + n) * 1024, v);
    }
  }
  {
    const int n4 = ntile - 12;
    c.tstart = wn <= n4 ? 4 * wn : 4 * n4 + 3 * (wn - n4);
    if (!isffn) c.ftile0 = 1 + 6 * c.wm;
    else { const int fi = (wn - n4) * 2 + c.wm; c.ftile0 = 1 + 2 * (n4 == 2 ? fi : (fi & 1)); }
    if (wn < n4) phase1<4, 1, 0>(c, smem, lds0, ws);
    else if (!isffn) phase1<3, 2, 3>(c, smem, lds0, ws);
    else phase1<3, 2, 1>(c, smem, lds0, ws);
  }
  STAMP(9);
  const int rh = j8 >> 2, c3 = sub;
  const int r = lane & 15, g = lane >> 4;
  v2u xr[3];
  auto put = [&](const f32x4 (&acc)[4][5], int p0, int p1) {
    STAMP(6);
    asm volatile("s_nop 7\n\ts_nop 3\n\ts_waitcnt vmcnt(0) lgkmcnt(0)" ::: "memory");
#pragma unroll
    for (int m = 0; m < 4; m++)
#pragma unroll
      for (int n = 0; n < 5; n++) *(LAS f32x4*)(smem + (m < 2 ? p0 : p1) + (m * 5 + n) * 1024 + lane * 16) = acc[m][n];
  };
  auto ldx = [&]() {
#pragma unroll
    for (int i = 0; i < 3; i++) {
      const int tl = w + 8 * i, tt = tl < 20 ? tl : 19, m = tt / 5, n = tt - 5 * m;
      xr[i] = ld64(c.rsA, (grp * 128 + rh * 64 + m * 16 + r) * (DM * 2) + (c3 * 80 + n * 16 + g * 4) * 2, 0);
    }
  };
  const int hrow = (grp * 4 + rh * 2) * NCT * 1024;
  if (w >= 4) {
    const int hw = w & 3, u0 = h_u0(hw), nks = h_nk(hw);
    v4u A[4][4], B[3][5];
    f32x4 acc[4][5];
#pragma unroll
    for (int m = 0; m < 4; m++)
#pragma unroll
      for (int n = 0; n < 5; n++) acc[m][n] = f32x4{0, 0, 0, 0};
    auto ldA = [&](int s) { ldA4(A[s & 3], c.rsH, lane * 16, (s != 9 || hw < 2) ? hrow + (40 + 2 * h_k(hw, c.rot, s < 10 ? s : nks - 14 + s)) * 1024 : OOB); };
    auto ldB = [&](int s) { ldB5(B[s % 3], c.rsT, lane * 16, (s < 9 || hw < 2) ? (20 + h_k(hw, c.rot, s)) * 5120 : OOB); };
    STAMP(4);
    ldB(0); ldB(1); ldB(2);
    ldx();
    {
      const int t0 = 2 * u0, t1 = 2 * (u0 + nks) - 1;
      const int f0 = t0 < 56 ? t0 / 14 : 4 + (t0 - 56) / 13, f1 = t1 < 56 ? t1 / 14 : 4 + (t1 - 56) / 13;
      u64 need = 0;
      for (int f = f0; f <= f1; f++) need |= 3ull << (((f >> 2) * 8 + 4 + (f & 3)) * 4 + rh * 2);
      wait_flags(c, need, need);
    }
    STAMP(5);
    ldA(0); ldA(1); ldA(2); ldA(3);
    LAS char* bl = smem + RING_OFF + hw * 20480 + lane * 16;
    static_for<14>([&](auto S) {
      constexpr int s = decltype(S)::value;
      WAITA(A[s & 3], hwait(s));
      if constexpr (s < 10) TIE5(B[s % 3]);
      mma20(acc, B[s % 3], A[s & 3]);
      SB;
      if constexpr (s + 4 < 14) ldA(s + 4);
      if constexpr (s + 3 < 10) ldB(s + 3);
      else if constexpr (s + 3 < 14) {
#pragma unroll
        for (int n = 0; n < 5; n++) B[s % 3][n] = *(LAS v4u*)(bl + (s + 3 - 10) * 5120 + n * 1024);
      }
      SB;
    });
    put(acc, RING_OFF + hw * 20480, RING_OFF + hw * 20480);
  } else {
    if (!isffn) {
      __builtin_amdgcn_s_setprio(3);
      const int row = w * 32 + (lane & 31);
      const int pos = grp * 128 + row;
#pragma nounroll
      for (int hp = 0; hp < 2; hp++) {
        LAS char* qp = smem + (row + 16) * IMG_LD + (hp * 2 + (lane >> 5)) * 40;
        unsigned qv[10];
#pragma unroll
        for (int i = 0; i < 5; i++) { const v2u t = *(LAS v2u*)(qp + i * 8); qv[2 * i] = t.x; qv[2 * i + 1] = t.y; }
        float lg[8];
#pragma unroll
        for (int k = 0; k < 8; k++) {
          LAS char* kp = qp + 160 - 2 * k * IMG_LD;
          float a0 = 0, a1 = 0;
#pragma unroll
          for (int i = 0; i < 5; i++) { const v2u t = *(LAS v2u*)(kp + i * 8); a0 = dot2(qv[2 * i], t.x, a0); a1 = dot2(qv[2 * i + 1], t.y, a1); }
          lg[k] = pos - 2 * k >= 0 ? a0 + a1 : -4e9f;
        }
        float mx = lg[0];
#pragma unroll
        for (int k = 1; k < 8; k++) mx = fmaxf(mx, lg[k]);
        float sum = 0;
        constexpr float C = 0.22360679774997896f * 1.4426950408889634f;
#pragma unroll
        for (int k = 0; k < 8; k++) { lg[k] = __builtin_amdgcn_exp2f((lg[k] - mx) * C); sum += lg[k]; }
        const float inv = __builtin_amdgcn_rcpf(sum);
        float o[20];
#pragma unroll
        for (int d = 0; d < 20; d++) o[d] = 0;
#pragma unroll
        for (int k = 0; k < 8; k++) {
          const unsigned plo = pk2(lg[k] * inv, 0.f), phi = plo << 16;
          LAS char* vp = qp + 320 - 2 * k * IMG_LD;
#pragma unroll
          for (int i = 0; i < 5; i++) {
            const v2u t = *(LAS v2u*)(vp + i * 8);
            o[4 * i] = dot2(t.x, plo, o[4 * i]); o[4 * i + 1] = dot2(t.x, phi, o[4 * i + 1]);
            o[4 * i + 2] = dot2(t.y, plo, o[4 * i + 2]); o[4 * i + 3] = dot2(t.y, phi, o[4 * i + 3]);
          }
        }
#pragma unroll
        for (int i = 0; i < 5; i++) { v2u t; t.x = pk2(o[4 * i], o[4 * i + 1]); t.y = pk2(o[4 * i + 2], o[4 * i + 3]); *(LAS v2u*)(qp + i * 8) = t; }
      }
      asm volatile("s_waitcnt lgkmcnt(0)" ::: "memory");
      __builtin_amdgcn_s_setprio(0);
      STAMP(11);
#pragma unroll
      for (int t = 0; t < 5; t++) {
        LAS char* sp = smem + (16 + w * 32 + r) * IMG_LD + t * 32 + g * 8;
        const v2u lo = *(LAS v2u*)sp, hi = *(LAS v2u*)(sp + 16 * IMG_LD);
        v4u v; v.x = lo.x; v.y = lo.y; v.z = hi.x; v.w = hi.y;
        st128_sc1(c.rsH, lane * 16, ((grp * 4 + w) * NCT + sub * 5 + t) * 1024, v);
      }
      drain();
      st32_sc1(c.rsC, lane == 0 ? FLAG_OFF + grp * 512 + (mem * 8 + w) * 4 : OOB, c.tag);
    }
    STAMP(4);
    v4u bo[5][5];
#pragma unroll
    for (int s = 0; s < 5; s++) ldB5(bo[s], c.rsT, lane * 16, (5 * w + s) * 5120);
    ldx();
    {
      const int m0 = (w >> 1) * 8 + (w & 1) * 2;
      u64 need = (1ull << (m0 * 4 + rh)) | (1ull << (m0 * 4 + 4 + rh));
      need |= (isffn ? 15ull : 3ull) << (mem * 4);
      wait_flags(c, need, need);
    }
    STAMP(5);
#pragma unroll
    for (int s = 0; s < 5; s++) TIE5(bo[s]);
    f32x4 acc[4][5];
#pragma unroll
    for (int m = 0; m < 4; m++)
#pragma unroll
      for (int n = 0; n < 5; n++) acc[m][n] = f32x4{0, 0, 0, 0};
    const int hso = hrow + 10 * w * 1024;
    const int aoff = w * 20480, aoff2 = w == 3 ? CS_OFF - 10240 : aoff;
#pragma unroll
    for (int b = 0; b < 20; b++) dma16_sc1(c.rsH, lds0 + (b < 10 ? aoff : aoff2) + b * 1024, lane * 16, hso + (b >> 2) * 2048 + (b & 1) * 1024 + ((b >> 1) & 1) * NCT * 1024);
    static_for<5>([&](auto S) {
      constexpr int s = decltype(S)::value;
      asm volatile("s_waitcnt vmcnt(%0)" ::"i"(16 - 4 * s) : "memory");
      v4u A[4];
#pragma unroll
      for (int i = 0; i < 4; i++) A[i] = *(LAS v4u*)(smem + (4 * s + i < 10 ? aoff : aoff2) + (4 * s + i) * 1024 + lane * 16);
      mma20(acc, bo[s], A);
      SB;
    });
    put(acc, aoff, aoff2);
  }
  asm volatile("" : "+v"(xr[0]), "+v"(xr[1]), "+v"(xr[2]));
  STAMP(12);
  ldsbar();
  STAMP(13);
  if (w == 0 && mem == 0) { v2u t; t.x = (unsigned)tag64; t.y = (unsigned)(tag64 >> 32); st64_sc1(c.rsC, lane == 0 ? EPOCH_OFF + grp * 256 : OOB, t); }
  if (w == 0 && grp == 0 && isffn) { v2u t; t.x = (unsigned)ttag64; t.y = (unsigned)(ttag64 >> 32); st64_sc1(c.rsC, lane == 0 ? TEPOCH_OFF + sub * 256 : OOB, t); }
#pragma unroll
  for (int i = 0; i < 3; i++) {
    const int tl = w + 8 * i;
    if (tl < 20) {
      const int m = tl / 5, n = tl - 5 * m;
      LAS char* pp = smem + tl * 1024 + lane * 16;
      const f32x4 s = (*(LAS f32x4*)pp + *(LAS f32x4*)(pp + 20480)) + (*(LAS f32x4*)(pp + 2 * 20480) + *(LAS f32x4*)(pp + (tl < 10 ? 3 * 20480 : CS_OFF - 10240)));
      const f32x4 f = (*(LAS f32x4*)(pp + RING_OFF) + *(LAS f32x4*)(pp + RING_OFF + 20480)) + (*(LAS f32x4*)(pp + RING_OFF + 2 * 20480) + *(LAS f32x4*)(pp + RING_OFF + 3 * 20480));
      float y[4];
#pragma unroll
      for (int j = 0; j < 4; j++) {
        const float xv = (j & 1) ? bfhi(xr[i][j >> 1]) : bflo(xr[i][j >> 1]);
        y[j] = xv + rbf(rbf(s[j]) * rbf(silu_f(rbf(f[j]))));
      }
      v2u o; o.x = pk2(y[0], y[1]); o.y = pk2(y[2], y[3]);
      *(GAS v2u*)((GAS char*)out + (size_t)(grp * 128 + rh * 64 + m * 16 + r) * (DM * 2) + (c3 * 80 + n * 16 + g * 4) * 2) = o;
    }
  }
  STAMP(7);
  asm volatile(".Lcode_end_mark:");
}
}

extern "C" void kernel_launch(void* const* d_in, const int* in_sizes, int n_in, void* d_out, int out_size, void* d_ws, size_t ws_size,
                              hipStream_t stream) {
  (void)hipFuncSetAttribute((const void*)fused_kernel, hipFuncAttributeMaxDynamicSharedMemorySize, LDS_BYTES);
  fused_kernel<<<256, 512, LDS_BYTES, stream>>>((const u16*)d_in[0], (const u16*)d_in[1], (const u16*)d_in[2], (const u16*)d_in[4],
                                                (const u16*)d_in[5], (u16*)d_out, (char*)d_ws);
}
```

```cpp
#include <hip/hip_runtime.h>
#include <stdint.h>
#include <type_traits>
#include <utility>

#define GAS __attribute__((address_space(1)))
#define LAS __attribute__((address_space(3)))
typedef unsigned short u16;
typedef unsigned long long u64;
typedef __bf16 v2bf __attribute__((ext_vector_type(2)));
typedef __bf16 v4bf __attribute__((ext_vector_type(4)));
typedef __bf16 v8bf __attribute__((ext_vector_type(8)));
typedef float f32x2 __attribute__((ext_vector_type(2)));
typedef float f32x4 __attribute__((ext_vector_type(4)));
typedef unsigned v4u __attribute__((ext_vector_type(4)));
typedef unsigned v2u __attribute__((ext_vector_type(2)));
typedef int v4i __attribute__((ext_vector_type(4)));

namespace {
constexpr int SEQ = 2048, DM = 640, FF = 1728;
constexpr int NCT = 148;
constexpr int H_BYTES = 64 * NCT * 1024;
constexpr size_t CTRL_OFF = 16u << 20;
constexpr int EPOCH_OFF = 0, RDY_OFF = 2048, FLAG_OFF = 8192, STAT_OFF = 65536;
constexpr size_t BT_OFF = 12u << 20;
constexpr int BT_SLICE = 74 * 5120;
constexpr int A_BYTES = 144 * 128, B_BYTES = 64 * 512, ST_BYTES = A_BYTES + B_BYTES;
constexpr int CS_OFF = 3 * ST_BYTES, RS_OFF = CS_OFF + 1024;
constexpr int RDYL_OFF = CS_OFF + 6144;
constexpr int DUMP_OFF = CS_OFF + 4096;
constexpr int IMG_LD = 488;
constexpr int RING_OFF = 71680;
constexpr int LDS_BYTES = 163840;
constexpr int OOB = 0x7FF00000;
constexpr unsigned SPIN_CAP = 1u << 16;
#define MAP3 1
#define POLL_SLEEP 8
#define STAMP(i)
#define SB __builtin_amdgcn_sched_barrier(0)
#define LDPOL2 " sc1"

__device__ __forceinline__ v4i mkrs(const void* p, int bytes) {
  u64 a = (u64)p; v4i r; r.x = (int)(unsigned)a; r.y = (int)((unsigned)(a >> 32) & 0xFFFFu); r.z = bytes; r.w = 0x00020000; return r;
}
__device__ __forceinline__ void dma16(v4i rs, unsigned lds, int voff, int soff) {
  asm volatile("s_mov_b32 m0, %0\n\ts_nop 0\n\tbuffer_load_dwordx4 %1, %2, %3 offen lds" ::"s"(lds), "v"(voff), "s"(rs), "s"(soff));
}
__device__ __forceinline__ void dma16_sc1(v4i rs, unsigned lds, int voff, int soff) {
  asm volatile("s_mov_b32 m0, %0\n\ts_nop 0\n\tbuffer_load_dwordx4 %1, %2, %3 offen" LDPOL2 " lds" ::"s"(lds), "v"(voff), "s"(rs), "s"(soff));
}
__device__ __forceinline__ void dma16_50(v4i rs, unsigned lds, int voff, int soff) {
  asm volatile("s_mov_b32 m0, %0\n\ts_mov_b32 exec_hi, 0x3ffff\n\tbuffer_load_dwordx4 %1, %2, %3 offen lds\n\ts_mov_b32 exec_hi, -1" ::"s"(lds), "v"(voff), "s"(rs), "s"(soff));
}
#define LDPOL " sc1"
#define STPOL " sc1"
template <int IMM> __device__ __forceinline__ v4u ld128_sc1(v4i rs, int voff, int soff) {
  v4u r; asm volatile("buffer_load_dwordx4 %0, %1, %2, %3 offen offset:%4" LDPOL : "=v"(r) : "v"(voff), "s"(rs), "s"(soff), "i"(IMM)); return r;
}
template <int IMM> __device__ __forceinline__ v4u ld128(v4i rs, int voff, int soff) {
  v4u r; asm volatile("buffer_load_dwordx4 %0, %1, %2, %3 offen offset:%4" : "=v"(r) : "v"(voff), "s"(rs), "s"(soff), "i"(IMM)); return r;
}
template <int IMM> __device__ __forceinline__ unsigned ld16(v4i rs, int voff, int soff) {
  unsigned r; asm volatile("buffer_load_ushort %0, %1, %2, %3 offen offset:%4" : "=v"(r) : "v"(voff), "s"(rs), "s"(soff), "i"(IMM)); return r;
}
__device__ __forceinline__ v2u ld64(v4i rs, int voff, int soff) {
  v2u r; asm volatile("buffer_load_dwordx2 %0, %1, %2, %3 offen" : "=v"(r) : "v"(voff), "s"(rs), "s"(soff)); return r;
}
__device__ __forceinline__ v2u ld64_sc1(v4i rs, int voff, int soff) {
  v2u r; asm volatile("buffer_load_dwordx2 %0, %1, %2, %3 offen sc1" : "=v"(r) : "v"(voff), "s"(rs), "s"(soff)); return r;
}
__device__ __forceinline__ v2u ld64_sc1_wait(v4i rs, int voff) {
  v2u r; asm volatile("buffer_load_dwordx2 %0, %1, %2, 0 offen sc1\n\ts_waitcnt vmcnt(0)" : "=&v"(r) : "v"(voff), "s"(rs) : "memory"); return r;
}
__device__ __forceinline__ void st128_sc1(v4i rs, int voff, int soff, v4u v) {
  asm volatile("buffer_store_dwordx4 %0, %1, %2, %3 offen" STPOL "\n\ts_nop 1" ::"v"(v), "v"(voff), "s"(rs), "s"(soff) : "memory");
}
__device__ __forceinline__ void st64_sc1(v4i rs, int voff, v2u v) {
  asm volatile("buffer_store_dwordx2 %0, %1, %2, 0 offen sc1" ::"v"(v), "v"(voff), "s"(rs) : "memory");
}
__device__ __forceinline__ void st32_sc1(v4i rs, int voff, unsigned v) {
  asm volatile("buffer_store_dword %0, %1, %2, 0 offen sc1" ::"v"(v), "v"(voff), "s"(rs) : "memory");
}
template <int N> __device__ __forceinline__ void waitbar() { asm volatile("s_waitcnt vmcnt(%0) lgkmcnt(0)\n\ts_barrier" ::"i"(N) : "memory"); }
__device__ __forceinline__ void ldsbar() { asm volatile("s_waitcnt lgkmcnt(0)\n\ts_barrier" ::: "memory"); }
__device__ __forceinline__ void drain() { asm volatile("s_waitcnt vmcnt(0)" ::: "memory"); }
__device__ __forceinline__ float bflo(unsigned u) { return __builtin_bit_cast(float, u << 16); }
__device__ __forceinline__ float bfhi(unsigned u) { return __builtin_bit_cast(float, u & 0xFFFF0000u); }
__device__ __forceinline__ unsigned pk2(float a, float b) {
  v2bf v; v.x = (__bf16)a; v.y = (__bf16)b; return __builtin_bit_cast(unsigned, v);
}
__device__ __forceinline__ float rbf(float a) { return (float)(__bf16)a; }
__device__ __forceinline__ float dot2(unsigned a, unsigned b, float c) {
  return __builtin_amdgcn_fdot2_f32_bf16(__builtin_bit_cast(v2bf, a), __builtin_bit_cast(v2bf, b), c, false);
}
__device__ __forceinline__ f32x4 mfma(v8bf a, v8bf b, f32x4 c) { return __builtin_amdgcn_mfma_f32_16x16x32_bf16(a, b, c, 0, 0, 0); }
__device__ __forceinline__ v8bf trfrag(LAS char* p, int second) {
  v4bf lo = __builtin_amdgcn_ds_read_tr16_b64_v4bf16((LAS v4bf*)p);
  v4bf hi = __builtin_amdgcn_ds_read_tr16_b64_v4bf16((LAS v4bf*)(p + second));
  return __builtin_shufflevector(lo, hi, 0, 1, 2, 3, 4, 5, 6, 7);
}
__device__ __forceinline__ float row8_sum(float v) {
  v += __builtin_bit_cast(float, __builtin_amdgcn_update_dpp(0, __builtin_bit_cast(int, v), 0xB1, 0xF, 0xF, false));
  v += __builtin_bit_cast(float, __builtin_amdgcn_update_dpp(0, __builtin_bit_cast(int, v), 0x4E, 0xF, 0xF, false));
  v += __builtin_bit_cast(float, __builtin_amdgcn_update_dpp(0, __builtin_bit_cast(int, v), 0x141, 0xF, 0xF, false));
  return v;
}
__device__ __forceinline__ float silu_f(float y) { return y * __builtin_amdgcn_rcpf(1.0f + __builtin_amdgcn_exp2f(y * -1.4426950408889634f)); }
template <class F, int... I> __device__ __forceinline__ void static_for_impl(F&& f, std::integer_sequence<int, I...>) { (f(std::integral_constant<int, I>{}), ...); }
template <int N, class F> __device__ __forceinline__ void static_for(F&& f) { static_for_impl(f, std::make_integer_sequence<int, N>{}); }

struct P1 {
  v4i rsA, rsB, rsH, rsC, rsT, rsPf;
  int avoff, asvoff, bvoff, ldb2;
  int pfso, bid;
  int w, wm, lane, grp, mem, rot;
  const char* bt;
  int tstart, ftile0;
  int isffn, htile0;
  unsigned tag;
};
__device__ __forceinline__ int k0_of(int w) { return 9 * w + (w >= 1) + (w >= 2); }

__device__ __forceinline__ void p1_issue(const P1& c, unsigned lds0, int st, int slot) {
  const unsigned base = lds0 + slot * ST_BYTES;
  if (!c.isffn && c.w < 2) dma16(c.rsA, base + c.w * 1024, c.asvoff, st * 128);
#pragma unroll
  for (int i = 0; i < 2; i++) dma16(c.rsA, base + (2 + c.w + 8 * i) * 1024, c.avoff, st * 128 + i * 64 * (DM * 2));
#pragma unroll
  for (int i = 0; i < 4; i++) dma16(c.rsB, base + A_BYTES + (c.w + 8 * i) * 1024, c.bvoff, (st * 64 + i * 16) * c.ldb2);
}
template <int B0, int B1> __device__ __forceinline__ void tail_issue(const P1& c, unsigned lds0) {
  int w = c.w;
  asm volatile("" : "+s"(w));
#pragma unroll
  for (int i = 0; i < (B1 - B0 + 7) / 8; i++) {
    int b = B0 + w + 8 * i; b = b < B1 ? b : B1 - 1;
    const int wv = b / 10;
    dma16_sc1(c.rsT, lds0 + RING_OFF + b * 1024, c.lane * 16, (k0_of(wv) + 4) * 5120 + (b - 10 * wv) * 1024);
  }
}

template <int NT, int NOWN, int NFOR>
__device__ __forceinline__ void phase1(P1& c, LAS char* smem, unsigned lds0, char* ws) {
  constexpr int NB = NT + NFOR, NX = NOWN + NFOR;
  const int lane = c.lane, r = lane & 15, g = lane >> 4, q = r >> 2, p = lane & 3;
  int aad[2];
#pragma unroll
  for (int ks = 0; ks < 2; ks++) aad[ks] = r * 128 + (((4 * ks + g) ^ (r >> 1)) << 4);
  const int amain = (1 + c.wm * 4) * 2048;
  const int z = q | ((g & 1) << 2);
  int tile[NB], bad[NB];
#pragma unroll
  for (int n = 0; n < NB; n++) {
    if (n < NT) { int t = n + 2 * c.wm; t = t >= NT ? t - NT : t; tile[n] = c.tstart + t; }
    else tile[n] = c.ftile0 + 2 * (n - NT);
    bad[n] = A_BYTES + (8 * g + q) * 512 + (((tile[n] & 7) ^ z) << 5) + (tile[n] >> 3) * 256 + p * 8;
  }
  f32x4 acc[4][NT], accx[NX];
#pragma unroll
  for (int m = 0; m < 4; m++)
#pragma unroll
    for (int n = 0; n < NT; n++) acc[m][n] = f32x4{0, 0, 0, 0};
#pragma unroll
  for (int n = 0; n < NX; n++) accx[n] = f32x4{0, 0, 0, 0};
  v8bf af[2][4], as[2], bf[2][NB];
  const bool ones = (r == 0) || c.isffn;
  auto ldfr = [&](int buf, int slot, int ks) {
    LAS char* base = smem + slot * ST_BYTES;
#pragma unroll
    for (int m = 0; m < 4; m++) af[buf][m] = *(LAS v8bf*)(base + aad[ks] + amain + m * 2048);
#pragma unroll
    for (int n = 0; n < NB; n++) bf[buf][n] = trfrag(base + bad[n] + ks * 16384, 2048);
    as[buf] = *(LAS v8bf*)(base + aad[ks]);
  };
  auto mma = [&](int buf) {
#pragma unroll
    for (int m = 0; m < 4; m++)
#pragma unroll
      for (int n = 0; n < NT; n++) acc[m][n] = mfma(bf[buf][n], af[buf][m], acc[m][n]);
    v4u s = __builtin_bit_cast(v4u, as[buf]);
#pragma unroll
    for (int e = 0; e < 4; e++) s[e] = ones ? 0x3F803F80u : s[e];
    v8bf sv = __builtin_bit_cast(v8bf, s);
#pragma unroll
    for (int n = 0; n < NX; n++) accx[n] = mfma(bf[buf][n < NOWN ? n : NT + n - NOWN], sv, accx[n]);
#pragma unroll
    for (int i = 0; i < 4 * NT + NX; i++) {
      __builtin_amdgcn_sched_group_barrier(0x8, 1, 0); __builtin_amdgcn_sched_group_barrier(0x100, 1, 0); __builtin_amdgcn_sched_group_barrier(0x2, 1, 0);
    }
    SB;
  };
  waitbar<12>();
  STAMP(1);
  unsigned pfd = 0;
  auto touch = [&](v4i rs, int so) { asm volatile("buffer_load_dword %0, %1, %2, %3 offen" : "+v"(pfd) : "v"(lane * 128), "s"(rs), "s"(so)); };
  auto issue = [&](int st, int slot) {
    if (st < 7) {
      if (st == 4 && c.w == 0) dma16_sc1(c.rsC, lds0 + RDYL_OFF, lane * 16, RDY_OFF);
      p1_issue(c, lds0, st + 3, slot);
      if (st == 0) touch(c.rsPf, c.pfso);
      if (st == 2) st32_sc1(c.rsC, (c.w == 0 && lane == 0) ? RDY_OFF + c.bid * 4 : OOB, c.tag);
      if (st == 5) {
        v4u rdy = *(LAS v4u*)(smem + RDYL_OFF + lane * 16);
        for (unsigned spins = 0;; spins++) {
          if (__all(rdy.x == c.tag && rdy.y == c.tag && rdy.z == c.tag && rdy.w == c.tag) || spins > SPIN_CAP) break;
          __builtin_amdgcn_s_sleep(POLL_SLEEP);
          asm volatile("buffer_load_dwordx4 %0, %1, %2, 0 offen offset:%3 sc1\n\ts_waitcnt vmcnt(0)" : "=&v"(rdy) : "v"(lane * 16), "s"(c.rsC), "i"(RDY_OFF) : "memory");
        }
      }
    } else if (st == 7) tail_issue<0, 30>(c, lds0);
    else tail_issue<30, 80>(c, lds0);
  };
  ldfr(0, 0, 0);
  int slot = 0;
#pragma nounroll
  for (int st = 0; st < 9; st++) {
    ldfr(1, slot, 1);
    mma(0);
    const int nslot = slot == 2 ? 0 : slot + 1;
    if (st < 8) waitbar<6>(); else waitbar<4>();
    SB;
    if (c.w < 4) issue(st, slot);
    SB;
    ldfr(0, nslot, 0);
    mma(1);
    if (c.w >= 4) issue(st, slot);
    SB;
    slot = nslot;
  }
  ldfr(1, 0, 1);
  mma(0);
  mma(1);
  STAMP(2);
#pragma unroll
  for (int n = 0; n < NX; n++)
    *(LAS f32x4*)(smem + (r == 0 ? CS_OFF + (tile[n < NOWN ? n : NT + n - NOWN] * 16 + 4 * g) * 4 : DUMP_OFF + lane * 16)) = accx[n];
  if (c.w < 3) {
    const int t = c.w * 64 + lane, tt = t < 144 ? t : 143;
    v2u gr;
    for (unsigned spins = 0;; spins++) {
      gr = ld64_sc1_wait(c.rsC, STAT_OFF + c.grp * 2048 + tt * 8);
      if (__all(gr.y == c.tag) || spins > SPIN_CAP) break;
      __builtin_amdgcn_s_sleep(1);
    }
    f32x2 ms; ms.x = bflo(gr.x); ms.y = bfhi(gr.x);
    *(LAS f32x2*)(smem + RS_OFF + tt * 8) = ms;
  }
  waitbar<0>();
  asm volatile("" ::"v"(pfd));
  STAMP(8);
  f32x2 rs[4];
#pragma unroll
  for (int m = 0; m < 4; m++) rs[m] = *(LAS f32x2*)(smem + RS_OFF + (16 + c.wm * 64 + m * 16 + r) * 8);
  if (c.isffn) {
    float c1[4];
#pragma unroll
    for (int m = 0; m < 4; m++) c1[m] = __builtin_amdgcn_rcpf(rs[m].y) * -1.4426950408889634f;
#pragma unroll
    for (int n = 0; n < NT; n++) {
      const f32x4 cs = *(LAS f32x4*)(smem + CS_OFF + (tile[n] * 16 + 4 * g) * 4);
#pragma unroll
      for (int mp = 0; mp < 2; mp++) {
        v4u o;
#pragma unroll
        for (int h = 0; h < 2; h++) {
          const int m = 2 * mp + h;
          const f32x4 t = acc[m][n] - rs[m].x * cs;
          const f32x4 zz = t * c1[m];
          f32x4 e;
#pragma unroll
          for (int j = 0; j < 4; j++) e[j] = __builtin_amdgcn_exp2f(zz[j]);
          const f32x4 d = e * rs[m].y + rs[m].y;
          f32x4 rr;
#pragma unroll
          for (int j = 0; j < 4; j++) rr[j] = __builtin_amdgcn_rcpf(d[j]);
          const f32x4 y = t * rr;
          o[2 * h] = pk2(y[0], y[1]); o[2 * h + 1] = pk2(y[2], y[3]);
        }
        st128_sc1(c.rsH, lane * 16, ((c.grp * 4 + c.wm * 2 + mp) * NCT + c.htile0 + tile[n]) * 1024, o);
      }
    }
  } else {
    float ri[4];
#pragma unroll
    for (int m = 0; m < 4; m++) ri[m] = __builtin_amdgcn_rcpf(rs[m].y);
#pragma unroll
    for (int n = 0; n < NT; n++) {
      const f32x4 cs = *(LAS f32x4*)(smem + CS_OFF + (tile[n] * 16 + 4 * g) * 4);
#pragma unroll
      for (int m = 0; m < 4; m++) {
        const f32x4 y = (acc[m][n] - rs[m].x * cs) * ri[m];
        v2u o; o.x = pk2(y[0], y[1]); o.y = pk2(y[2], y[3]);
        *(LAS v2u*)(smem + (16 + c.wm * 64 + m * 16 + r) * IMG_LD + tile[n] * 32 + g * 8) = o;
      }
    }
    const f32x2 rh = *(LAS f32x2*)(smem + RS_OFF + r * 8);
    const float rih = __builtin_amdgcn_rcpf(rh.y);
#pragma unroll
    for (int n = 0; n < NX; n++) {
      const int t = tile[n < NOWN ? n : NT + n - NOWN];
      const f32x4 cs = *(LAS f32x4*)(smem + CS_OFF + (t * 16 + 4 * g) * 4);
      const f32x4 y = (accx[n] - rh.x * cs) * rih;
      v2u o; o.x = pk2(y[0], y[1]); o.y = pk2(y[2], y[3]);
      *(LAS v2u*)(smem + r * IMG_LD + t * 32 + g * 8) = o;
    }
    ldsbar();
  }
}

__device__ __forceinline__ void wait_flags(const P1& c, int off) {
  for (unsigned spins = 0;; spins++) {
    unsigned f;
    asm volatile("buffer_load_dword %0, %1, %2, 0 offen sc1\n\ts_waitcnt vmcnt(0)" : "=&v"(f) : "v"(off), "s"(c.rsC) : "memory");
    if (__all(off == OOB || f == c.tag) || spins > SPIN_CAP) break;
    __builtin_amdgcn_s_sleep(POLL_SLEEP);
  }
}
__device__ __forceinline__ void mma20(f32x4 (&acc)[4][5], const v4u (&b)[5], const v4u (&A)[4]) {
  v4u a[4];
#pragma unroll
  for (int h = 0; h < 2; h++) {
    const v4u l0 = A[2 * h], l1 = A[2 * h + 1];
    a[2 * h] = __builtin_shufflevector(l0, l1, 0, 1, 4, 5);
    a[2 * h + 1] = __builtin_shufflevector(l0, l1, 2, 3, 6, 7);
  }
  asm volatile("s_nop 1\n\t"
               "v_mfma_f32_16x16x32_bf16 %0, %20, %25, %0\n\t"
               "v_mfma_f32_16x16x32_bf16 %1, %21, %25, %1\n\t"
               "v_mfma_f32_16x16x32_bf16 %2, %22, %25, %2\n\t"
               "v_mfma_f32_16x16x32_bf16 %3, %23, %25, %3\n\t"
               "v_mfma_f32_16x16x32_bf16 %4, %24, %25, %4\n\t"
               "v_mfma_f32_16x16x32_bf16 %5, %20, %26, %5\n\t"
               "v_mfma_f32_16x16x32_bf16 %6, %21, %26, %6\n\t"
               "v_mfma_f32_16x16x32_bf16 %7, %22, %26, %7\n\t"
               "v_mfma_f32_16x16x32_bf16 %8, %23, %26, %8\n\t"
               "v_mfma_f32_16x16x32_bf16 %9, %24, %26, %9\n\t"
               "v_mfma_f32_16x16x32_bf16 %10, %20, %27, %10\n\t"
               "v_mfma_f32_16x16x32_bf16 %11, %21, %27, %11\n\t"
               "v_mfma_f32_16x16x32_bf16 %12, %22, %27, %12\n\t"
               "v_mfma_f32_16x16x32_bf16 %13, %23, %27, %13\n\t"
               "v_mfma_f32_16x16x32_bf16 %14, %24, %27, %14\n\t"
               "v_mfma_f32_16x16x32_bf16 %15, %20, %28, %15\n\t"
               "v_mfma_f32_16x16x32_bf16 %16, %21, %28, %16\n\t"
               "v_mfma_f32_16x16x32_bf16 %17, %22, %28, %17\n\t"
               "v_mfma_f32_16x16x32_bf16 %18, %23, %28, %18\n\t"
               "v_mfma_f32_16x16x32_bf16 %19, %24, %28, %19\n\t"
               : "+v"(acc[0][0]), "+v"(acc[0][1]), "+v"(acc[0][2]), "+v"(acc[0][3]), "+v"(acc[0][4]), "+v"(acc[1][0]), "+v"(acc[1][1]), "+v"(acc[1][2]), "+v"(acc[1][3]), "+v"(acc[1][4]), "+v"(acc[2][0]), "+v"(acc[2][1]), "+v"(acc[2][2]), "+v"(acc[2][3]), "+v"(acc[2][4]), "+v"(acc[3][0]), "+v"(acc[3][1]), "+v"(acc[3][2]), "+v"(acc[3][3]), "+v"(acc[3][4])
               : "v"(b[0]), "v"(b[1]), "v"(b[2]), "v"(b[3]), "v"(b[4]), "v"(a[0]), "v"(a[1]), "v"(a[2]), "v"(a[3]));
}
__device__ __forceinline__ void ldA4(v4u (&A)[4], v4i rsH, int voff, int so) {
  const int so2 = so == OOB ? OOB : so + NCT * 1024;
  A[0] = ld128_sc1<0>(rsH, voff, so); A[1] = ld128_sc1<1024>(rsH, voff, so);
  A[2] = ld128_sc1<0>(rsH, voff, so2); A[3] = ld128_sc1<1024>(rsH, voff, so2);
}
#define WAITA(A, N) asm volatile("s_waitcnt vmcnt(%4) lgkmcnt(0)" : "+v"((A)[0]), "+v"((A)[1]), "+v"((A)[2]), "+v"((A)[3]) : "i"(N) : "memory")
constexpr int p3wait(int s) {
  auto cnt = [](int t) { return (t <= 5 ? 4 : 0) + ((t == 4 || t == 5) ? 5 : 0); };
  int n = s < 4 ? 4 * (3 - s) : 0;
  for (int t = (s < 4 ? 0 : s - 3); t < s; t++) n += cnt(t);
  return n;
}
__device__ __forceinline__ void ldB5(v4u (&B)[5], v4i rsT, int voff, int so) {
  const int so2 = so == OOB ? OOB : so + 4096;
  B[0] = ld128_sc1<0>(rsT, voff, so); B[1] = ld128_sc1<1024>(rsT, voff, so); B[2] = ld128_sc1<2048>(rsT, voff, so);
  B[3] = ld128_sc1<3072>(rsT, voff, so); B[4] = ld128_sc1<0>(rsT, voff, so2);
}
#define TIE5(B) asm volatile("" : "+v"((B)[0]), "+v"((B)[1]), "+v"((B)[2]), "+v"((B)[3]), "+v"((B)[4]))

__global__ __launch_bounds__(512, 2) void fused_kernel(const u16* __restrict__ x, const u16* __restrict__ wqkv, const u16* __restrict__ wo,
                                                       const u16* __restrict__ w1, const u16* __restrict__ w2, u16* __restrict__ out,
                                                       char* __restrict__ ws) {
  extern __shared__ __attribute__((aligned(16))) char smem_[];
  LAS char* smem = (LAS char*)smem_;
  const unsigned lds0 = (unsigned)(size_t)smem;
  const int tid = threadIdx.x, lane = tid & 63, w = __builtin_amdgcn_readfirstlane(tid >> 6);
  const int bid = blockIdx.x, xcd = bid & 7, idx = bid >> 3, half = xcd & 1, j8 = idx & 7;
  const int grp = (xcd >> 1) * 4 + (idx >> 3), mem = half * 8 + j8;
  const int isffn = j8 >> 2, sub = half * 4 + (j8 & 3);
  STAMP(0);
  u64 pc0; unsigned codelen, cpf = 0;
  asm volatile("s_getpc_b64 %0\n.Lcode_begin_mark:\n\ts_mov_b32 %1, .Lcode_end_mark-.Lcode_begin_mark" : "=s"(pc0), "=s"(codelen));
  if (bid < 8) {
    const v4i rp = mkrs((const void*)pc0, (int)codelen);
    asm volatile("buffer_load_dword %0, %1, %2, %3 offen" : "=v"(cpf) : "v"(lane * 128), "s"(rp), "s"(w * 8192));
  }
  P1 c;
  c.rsA = mkrs(x, SEQ * DM * 2);
  c.rsB = isffn ? mkrs(w1, DM * FF * 2) : mkrs(wqkv, DM * 3 * DM * 2);
  c.rsH = mkrs(ws, H_BYTES);
  c.rsC = mkrs(ws + CTRL_OFF, 1 << 20);
  const int cg = (xcd >> 1) * 4 + half * 2 + (idx >> 4), rh = (idx >> 3) & 1, c3 = idx & 7;
  c.bt = ws + BT_OFF; c.bid = bid;
  c.rsT = mkrs(ws + BT_OFF + (size_t)c3 * BT_SLICE, BT_SLICE);
  c.ldb2 = isffn ? FF * 2 : 3 * DM * 2;
  c.w = w; c.lane = lane; c.grp = grp; c.isffn = isffn; c.mem = mem;
  c.wm = w >> 2;
  const int wn = w < 4 ? w : 7 - w;
  const int ftile0 = sub < 4 ? sub * 14 : 56 + (sub - 4) * 13;
  const int ntile = isffn ? (sub < 4 ? 14 : 13) : 15;
  c.htile0 = 40 + ftile0;
  v2u ep = ld64_sc1(c.rsC, EPOCH_OFF, 0);
  unsigned pre[40];
  v4u xs[10];
  const int sslot = w * 8 + (lane >> 3);
  const int kap = (7 - w) * 16 + grp;
  const bool dostat = isffn && w < 3, dotile = isffn && w >= 3 && kap < 74;
  if (dostat) {
    int rho = sub * 18 + (sslot < 18 ? sslot : 17);
    int row = grp * 128 - 16 + rho; row = row < 0 ? 0 : row;
    const int vo = row * (DM * 2) + (lane & 7) * 16;
    static_for<10>([&](auto I) { constexpr int i = decltype(I)::value; xs[i] = ld128<128 * i>(c.rsA, vo, 0); });
  } else if (dotile) {
    const v4i rw = mkrs(kap < 20 ? wo : w2, kap < 20 ? DM * DM * 2 : DM * FF * 2);
    int kq = kap;
    if (kap >= 20) asm volatile("s_sub_u32 %0, %0, 20" : "+s"(kq)::"scc");
    const int so = kq * 32 * (DM * 2);
    const int vo = (lane >> 4) * 4 * (DM * 2) + (sub * 80 + (lane & 15)) * 2;
    static_for<40>([&](auto I) { constexpr int i = decltype(I)::value, n = i >> 3, j = i & 7;
      pre[i] = ld16<(j & 3) * (DM * 2) + n * 32>(rw, vo, so + (j >> 2) * 16 * (DM * 2)); });
  }
  {
    const int ar = lane >> 3, sw = ((lane & 7) ^ ((4 * w + (lane >> 4)) & 7)) << 4;
    c.avoff = (grp * 128 + 8 * w + ar) * (DM * 2) + sw;
    int hr = grp * 128 - 16 + 8 * w + ar; hr = hr < 0 ? 0 : hr;
    c.asvoff = hr * (DM * 2) + sw;
    const int kk = 2 * w + (lane >> 5), zz = (kk & 3) | (((kk >> 3) & 1) << 2), pc = lane & 31, seg = (pc >> 1) ^ zz;
    int col;
    if (isffn) col = (ftile0 + seg) * 16;
    else { const int part = seg >= 10 ? 2 : (seg >= 5 ? 1 : 0); col = part * DM + sub * 80 + (seg - 5 * part) * 16; }
    c.bvoff = seg < ntile ? kk * c.ldb2 + col * 2 + (pc & 1) * 16 : OOB;
  }
  p1_issue(c, lds0, 0, 0); p1_issue(c, lds0, 1, 1); p1_issue(c, lds0, 2, 2);
  {
    const int gw = bid * 8 + w;
    if (gw < 300) { c.rsPf = mkrs(wqkv, DM * 3 * DM * 2); c.pfso = gw * 8192; }
    else if (gw < 570) { c.rsPf = mkrs(w1, DM * FF * 2); c.pfso = (gw - 300) * 8192; }
    else { c.rsPf = c.rsA; c.pfso = (gw - 570) * 8192; }
  }
  asm volatile("s_waitcnt vmcnt(18)" : "+v"(ep), "+v"(cpf)::"memory");
  u64 tag64;
  {
    const u64 e = ((u64)(unsigned)__builtin_amdgcn_readfirstlane(ep.y) << 32) | (unsigned)__builtin_amdgcn_readfirstlane(ep.x);
    tag64 = e * 0x5851F42D4C957F2Dull + 0x14057B7EF767814Full;
  }
  c.tag = (unsigned)tag64;
  if (dostat) {
    asm volatile("" : "+v"(xs[0]), "+v"(xs[1]), "+v"(xs[2]), "+v"(xs[3]), "+v"(xs[4]), "+v"(xs[5]), "+v"(xs[6]), "+v"(xs[7]), "+v"(xs[8]), "+v"(xs[9]));
    float s = 0;
#pragma unroll
    for (int i = 0; i < 40; i++) s += bflo(xs[i >> 2][i & 3]) + bfhi(xs[i >> 2][i & 3]);
    const float mean = row8_sum(s) * (1.0f / DM);
    float v = 0;
#pragma unroll
    for (int i = 0; i < 40; i++) { const float a = bflo(xs[i >> 2][i & 3]) - mean, b = bfhi(xs[i >> 2][i & 3]) - mean; v += a * a + b * b; }
    const float var = row8_sum(v) * (1.0f / DM);
    const float sd = __builtin_sqrtf(rbf(rbf(var) + 1e-5f));
    v2u gr; gr.x = pk2(mean, sd); gr.y = c.tag;
    st64_sc1(c.rsC, ((lane & 7) == 0 && sslot < 18) ? STAT_OFF + grp * 2048 + (sub * 18 + sslot) * 8 : OOB, gr);
  } else if (dotile) {
#pragma unroll
    for (int i = 0; i < 40; i += 10)
      asm volatile("" : "+v"(pre[i]), "+v"(pre[i + 1]), "+v"(pre[i + 2]), "+v"(pre[i + 3]), "+v"(pre[i + 4]), "+v"(pre[i + 5]), "+v"(pre[i + 6]), "+v"(pre[i + 7]), "+v"(pre[i + 8]), "+v"(pre[i + 9]));
#pragma unroll
    for (int n = 0; n < 5; n++) {
      v4u v;
#pragma unroll
      for (int e = 0; e < 4; e++) v[e] = pre[8 * n + 2 * e] | (pre[8 * n + 2 * e + 1] << 16);
      st128_sc1(mkrs(ws + BT_OFF, 8 * BT_SLICE), lane * 16, sub * BT_SLICE + (kap * 5 + n) * 1024, v);
    }
  }
  {
    const int n4 = ntile - 12;
    c.tstart = wn <= n4 ? 4 * wn : 4 * n4 + 3 * (wn - n4);
    if (!isffn) c.ftile0 = 1 + 6 * c.wm;
    else { const int fi = (wn - n4) * 2 + c.wm; c.ftile0 = 1 + 2 * (n4 == 2 ? fi : (fi & 1)); }
    if (wn < n4) phase1<4, 1, 0>(c, smem, lds0, ws);
    else if (!isffn) phase1<3, 2, 3>(c, smem, lds0, ws);
    else phase1<3, 2, 1>(c, smem, lds0, ws);
  }
  STAMP(9);
  const int r = lane & 15, g = lane >> 4;
  const int k0 = k0_of(w);
  v2u xr[3];
#pragma unroll
  for (int i = 0; i < 3; i++) {
    const int tl = w + 8 * i, tt = tl < 20 ? tl : 19, m = tt / 5, n = tt - 5 * m;
    xr[i] = ld64(c.rsA, (cg * 128 + rh * 64 + m * 16 + r) * (DM * 2) + (c3 * 80 + n * 16 + g * 4) * 2, 0);
  }
  v4u A[4][4], B[4][5];
#pragma unroll
  for (int s = 0; s < 4; s++) ldB5(B[s], c.rsT, lane * 16, (k0 + s) * 5120);
  if (!isffn) {
    const int row = (w & 1) * 64 + lane;
    const int pos = grp * 128 + row;
    LAS char* qp = smem + (row + 16) * IMG_LD + (w >> 1) * 40;
    unsigned qv[10];
#pragma unroll
    for (int i = 0; i < 5; i++) { const v2u t = *(LAS v2u*)(qp + i * 8); qv[2 * i] = t.x; qv[2 * i + 1] = t.y; }
    float lg[8];
#pragma unroll
    for (int k = 0; k < 8; k++) {
      LAS char* kp = qp + 160 - 2 * k * IMG_LD;
      float a0 = 0, a1 = 0;
#pragma unroll
      for (int i = 0; i < 5; i++) { const v2u t = *(LAS v2u*)(kp + i * 8); a0 = dot2(qv[2 * i], t.x, a0); a1 = dot2(qv[2 * i + 1], t.y, a1); }
      lg[k] = pos - 2 * k >= 0 ? a0 + a1 : -4e9f;
    }
    float mx = lg[0];
#pragma unroll
    for (int k = 1; k < 8; k++) mx = fmaxf(mx, lg[k]);
    float sum = 0;
    constexpr float C = 0.22360679774997896f * 1.4426950408889634f;
#pragma unroll
    for (int k = 0; k < 8; k++) { lg[k] = __builtin_amdgcn_exp2f((lg[k] - mx) * C); sum += lg[k]; }
    const float inv = __builtin_amdgcn_rcpf(sum);
    float o[20];
#pragma unroll
    for (int d = 0; d < 20; d++) o[d] = 0;
#pragma unroll
    for (int k = 0; k < 8; k++) {
      const unsigned plo = pk2(lg[k] * inv, 0.f), phi = plo << 16;
      LAS char* vp = qp + 320 - 2 * k * IMG_LD;
#pragma unroll
      for (int i = 0; i < 5; i++) {
        const v2u t = *(LAS v2u*)(vp + i * 8);
        o[4 * i] = dot2(t.x, plo, o[4 * i]); o[4 * i + 1] = dot2(t.x, phi, o[4 * i + 1]);
        o[4 * i + 2] = dot2(t.y, plo, o[4 * i + 2]); o[4 * i + 3] = dot2(t.y, phi, o[4 * i + 3]);
      }
    }
#pragma unroll
    for (int i = 0; i < 5; i++) { v2u t; t.x = pk2(o[4 * i], o[4 * i + 1]); t.y = pk2(o[4 * i + 2], o[4 * i + 3]); *(LAS v2u*)(qp + i * 8) = t; }
    STAMP(11);
    ldsbar();
#pragma unroll
    for (int i = 0; i < 3; i++) {
      const int b0 = w + 8 * i, b = b0 < 20 ? b0 : 19, rp = b / 5, t = b - 5 * rp;
      LAS char* sp = smem + (16 + rp * 32 + r) * IMG_LD + t * 32 + g * 8;
      const v2u lo = *(LAS v2u*)sp, hi = *(LAS v2u*)(sp + 16 * IMG_LD);
      v4u v; v.x = lo.x; v.y = lo.y; v.z = hi.x; v.w = hi.y;
      st128_sc1(c.rsH, lane * 16, b0 < 20 ? ((grp * 4 + rp) * NCT + sub * 5 + t) * 1024 : OOB, v);
    }
    waitbar<0>();
  } else waitbar<23>();
  st32_sc1(c.rsC, tid == 0 ? FLAG_OFF + grp * 256 + mem * 4 : OOB, c.tag);
  STAMP(4);
  const int e_off = RING_OFF + w * 10240, l_off = w < 7 ? w * 10240 : CS_OFF;
#pragma unroll
  for (int e = 0; e < 10; e++) dma16_sc1(c.rsT, lds0 + l_off + e * 1024, lane * 16, (k0 + 6) * 5120 + e * 1024);
  {
    int m;
    if (w < 2) m = lane < 4 ? w * 8 + lane : -1;
    else {
      const int t0 = 18 * (w - 2), t1 = t0 + 17;
      const int f0 = t0 < 56 ? t0 / 14 : 4 + (t0 - 56) / 13, f1 = t1 < 56 ? t1 / 14 : 4 + (t1 - 56) / 13, f = f0 + lane;
      m = f <= f1 ? (f >> 2) * 8 + 4 + (f & 3) : -1;
    }
    wait_flags(c, m >= 0 ? FLAG_OFF + cg * 256 + m * 4 : OOB);
  }
  STAMP(5);
  const int hso = ((cg * 4 + rh * 2) * NCT + 2 * k0) * 1024;
  auto ldA = [&](int s) { ldA4(A[s & 3], c.rsH, lane * 16, (s < 9 || w < 2) ? hso + s * 2048 : OOB); };
  ldA(0); ldA(1); ldA(2); ldA(3);
  f32x4 acc[4][5];
#pragma unroll
  for (int m = 0; m < 4; m++)
#pragma unroll
    for (int n = 0; n < 5; n++) acc[m][n] = f32x4{0, 0, 0, 0};
  static_for<10>([&](auto S) {
    constexpr int s = decltype(S)::value;
    WAITA(A[s & 3], p3wait(s));
    if constexpr (s < 4 || s >= 8) TIE5(B[s & 3]);
    mma20(acc, B[s & 3], A[s & 3]);
    SB;
    if constexpr (s + 4 < 10) ldA(s + 4);
    if constexpr (s < 4) {
#pragma unroll
      for (int n = 0; n < 5; n++) B[s][n] = *(LAS v4u*)(smem + (s < 2 ? e_off : l_off) + (s & 1) * 5120 + n * 1024 + lane * 16);
    } else if constexpr (s < 6) ldB5(B[s & 3], c.rsT, lane * 16, (s == 4 || w < 2) ? (k0 + s + 4) * 5120 : OOB);
    SB;
  });
  STAMP(6);
  asm volatile("s_nop 7\n\ts_nop 3\n\ts_waitcnt vmcnt(0) lgkmcnt(0)" : "+v"(xr[0]), "+v"(xr[1]), "+v"(xr[2])::"memory");
#pragma unroll
  for (int m = 0; m < 4; m++)
#pragma unroll
    for (int n = 0; n < 5; n++) *(LAS f32x4*)(smem + (m < 2 ? e_off : l_off) + ((m & 1) * 5 + n) * 1024 + lane * 16) = acc[m][n];
  STAMP(12);
  ldsbar();
  STAMP(13);
  if (w == 0 && bid == 0) { v2u t; t.x = (unsigned)tag64; t.y = (unsigned)(tag64 >> 32); st64_sc1(c.rsC, lane == 0 ? EPOCH_OFF : OOB, t); }
#pragma unroll
  for (int i = 0; i < 3; i++) {
    const int tl = w + 8 * i;
    if (tl < 20) {
      const int m = tl / 5, n = tl - 5 * m;
      LAS char* pp = smem + (tl < 10 ? RING_OFF + tl * 1024 : (tl - 10) * 1024) + lane * 16;
      const f32x4 s = *(LAS f32x4*)pp + *(LAS f32x4*)(pp + 10240);
      const f32x4 f = ((*(LAS f32x4*)(pp + 2 * 10240) + *(LAS f32x4*)(pp + 3 * 10240)) + (*(LAS f32x4*)(pp + 4 * 10240) + *(LAS f32x4*)(pp + 5 * 10240))) +
                      (*(LAS f32x4*)(pp + 6 * 10240) + *(LAS f32x4*)(pp + (tl < 10 ? 7 * 10240 : CS_OFF)));
      float y[4];
#pragma unroll
      for (int j = 0; j < 4; j++) {
        const float xv = (j & 1) ? bfhi(xr[i][j >> 1]) : bflo(xr[i][j >> 1]);
        y[j] = xv + rbf(rbf(s[j]) * rbf(silu_f(rbf(f[j]))));
      }
      v2u o; o.x = pk2(y[0], y[1]); o.y = pk2(y[2], y[3]);
      *(GAS v2u*)((GAS char*)out + (size_t)(cg * 128 + rh * 64 + m * 16 + r) * (DM * 2) + (c3 * 80 + n * 16 + g * 4) * 2) = o;
    }
  }
  STAMP(7);
  asm volatile(".Lcode_end_mark:");
}
}

extern "C" void kernel_launch(void* const* d_in, const int* in_sizes, int n_in, void* d_out, int out_size, void* d_ws, size_t ws_size,
                              hipStream_t stream) {
  (void)hipFuncSetAttribute((const void*)fused_kernel, hipFuncAttributeMaxDynamicSharedMemorySize, LDS_BYTES);
  fused_kernel<<<256, 512, LDS_BYTES, stream>>>((const u16*)d_in[0], (const u16*)d_in[1], (const u16*)d_in[2], (const u16*)d_in[4],
                                                (const u16*)d_in[5], (u16*)d_out, (char*)d_ws);
}
```

```cpp
#include <hip/hip_runtime.h>
#include <stdint.h>
#include <type_traits>
#include <utility>

#define GAS __attribute__((address_space(1)))
#define LAS __attribute__((address_space(3)))
typedef unsigned short u16;
typedef unsigned long long u64;
typedef __bf16 v2bf __attribute__((ext_vector_type(2)));
typedef __bf16 v4bf __attribute__((ext_vector_type(4)));
typedef __bf16 v8bf __attribute__((ext_vector_type(8)));
typedef float f32x2 __attribute__((ext_vector_type(2)));
typedef float f32x4 __attribute__((ext_vector_type(4)));
typedef unsigned v4u __attribute__((ext_vector_type(4)));
typedef unsigned v2u __attribute__((ext_vector_type(2)));
typedef int v4i __attribute__((ext_vector_type(4)));

namespace {
constexpr int SEQ = 2048, DM = 640, FF = 1728;
constexpr int NCT = 148;
constexpr int H_BYTES = 64 * NCT * 1024;
constexpr size_t CTRL_OFF = 16u << 20;
constexpr int EPOCH_OFF = 0, RDY_OFF = 2048, FLAG_OFF = 8192, STAT_OFF = 65536;
constexpr size_t BT_OFF = 12u << 20;
constexpr int BT_SLICE = 74 * 5120;
constexpr int A_BYTES = 144 * 128, B_BYTES = 64 * 512, ST_BYTES = A_BYTES + B_BYTES;
constexpr int CS_OFF = 3 * ST_BYTES, RS_OFF = CS_OFF + 1024;
constexpr int RDYL_OFF = CS_OFF + 6144, EPL_OFF = CS_OFF + 7168;
constexpr int DUMP_OFF = CS_OFF + 4096;
constexpr int IMG_LD = 488;
constexpr int RING_OFF = 71680;
constexpr int LDS_BYTES = 163840;
constexpr int OOB = 0x7FF00000;
constexpr unsigned SPIN_CAP = 1u << 16;
#define SGB 1
#define MAP3 1
#define POLL_SLEEP 8
#define STAMP(i)
#define SB __builtin_amdgcn_sched_barrier(0)
#define LDPOL2 " sc1"

__device__ __forceinline__ v4i mkrs(const void* p, int bytes) {
  u64 a = (u64)p; v4i r; r.x = (int)(unsigned)a; r.y = (int)((unsigned)(a >> 32) & 0xFFFFu); r.z = bytes; r.w = 0x00020000; return r;
}
__device__ __forceinline__ void dma16(v4i rs, unsigned lds, int voff, int soff) {
  asm volatile("s_mov_b32 m0, %0\n\ts_nop 0\n\tbuffer_load_dwordx4 %1, %2, %3 offen lds" ::"s"(lds), "v"(voff), "s"(rs), "s"(soff));
}
__device__ __forceinline__ void dma16_sc1(v4i rs, unsigned lds, int voff, int soff) {
  asm volatile("s_mov_b32 m0, %0\n\ts_nop 3\n\tbuffer_load_dwordx4 %1, %2, %3 offen" LDPOL2 " lds" ::"s"(lds), "v"(voff), "s"(rs), "s"(soff));
}
__device__ __forceinline__ void dma16_50(v4i rs, unsigned lds, int voff, int soff) {
  asm volatile("s_mov_b32 m0, %0\n\ts_mov_b32 exec_hi, 0x3ffff\n\tbuffer_load_dwordx4 %1, %2, %3 offen lds\n\ts_mov_b32 exec_hi, -1" ::"s"(lds), "v"(voff), "s"(rs), "s"(soff));
}
#define LDPOL " sc1"
#define STPOL " sc1"
template <int IMM> __device__ __forceinline__ v4u ld128_sc1(v4i rs, int voff, int soff) {
  v4u r; asm volatile("buffer_load_dwordx4 %0, %1, %2, %3 offen offset:%4" LDPOL : "=v"(r) : "v"(voff), "s"(rs), "s"(soff), "i"(IMM)); return r;
}
template <int IMM> __device__ __forceinline__ v4u ld128(v4i rs, int voff, int soff) {
  v4u r; asm volatile("buffer_load_dwordx4 %0, %1, %2, %3 offen offset:%4" : "=v"(r) : "v"(voff), "s"(rs), "s"(soff), "i"(IMM)); return r;
}
template <int IMM> __device__ __forceinline__ unsigned ld16(v4i rs, int voff, int soff) {
  unsigned r; asm volatile("buffer_load_ushort %0, %1, %2, %3 offen offset:%4" : "=v"(r) : "v"(voff), "s"(rs), "s"(soff), "i"(IMM)); return r;
}
__device__ __forceinline__ v2u ld64(v4i rs, int voff, int soff) {
  v2u r; asm volatile("buffer_load_dwordx2 %0, %1, %2, %3 offen" : "=v"(r) : "v"(voff), "s"(rs), "s"(soff)); return r;
}
__device__ __forceinline__ v2u ld64_sc1(v4i rs, int voff, int soff) {
  v2u r; asm volatile("buffer_load_dwordx2 %0, %1, %2, %3 offen sc1" : "=v"(r) : "v"(voff), "s"(rs), "s"(soff)); return r;
}
__device__ __forceinline__ v2u ld64_sc1_wait(v4i rs, int voff) {
  v2u r; asm volatile("buffer_load_dwordx2 %0, %1, %2, 0 offen sc1\n\ts_waitcnt vmcnt(0)" : "=&v"(r) : "v"(voff), "s"(rs) : "memory"); return r;
}
__device__ __forceinline__ void st128_sc1(v4i rs, int voff, int soff, v4u v) {
  asm volatile("buffer_store_dwordx4 %0, %1, %2, %3 offen" STPOL "\n\ts_nop 1" ::"v"(v), "v"(voff), "s"(rs), "s"(soff) : "memory");
}
__device__ __forceinline__ void st64_sc1(v4i rs, int voff, v2u v) {
  asm volatile("buffer_store_dwordx2 %0, %1, %2, 0 offen sc1" ::"v"(v), "v"(voff), "s"(rs) : "memory");
}
__device__ __forceinline__ void st32_sc1(v4i rs, int voff, unsigned v) {
  asm volatile("buffer_store_dword %0, %1, %2, 0 offen sc1" ::"v"(v), "v"(voff), "s"(rs) : "memory");
}
template <int N> __device__ __forceinline__ void waitbar() { asm volatile("s_waitcnt vmcnt(%0) lgkmcnt(0)\n\ts_barrier" ::"i"(N) : "memory"); }
__device__ __forceinline__ void ldsbar() { asm volatile("s_waitcnt lgkmcnt(0)\n\ts_barrier" ::: "memory"); }
__device__ __forceinline__ void drain() { asm volatile("s_waitcnt vmcnt(0)" ::: "memory"); }
__device__ __forceinline__ float bflo(unsigned u) { return __builtin_bit_cast(float, u << 16); }
__device__ __forceinline__ float bfhi(unsigned u) { return __builtin_bit_cast(float, u & 0xFFFF0000u); }
__device__ __forceinline__ unsigned pk2(float a, float b) {
  v2bf v; v.x = (__bf16)a; v.y = (__bf16)b; return __builtin_bit_cast(unsigned, v);
}
__device__ __forceinline__ float rbf(float a) { return (float)(__bf16)a; }
__device__ __forceinline__ float dot2(unsigned a, unsigned b, float c) {
  return __builtin_amdgcn_fdot2_f32_bf16(__builtin_bit_cast(v2bf, a), __builtin_bit_cast(v2bf, b), c, false);
}
__device__ __forceinline__ f32x4 mfma(v8bf a, v8bf b, f32x4 c) { return __builtin_amdgcn_mfma_f32_16x16x32_bf16(a, b, c, 0, 0, 0); }
__device__ __forceinline__ v8bf trfrag(LAS char* p, int second) {
  v4bf lo = __builtin_amdgcn_ds_read_tr16_b64_v4bf16((LAS v4bf*)p);
  v4bf hi = __builtin_amdgcn_ds_read_tr16_b64_v4bf16((LAS v4bf*)(p + second));
  return __builtin_shufflevector(lo, hi, 0, 1, 2, 3, 4, 5, 6, 7);
}
__device__ __forceinline__ float row8_sum(float v) {
  v += __builtin_bit_cast(float, __builtin_amdgcn_update_dpp(0, __builtin_bit_cast(int, v), 0xB1, 0xF, 0xF, false));
  v += __builtin_bit_cast(float, __builtin_amdgcn_update_dpp(0, __builtin_bit_cast(int, v), 0x4E, 0xF, 0xF, false));
  v += __builtin_bit_cast(float, __builtin_amdgcn_update_dpp(0, __builtin_bit_cast(int, v), 0x141, 0xF, 0xF, false));
  return v;
}
__device__ __forceinline__ float silu_f(float y) { return y * __builtin_amdgcn_rcpf(1.0f + __builtin_amdgcn_exp2f(y * -1.4426950408889634f)); }
template <class F, int... I> __device__ __forceinline__ void static_for_impl(F&& f, std::integer_sequence<int, I...>) { (f(std::integral_constant<int, I>{}), ...); }
template <int N, class F> __device__ __forceinline__ void static_for(F&& f) { static_for_impl(f, std::make_integer_sequence<int, N>{}); }

struct P1 {
  v4i rsA, rsB, rsH, rsC, rsT, rsPf;
  int avoff, asvoff, bvoff, ldb2;
  int pfso, bid;
  int w, wm, lane, grp, mem, rot;
  const char* bt;
  int tstart, ftile0;
  int isffn, htile0;
  unsigned tag, taghi;
  int k0, k0t, halo;
};
__device__ __forceinline__ int hk0(int hw) { return 20 + 14 * hw - ((hw + 1) >> 2); }

template <int I> __device__ __forceinline__ void p1_req(const P1& c, unsigned base, int st) {
  if (I == 0 && c.halo) dma16(c.rsA, base + c.w * 1024, c.asvoff, st * 128);
  if (I < 2) dma16(c.rsA, base + (2 + c.w + 8 * I) * 1024, c.avoff, st * 128 + I * 64 * (DM * 2));
  else dma16(c.rsB, base + A_BYTES + (c.w + 8 * (I - 2)) * 1024, c.bvoff, (st * 64 + (I - 2) * 16) * c.ldb2);
}
template <int E> __device__ __forceinline__ void tail_req(const P1& c, unsigned lds0) {
  dma16_sc1(c.rsT, lds0 + RING_OFF + E * 8192 + c.w * 1024, c.lane * 16, c.k0t * 5120 + E * 1024);
}

__device__ __forceinline__ void ldB5(v4u (&B)[5], v4i rsT, int voff, int so) {
  const int so2 = so == OOB ? OOB : so + 4096;
  B[0] = ld128_sc1<0>(rsT, voff, so); B[1] = ld128_sc1<1024>(rsT, voff, so); B[2] = ld128_sc1<2048>(rsT, voff, so);
  B[3] = ld128_sc1<3072>(rsT, voff, so); B[4] = ld128_sc1<0>(rsT, voff, so2);
}
#define TIE5(B) asm volatile("" : "+v"((B)[0]), "+v"((B)[1]), "+v"((B)[2]), "+v"((B)[3]), "+v"((B)[4]))

template <int NT, int NOWN, int NFOR>
__device__ __forceinline__ void phase1(P1& c, LAS char* smem, unsigned lds0, char* ws, v4u (&B)[5][5]) {
  constexpr int NB = NT + NFOR, NX = NOWN + NFOR;
  const int lane = c.lane, r = lane & 15, g = lane >> 4, q = r >> 2, p = lane & 3;
  int aad[2];
#pragma unroll
  for (int ks = 0; ks < 2; ks++) aad[ks] = r * 128 + (((4 * ks + g) ^ (r >> 1)) << 4);
  const int amain = (1 + c.wm * 4) * 2048;
  const int z = q | ((g & 1) << 2);
  int tile[NB], bad[NB];
#pragma unroll
  for (int n = 0; n < NB; n++) {
    if (n < NT) { int t = n + 2 * c.wm; t = t >= NT ? t - NT : t; tile[n] = c.tstart + t; }
    else tile[n] = c.ftile0 + 2 * (n - NT);
    bad[n] = A_BYTES + (8 * g + q) * 512 + (((tile[n] & 7) ^ z) << 5) + (tile[n] >> 3) * 256 + p * 8;
  }
  f32x4 acc[4][NT], accx[NX];
#pragma unroll
  for (int m = 0; m < 4; m++)
#pragma unroll
    for (int n = 0; n < NT; n++) acc[m][n] = f32x4{0, 0, 0, 0};
#pragma unroll
  for (int n = 0; n < NX; n++) accx[n] = f32x4{0, 0, 0, 0};
  v8bf af[2][4], as[2], bf[2][NB];
  const bool ones = (r == 0) || c.isffn;
  auto ldq = [&](int buf, LAS char* base, int ks, int i) {
    af[buf][i] = *(LAS v8bf*)(base + aad[ks] + amain + i * 2048);
#pragma unroll
    for (int n = 0; n < NB; n++) if ((n & 3) == i) bf[buf][n] = trfrag(base + bad[n] + ks * 16384, 2048);
    if (i == 3) as[buf] = *(LAS v8bf*)(base + aad[ks]);
  };
  auto row = [&](int buf, int m) {
#pragma unroll
    for (int n = 0; n < NT; n++) acc[m][n] = mfma(bf[buf][n], af[buf][m], acc[m][n]);
    if (m == 3) {
      v4u s = __builtin_bit_cast(v4u, as[buf]);
#pragma unroll
      for (int e = 0; e < 4; e++) s[e] = ones ? 0x3F803F80u : s[e];
      v8bf sv = __builtin_bit_cast(v8bf, s);
#pragma unroll
      for (int n = 0; n < NX; n++) accx[n] = mfma(bf[buf][n < NOWN ? n : NT + n - NOWN], sv, accx[n]);
    }
#pragma unroll
    for (int i = 0; i < NT; i++) {
      __builtin_amdgcn_sched_group_barrier(0x8, 1, 0); __builtin_amdgcn_sched_group_barrier(0x100, 2, 0); __builtin_amdgcn_sched_group_barrier(0x2, 1, 0);
    }
    SB;
  };
  auto halfstage = [&](int buf, bool rd, LAS char* rbase, int rks, auto&& rq0, auto&& rq1, auto&& rq2) {
    if (rd) { ldq(buf ^ 1, rbase, rks, 0); ldq(buf ^ 1, rbase, rks, 1); }
    row(buf, 0); rq0(); SB;
    if (rd) ldq(buf ^ 1, rbase, rks, 2);
    row(buf, 1); rq1(); SB;
    if (rd) ldq(buf ^ 1, rbase, rks, 3);
    row(buf, 2); rq2(); SB;
    row(buf, 3);
  };
  auto none = [&]() {};
  auto lst = [&](int i) {};
#pragma unroll
  for (int m = 0; m < 4; m++)
#pragma unroll
    for (int n = 0; n < NT; n++) asm volatile("" : "+v"(acc[m][n]));
#pragma unroll
  for (int n = 0; n < NX; n++) asm volatile("" : "+v"(accx[n]));
#pragma unroll
  for (int n = 0; n < NB; n++) asm volatile("" : "+v"(bad[n]));
  asm volatile("" : "+v"(aad[0]), "+v"(aad[1]));
  waitbar<9>();
  lst(0);
  STAMP(1);
  unsigned pfd = 0;
  auto special = [&](int st) {
    if (st == 0)
      asm volatile("buffer_load_dword %0, %1, %2, %3 offen" : "+v"(pfd) : "v"(lane * 128), "s"(c.rsPf), "s"(c.pfso));
    else if (st == 2) {
      const v2u ep = *(LAS v2u*)(smem + EPL_OFF);
      const u64 e = ((u64)(unsigned)__builtin_amdgcn_readfirstlane(ep.y) << 32) | (unsigned)__builtin_amdgcn_readfirstlane(ep.x);
      const u64 t = e * 0x5851F42D4C957F2Dull + 0x14057B7EF767814Full;
      c.tag = (unsigned)t; c.taghi = (unsigned)(t >> 32);
      if (c.w == 0) st32_sc1(c.rsC, lane == 0 ? RDY_OFF + c.bid * 4 : OOB, c.tag);
    } else if (st == 5) {
      if (c.w == 0) dma16_sc1(c.rsC, lds0 + RDYL_OFF, lane * 16, RDY_OFF);
    } else if (st == 6) {
      v4u rdy = *(LAS v4u*)(smem + RDYL_OFF + lane * 16);
      for (unsigned spins = 0;; spins++) {
        if (__all(rdy.x == c.tag && rdy.y == c.tag && rdy.z == c.tag && rdy.w == c.tag) || spins > SPIN_CAP) break;
        __builtin_amdgcn_s_sleep(POLL_SLEEP);
        asm volatile("buffer_load_dwordx4 %0, %1, %2, 0 offen offset:%3 sc1\n\ts_waitcnt vmcnt(0)" : "=&v"(rdy) : "v"(lane * 16), "s"(c.rsC), "i"(RDY_OFF) : "memory");
      }
      if (c.w < 2) dma16_sc1(c.rsC, lds0 + RS_OFF + c.w * 1024, lane * 16, STAT_OFF + c.grp * 2048 + c.w * 1024);
    }
  };
#pragma unroll
  for (int i = 0; i < 4; i++) ldq(0, smem, 0, i);
  int so = 0, po = 2 * ST_BYTES;
#pragma nounroll
  for (int st = 0; st < 7; st++) {
    const int no = so == 2 * ST_BYTES ? 0 : so + ST_BYTES;
    halfstage(0, true, smem + so, 1, [&]() { p1_req<3>(c, lds0 + po, st + 2); }, [&]() { p1_req<4>(c, lds0 + po, st + 2); }, [&]() { p1_req<5>(c, lds0 + po, st + 2); });
    waitbar<6>();
    SB;
    lst(st + 1);
    if ((0x65 >> st) & 1) special(st);
    SB;
    halfstage(1, true, smem + no, 0, [&]() { p1_req<0>(c, lds0 + so, st + 3); }, [&]() { p1_req<1>(c, lds0 + so, st + 3); }, [&]() { p1_req<2>(c, lds0 + so, st + 3); });
    po = so; so = no;
  }
  halfstage(0, true, smem + ST_BYTES, 1, [&]() { p1_req<3>(c, lds0, 9); }, [&]() { p1_req<4>(c, lds0, 9); }, [&]() { p1_req<5>(c, lds0, 9); });
  waitbar<6>(); SB; lst(8);
  halfstage(1, true, smem + 2 * ST_BYTES, 0, [&]() { tail_req<0>(c, lds0); }, [&]() { tail_req<1>(c, lds0); }, [&]() { tail_req<2>(c, lds0); });
  halfstage(0, true, smem + 2 * ST_BYTES, 1, none, none, none);
  waitbar<3>(); SB; lst(9);
  halfstage(1, true, smem, 0, [&]() { tail_req<3>(c, lds0); }, [&]() { tail_req<4>(c, lds0); }, [&]() { tail_req<5>(c, lds0); });
  halfstage(0, true, smem, 1, [&]() { tail_req<6>(c, lds0); }, [&]() { tail_req<7>(c, lds0); }, [&]() { tail_req<8>(c, lds0); });
  halfstage(1, false, smem, 0, [&]() { tail_req<9>(c, lds0); }, none, none);
  lst(10);
  STAMP(2);
#pragma unroll
  for (int n = 0; n < NX; n++)
    *(LAS f32x4*)(smem + (r == 0 ? CS_OFF + (tile[n < NOWN ? n : NT + n - NOWN] * 16 + 4 * g) * 4 : DUMP_OFF + lane * 16)) = accx[n];
  ldsbar();
  asm volatile("" ::"v"(pfd));
  STAMP(8);
  f32x2 rs[4];
#pragma unroll
  for (int m = 0; m < 4; m++) rs[m] = *(LAS f32x2*)(smem + RS_OFF + (16 + c.wm * 64 + m * 16 + r) * 8);
  auto ldB = [&](int s) { SB; ldB5(B[s], c.rsT, lane * 16, (c.k0 + s) * 5120); SB; };
  if (c.isffn) {
    float c1[4];
#pragma unroll
    for (int m = 0; m < 4; m++) c1[m] = __builtin_amdgcn_rcpf(rs[m].y) * -1.4426950408889634f;
#pragma unroll
    for (int n = 0; n < NT; n++) {
      const f32x4 cs = *(LAS f32x4*)(smem + CS_OFF + (tile[n] * 16 + 4 * g) * 4);
#pragma unroll
      for (int mp = 0; mp < 2; mp++) {
        v4u o;
#pragma unroll
        for (int h = 0; h < 2; h++) {
          const int m = 2 * mp + h;
          const f32x4 t = acc[m][n] - rs[m].x * cs;
          const f32x4 zz = t * c1[m];
          f32x4 e;
#pragma unroll
          for (int j = 0; j < 4; j++) e[j] = __builtin_amdgcn_exp2f(zz[j]);
          const f32x4 d = e * rs[m].y + rs[m].y;
          f32x4 rr;
#pragma unroll
          for (int j = 0; j < 4; j++) rr[j] = __builtin_amdgcn_rcpf(d[j]);
          const f32x4 y = t * rr;
          o[2 * h] = pk2(y[0], y[1]); o[2 * h + 1] = pk2(y[2], y[3]);
        }
        st128_sc1(c.rsH, lane * 16, ((c.grp * 4 + c.wm * 2 + mp) * NCT + c.htile0 + tile[n]) * 1024, o);
      }
    }
  } else {
    if (NT == 3) ldB(3);
    float ri[4];
#pragma unroll
    for (int m = 0; m < 4; m++) ri[m] = __builtin_amdgcn_rcpf(rs[m].y);
#pragma unroll
    for (int n = 0; n < NT; n++) {
      ldB(n);
      const f32x4 cs = *(LAS f32x4*)(smem + CS_OFF + (tile[n] * 16 + 4 * g) * 4);
#pragma unroll
      for (int m = 0; m < 4; m++) {
        const f32x4 y = (acc[m][n] - rs[m].x * cs) * ri[m];
        v2u o; o.x = pk2(y[0], y[1]); o.y = pk2(y[2], y[3]);
        *(LAS v2u*)(smem + (16 + c.wm * 64 + m * 16 + r) * IMG_LD + tile[n] * 32 + g * 8) = o;
      }
    }
    const f32x2 rh = *(LAS f32x2*)(smem + RS_OFF + r * 8);
    const float rih = __builtin_amdgcn_rcpf(rh.y);
#pragma unroll
    for (int n = 0; n < NX; n++) {
      const int t = tile[n < NOWN ? n : NT + n - NOWN];
      const f32x4 cs = *(LAS f32x4*)(smem + CS_OFF + (t * 16 + 4 * g) * 4);
      const f32x4 y = (accx[n] - rh.x * cs) * rih;
      v2u o; o.x = pk2(y[0], y[1]); o.y = pk2(y[2], y[3]);
      *(LAS v2u*)(smem + r * IMG_LD + t * 32 + g * 8) = o;
    }
    ldsbar();
  }
}

__device__ __forceinline__ void wait_flags(const P1& c, int off) {
  for (unsigned spins = 0;; spins++) {
    unsigned f;
    asm volatile("buffer_load_dword %0, %1, %2, 0 offen sc1\n\ts_waitcnt vmcnt(0)" : "=&v"(f) : "v"(off), "s"(c.rsC) : "memory");
    if (__all(off == OOB || f == c.tag) || spins > SPIN_CAP) break;
    __builtin_amdgcn_s_sleep(POLL_SLEEP);
  }
}
__device__ __forceinline__ void mfma_ip(f32x4& acc, const v4u& b, const v4u& a) {
  asm volatile("v_mfma_f32_16x16x32_bf16 %0, %1, %2, %0" : "+v"(acc) : "v"(b), "v"(a));
}
__device__ __forceinline__ void swap_pair(v4u& l0, v4u& l1) {
  unsigned a = l0.z, b = l1.x, c = l0.w, d = l1.y;
  asm volatile("v_swap_b32 %0, %1" : "+v"(a), "+v"(b));
  l0.z = a; l1.x = b;
  asm volatile("v_swap_b32 %0, %1" : "+v"(c), "+v"(d));
  l0.w = c; l1.y = d;
}
template <int I> __device__ __forceinline__ v4u ldA1(v4i rsH, int voff, int so) {
  return ld128_sc1<(I & 1) * 1024>(rsH, voff, I < 2 ? so : so + NCT * 1024);
}
template <int I> __device__ __forceinline__ v4u ldB1(v4i rsT, int voff, int so) {
  return ld128_sc1<(I & 3) * 1024>(rsT, voff, I < 4 ? so : so + 4096);
}
__device__ __forceinline__ void ldA4(v4u (&A)[4], v4i rsH, int voff, int so) {
  const int so2 = so == OOB ? OOB : so + NCT * 1024;
  A[0] = ld128_sc1<0>(rsH, voff, so); A[1] = ld128_sc1<1024>(rsH, voff, so);
  A[2] = ld128_sc1<0>(rsH, voff, so2); A[3] = ld128_sc1<1024>(rsH, voff, so2);
}
#define WAITA(A, N) asm volatile("s_waitcnt vmcnt(%4) lgkmcnt(0)" : "+v"((A)[0]), "+v"((A)[1]), "+v"((A)[2]), "+v"((A)[3]) : "i"(N) : "memory")
constexpr int h_na(int t) { return t == 1 ? 2 : (t + 3 < 14 ? 1 : 0); }
constexpr int h_a0(int t) { int n = 2; for (int i = 0; i < t; i++) n += h_na(i); return n; }
constexpr int hcnt(int t) { return 4 * h_na(t) + ((t >= 6 && t <= 10) ? 5 : 0) + (t == 8 ? 3 : 0); }
constexpr int hwait(int s) {
  int n = 0;
  if (s >= 9) { n = (s - 3 == 8 ? 3 : 0); for (int t = s - 2; t < s; t++) n += hcnt(t); return n; }
  if (s < 2) { n = 4 * (1 - s); for (int t = 0; t < s; t++) n += hcnt(t); return n; }
  int t0 = 0; while (h_a0(t0) + h_na(t0) <= s) t0++;
  n = hcnt(t0) - 4 * (s - h_a0(t0) + 1);
  for (int t = t0 + 1; t < s; t++) n += hcnt(t);
  return n;
}

__global__ __launch_bounds__(512, 2) void fused_kernel(const u16* __restrict__ x, const u16* __restrict__ wqkv, const u16* __restrict__ wo,
                                                       const u16* __restrict__ w1, const u16* __restrict__ w2, u16* __restrict__ out,
                                                       char* __restrict__ ws) {
  extern __shared__ __attribute__((aligned(16))) char smem_[];
  LAS char* smem = (LAS char*)smem_;
  const unsigned lds0 = (unsigned)(size_t)smem;
  const int tid = threadIdx.x, lane = tid & 63, w = __builtin_amdgcn_readfirstlane(tid >> 6);
  const int bid = blockIdx.x, xcd = bid & 7, idx = bid >> 3, half = xcd & 1, j8 = idx & 7;
  const int grp = (xcd >> 1) * 4 + (idx >> 3), mem = half * 8 + j8;
  const int isffn = j8 >> 2, sub = half * 4 + (j8 & 3);
  STAMP(0);
  P1 c;
  c.rsA = mkrs(x, SEQ * DM * 2);
  c.rsB = mkrs(isffn ? w1 : wqkv, isffn ? DM * FF * 2 : DM * 3 * DM * 2);
  c.ldb2 = isffn ? FF * 2 : 3 * DM * 2;
  c.w = w; c.lane = lane; c.grp = grp; c.isffn = isffn; c.mem = mem;
  const int ftile0 = sub < 4 ? sub * 14 : 56 + (sub - 4) * 13;
  const int ntile = isffn ? (sub < 4 ? 14 : 13) : 15;
  {
    const int ar = lane >> 3, sw = ((lane & 7) ^ ((4 * w + (lane >> 4)) & 7)) << 4;
    c.avoff = (grp * 128 + 8 * w + ar) * (DM * 2) + sw;
    int hr = grp * 128 - 16 + 8 * w + ar; hr = hr < 0 ? 0 : hr;
    c.asvoff = hr * (DM * 2) + sw;
    const int kk = 2 * w + (lane >> 5), zz = (kk & 3) | (((kk >> 3) & 1) << 2), pc = lane & 31, seg = (pc >> 1) ^ zz;
    const int part = (seg >= 10) + (seg >= 5);
    const int col = isffn ? (ftile0 + seg) * 16 : part * (DM - 80) + sub * 80 + seg * 16;
    c.bvoff = seg < ntile ? kk * c.ldb2 + col * 2 + (pc & 1) * 16 : OOB;
  }
  c.halo = !isffn && w < 2;
  static_for<6>([&](auto I) { p1_req<decltype(I)::value>(c, lds0, 0); });
  c.rsH = mkrs(ws, H_BYTES);
  c.rsC = mkrs(ws + CTRL_OFF, 1 << 20);
  const int cg = (xcd >> 1) * 4 + half * 2 + (idx >> 4), rh = (idx >> 3) & 1, c3 = idx & 7;
  c.bt = ws + BT_OFF; c.bid = bid;
  c.rsT = mkrs(ws + BT_OFF + (size_t)c3 * BT_SLICE, BT_SLICE);
  c.wm = w >> 2;
  c.k0 = w < 4 ? 5 * w : hk0(w & 3);
  c.k0t = hk0(w & 3) + 5 + 2 * (w >> 2);
  c.tag = 0; c.taghi = 0;
  const int wn = w < 4 ? w : 7 - w;
  c.htile0 = 40 + ftile0;
  unsigned pre[40];
  v4u xs[10];
  const int sslot = w * 8 + (lane >> 3);
  const int kap = (7 - w) * 16 + grp;
  const bool dostat = isffn && w < 3, dotile = isffn && w >= 3 && kap < 74;
  if (dostat) {
    int rho = sub * 18 + (sslot < 18 ? sslot : 17);
    int row = grp * 128 - 16 + rho; row = row < 0 ? 0 : row;
    const int vo = row * (DM * 2) + (lane & 7) * 16;
    static_for<10>([&](auto I) { constexpr int i = decltype(I)::value; xs[i] = ld128<128 * i>(c.rsA, vo, 0); });
  } else if (dotile) {
    const v4i rw = mkrs(kap < 20 ? wo : w2, kap < 20 ? DM * DM * 2 : DM * FF * 2);
    int kq = kap;
    if (kap >= 20) asm volatile("s_sub_u32 %0, %0, 20" : "+s"(kq)::"scc");
    const int so = kq * 32 * (DM * 2);
    const int vo = (lane >> 4) * 4 * (DM * 2) + (sub * 80 + (lane & 15)) * 2;
    static_for<40>([&](auto I) { constexpr int i = decltype(I)::value, n = i >> 3, j = i & 7;
      pre[i] = ld16<(j & 3) * (DM * 2) + n * 32>(rw, vo, so + (j >> 2) * 16 * (DM * 2)); });
  }
  static_for<6>([&](auto I) { p1_req<decltype(I)::value>(c, lds0 + ST_BYTES, 1); });
  static_for<3>([&](auto I) { p1_req<decltype(I)::value>(c, lds0 + 2 * ST_BYTES, 2); });
  {
    u64 pc0; unsigned codelen;
    asm volatile("s_getpc_b64 %0\n.Lcode_begin_mark:\n\ts_mov_b32 %1, .Lcode_end_mark-.Lcode_begin_mark" : "=s"(pc0), "=s"(codelen));
    const int gw = (bid - 8) * 8 + w;
    if (bid < 8) { c.rsPf = mkrs((const void*)pc0, (int)codelen); c.pfso = w * 8192; }
    else if (gw < 300) { c.rsPf = mkrs(wqkv, DM * 3 * DM * 2); c.pfso = gw * 8192; }
    else if (gw < 570) { c.rsPf = mkrs(w1, DM * FF * 2); c.pfso = (gw - 300) * 8192; }
    else { c.rsPf = c.rsA; c.pfso = (gw - 570) * 8192; }
  }
  if (w == 7) dma16_sc1(c.rsC, lds0 + EPL_OFF, lane == 0 ? EPOCH_OFF : OOB, 0);
  if (dostat) {
    asm volatile("s_waitcnt vmcnt(9)" : "+v"(xs[0]), "+v"(xs[1]), "+v"(xs[2]), "+v"(xs[3]), "+v"(xs[4]), "+v"(xs[5]), "+v"(xs[6]), "+v"(xs[7]), "+v"(xs[8]), "+v"(xs[9])::"memory");
    float s = 0;
#pragma unroll
    for (int i = 0; i < 40; i++) s += bflo(xs[i >> 2][i & 3]) + bfhi(xs[i >> 2][i & 3]);
    const float mean = row8_sum(s) * (1.0f / DM);
    float v = 0;
#pragma unroll
    for (int i = 0; i < 40; i++) { const float a = bflo(xs[i >> 2][i & 3]) - mean, b = bfhi(xs[i >> 2][i & 3]) - mean; v += a * a + b * b; }
    const float var = row8_sum(v) * (1.0f / DM);
    const float sd = __builtin_sqrtf(rbf(rbf(var) + 1e-5f));
    v2u gr; gr.x = __builtin_bit_cast(unsigned, rbf(mean)); gr.y = __builtin_bit_cast(unsigned, rbf(sd));
    st64_sc1(c.rsC, ((lane & 7) == 0 && sslot < 18) ? STAT_OFF + grp * 2048 + (sub * 18 + sslot) * 8 : OOB, gr);
  } else if (dotile) {
    asm volatile("s_waitcnt vmcnt(9)" ::: "memory");
#pragma unroll
    for (int i = 0; i < 40; i += 10)
      asm volatile("" : "+v"(pre[i]), "+v"(pre[i + 1]), "+v"(pre[i + 2]), "+v"(pre[i + 3]), "+v"(pre[i + 4]), "+v"(pre[i + 5]), "+v"(pre[i + 6]), "+v"(pre[i + 7]), "+v"(pre[i + 8]), "+v"(pre[i + 9]));
#pragma unroll
    for (int n = 0; n < 5; n++) {
      v4u v;
#pragma unroll
      for (int e = 0; e < 4; e++) v[e] = pre[8 * n + 2 * e] | (pre[8 * n + 2 * e + 1] << 16);
      st128_sc1(mkrs(ws + BT_OFF, 8 * BT_SLICE), lane * 16, sub * BT_SLICE + (kap * 5 + n) * 1024, v);
    }
  }
  v4u B[5][5];
  {
    const int n4 = ntile - 12;
    c.tstart = wn <= n4 ? 4 * wn : 4 * n4 + 3 * (wn - n4);
    if (!isffn) c.ftile0 = 1 + 6 * c.wm;
    else { const int fi = (wn - n4) * 2 + c.wm; c.ftile0 = 1 + 2 * (n4 == 2 ? fi : (fi & 1)); }
    if (wn < n4) phase1<4, 1, 0>(c, smem, lds0, ws, B);
    else if (!isffn) phase1<3, 2, 3>(c, smem, lds0, ws, B);
    else phase1<3, 2, 1>(c, smem, lds0, ws, B);
  }
  STAMP(9);
  const int r = lane & 15, g = lane >> 4;
  const int k0 = c.k0;
  auto ldB = [&](int s) { SB; ldB5(B[s], c.rsT, lane * 16, (k0 + s) * 5120); SB; };
  if (!isffn) {
    const int row = (w & 1) * 64 + lane;
    const int pos = grp * 128 + row;
    LAS char* qp = smem + (row + 16) * IMG_LD + (w >> 1) * 40;
    unsigned qv[10];
#pragma unroll
    for (int i = 0; i < 5; i++) { const v2u t = *(LAS v2u*)(qp + i * 8); qv[2 * i] = t.x; qv[2 * i + 1] = t.y; }
    float lg[8];
#pragma unroll
    for (int k = 0; k < 8; k++) {
      if (k == 2) ldB(4);
      LAS char* kp = qp + 160 - 2 * k * IMG_LD;
      float a0 = 0, a1 = 0;
#pragma unroll
      for (int i = 0; i < 5; i++) { const v2u t = *(LAS v2u*)(kp + i * 8); a0 = dot2(qv[2 * i], t.x, a0); a1 = dot2(qv[2 * i + 1], t.y, a1); }
      lg[k] = pos - 2 * k >= 0 ? a0 + a1 : -4e9f;
    }
    float mx = lg[0];
#pragma unroll
    for (int k = 1; k < 8; k++) mx = fmaxf(mx, lg[k]);
    float sum = 0;
    constexpr float C = 0.22360679774997896f * 1.4426950408889634f;
#pragma unroll
    for (int k = 0; k < 8; k++) { lg[k] = __builtin_amdgcn_exp2f((lg[k] - mx) * C); sum += lg[k]; }
    const float inv = __builtin_amdgcn_rcpf(sum);
    float o[20];
#pragma unroll
    for (int d = 0; d < 20; d++) o[d] = 0;
#pragma unroll
    for (int k = 0; k < 8; k++) {
      const unsigned plo = pk2(lg[k] * inv, 0.f), phi = plo << 16;
      LAS char* vp = qp + 320 - 2 * k * IMG_LD;
#pragma unroll
      for (int i = 0; i < 5; i++) {
        const v2u t = *(LAS v2u*)(vp + i * 8);
        o[4 * i] = dot2(t.x, plo, o[4 * i]); o[4 * i + 1] = dot2(t.x, phi, o[4 * i + 1]);
        o[4 * i + 2] = dot2(t.y, plo, o[4 * i + 2]); o[4 * i + 3] = dot2(t.y, phi, o[4 * i + 3]);
      }
    }
#pragma unroll
    for (int i = 0; i < 5; i++) { v2u t; t.x = pk2(o[4 * i], o[4 * i + 1]); t.y = pk2(o[4 * i + 2], o[4 * i + 3]); *(LAS v2u*)(qp + i * 8) = t; }
    STAMP(11);
    ldsbar();
#pragma unroll
    for (int i = 0; i < 3; i++) {
      const int b0 = w + 8 * i, b = b0 < 20 ? b0 : 19, rp = b / 5, t = b - 5 * rp;
      LAS char* sp = smem + (16 + rp * 32 + r) * IMG_LD + t * 32 + g * 8;
      const v2u lo = *(LAS v2u*)sp, hi = *(LAS v2u*)(sp + 16 * IMG_LD);
      v4u v; v.x = lo.x; v.y = lo.y; v.z = hi.x; v.w = hi.y;
      st128_sc1(c.rsH, lane * 16, b0 < 20 ? ((grp * 4 + rp) * NCT + sub * 5 + t) * 1024 : OOB, v);
    }
  }
  waitbar<0>();
  st32_sc1(c.rsC, tid == 0 ? FLAG_OFF + grp * 256 + mem * 4 : OOB, c.tag);
  STAMP(4);
  if (isffn) {
    if (w < 4) __builtin_amdgcn_s_sleep(40);
    ldB(0); ldB(1); ldB(2); ldB(3); ldB(4);
  }
  {
    int m;
    if (w < 4) m = lane < 2 ? (w >> 1) * 8 + (w & 1) * 2 + lane : -1;
    else {
      const int t0 = 2 * (k0 - 20), t1 = t0 + 27 - (w & 2);
      const int f0 = t0 < 56 ? t0 / 14 : 4 + (t0 - 56) / 13, f1 = t1 < 56 ? t1 / 14 : 4 + (t1 - 56) / 13, f = f0 + lane;
      m = f <= f1 ? (f >> 2) * 8 + 4 + (f & 3) : -1;
    }
    wait_flags(c, m >= 0 ? FLAG_OFF + cg * 256 + m * 4 : OOB);
  }
  STAMP(5);
  const int hso = ((cg * 4 + rh * 2) * NCT + 2 * k0) * 1024;
  v2u xr[3];
  auto ldx = [&]() {
#pragma unroll
    for (int i = 0; i < 3; i++) {
      const int tl = w + 8 * i, tt = tl < 20 ? tl : 19, m = tt / 5, n = tt - 5 * m;
      xr[i] = ld64(c.rsA, (cg * 128 + rh * 64 + m * 16 + r) * (DM * 2) + (c3 * 80 + n * 16 + g * 4) * 2, 0);
    }
  };
  auto put = [&](const f32x4 (&acc)[4][5], int p0, int p1, int stride) {
    STAMP(6);
    asm volatile("s_nop 7\n\ts_nop 3\n\ts_waitcnt vmcnt(0) lgkmcnt(0)" : "+v"(xr[0]), "+v"(xr[1]), "+v"(xr[2])::"memory");
#pragma unroll
    for (int m = 0; m < 4; m++)
#pragma unroll
      for (int n = 0; n < 5; n++) *(LAS f32x4*)(smem + (m < 2 ? p0 : p1) + ((m & 1) * 5 + n) * stride + lane * 16) = acc[m][n];
  };
  const int vo = lane * 16;
  if (w < 4) {
    v4u A[5][4];
    auto ldA = [&](int s) { ldA4(A[s], c.rsH, vo, hso + s * 2048); };
    ldA(0); ldA(1); ldA(2);
    f32x4 acc[4][5];
#pragma unroll
    for (int m = 0; m < 4; m++)
#pragma unroll
      for (int n = 0; n < 5; n++) acc[m][n] = f32x4{0, 0, 0, 0};
#pragma unroll
    for (int m = 0; m < 4; m++) TIE5(acc[m]);
#pragma unroll
    for (int s = 0; s < 5; s++) TIE5(B[s]);
    WAITA(A[0], 8); swap_pair(A[0][0], A[0][1]); swap_pair(A[0][2], A[0][3]);
    asm volatile("s_nop 1");
    static_for<5>([&](auto S) {
      constexpr int s = decltype(S)::value;
      static_for<20>([&](auto I) {
        constexpr int i = decltype(I)::value;
        mfma_ip(acc[i / 5][i % 5], B[s][i % 5], A[s][i / 5]);
        if constexpr ((s == 1 || s == 2) && i < 8 && (i & 1)) A[s + 2][i >> 1] = ldA1<(i >> 1)>(c.rsH, vo, hso + (s + 2) * 2048);
        if constexpr (s == 2 && i == 9) ldx();
        if constexpr (s < 4 && i == 13) WAITA(A[s + 1], (s == 0 ? 4 : s == 1 ? 4 : s == 2 ? 7 : 3));
        if constexpr (s < 4 && i == 14) swap_pair(A[s + 1][0], A[s + 1][1]);
        if constexpr (s < 4 && i == 15) swap_pair(A[s + 1][2], A[s + 1][3]);
      });
    });
    put(acc, w * 20480, w < 3 ? w * 20480 + 10240 : CS_OFF, 1024);
#pragma unroll
    for (int s = 0; s < 5; s++)
#pragma unroll
      for (int n = 0; n < 5; n++) asm volatile("" : "=v"(B[s][n]));
  }
  int w_h = w;
  asm volatile("" : "+s"(w_h));
  if (w_h >= 4) {
    const int hw = w & 3;
    v4u A[14][4], Bs[14][5];
    const int last = hw < 2 ? 0 : OOB;
    ldA4(A[0], c.rsH, vo, hso); ldA4(A[1], c.rsH, vo, hso + 2048);
    f32x4 acc[4][5];
#pragma unroll
    for (int m = 0; m < 4; m++)
#pragma unroll
      for (int n = 0; n < 5; n++) acc[m][n] = f32x4{0, 0, 0, 0};
#pragma unroll
    for (int m = 0; m < 4; m++) TIE5(acc[m]);
    LAS char* eb = smem + RING_OFF + hw * 1024 + lane * 16;
#pragma unroll
    for (int s = 0; s < 5; s++) {
      TIE5(B[s]);
#pragma unroll
      for (int n = 0; n < 5; n++) Bs[s][n] = B[s][n];
    }
    WAITA(A[0], 4); swap_pair(A[0][0], A[0][1]); swap_pair(A[0][2], A[0][3]);
    asm volatile("s_nop 1");
    static_for<14>([&](auto S) {
      constexpr int s = decltype(S)::value;
      static_for<20>([&](auto I) {
        constexpr int i = decltype(I)::value;
        mfma_ip(acc[i / 5][i % 5], Bs[s][i % 5], A[s][i / 5]);
        if constexpr (i < 4 * h_na(s)) {
          constexpr int k = h_a0(s) + (i >> 2);
          A[k][i & 3] = ldA1<(i & 3)>(c.rsH, k == 13 ? (vo | last) : vo, hso + k * 2048);
        }
        if constexpr (s >= 6 && s <= 10 && i >= 4 && i < 9) Bs[s + 3][i - 4] = ldB1<i - 4>(c.rsT, s == 10 ? (vo | last) : vo, (k0 + s + 3) * 5120);
        if constexpr (s == 8 && i == 9) ldx();
        if constexpr (s >= 4 && s < 8 && i >= 9 && i < 14) {
          SB; Bs[s + 1][i - 9] = *(LAS v4u*)(eb + ((s & 1) * 5 + i - 9) * 8192 + ((s - 4) >> 1) * 4096); SB;
        }
        if constexpr (s < 13 && i == 14) {
          asm volatile("s_waitcnt vmcnt(%4)" : "+v"(A[s + 1][0]), "+v"(A[s + 1][1]), "+v"(A[s + 1][2]), "+v"(A[s + 1][3]) : "i"(hwait(s + 1)) : "memory");
          if constexpr (s + 1 >= 9) TIE5(Bs[s + 1]);
        }
        if constexpr (s < 13 && i == 15) swap_pair(A[s + 1][0], A[s + 1][1]);
        if constexpr (s < 13 && i == 16) swap_pair(A[s + 1][2], A[s + 1][3]);
      });
    });
    put(acc, RING_OFF + hw * 1024, RING_OFF + hw * 1024 + 4096, 8192);
  }
  STAMP(12);
  ldsbar();
  STAMP(13);
  if (w == 0 && bid == 0) { v2u t; t.x = c.tag; t.y = c.taghi; st64_sc1(c.rsC, lane == 0 ? EPOCH_OFF : OOB, t); }
#pragma unroll
  for (int i = 0; i < 3; i++) {
    const int tl = w + 8 * i;
    if (tl < 20) {
      const int m = tl / 5, n = tl - 5 * m;
      LAS char* ps = smem + tl * 1024 + lane * 16;
      LAS char* pf = smem + RING_OFF + (tl < 10 ? tl * 8192 : (tl - 10) * 8192 + 4096) + lane * 16;
      const f32x4 s = (*(LAS f32x4*)ps + *(LAS f32x4*)(ps + 20480)) + (*(LAS f32x4*)(ps + 40960) + *(LAS f32x4*)(ps + (tl < 10 ? 61440 : CS_OFF - 10240)));
      const f32x4 f = (*(LAS f32x4*)pf + *(LAS f32x4*)(pf + 1024)) + (*(LAS f32x4*)(pf + 2048) + *(LAS f32x4*)(pf + 3072));
      float y[4];
#pragma unroll
      for (int j = 0; j < 4; j++) {
        const float xv = (j & 1) ? bfhi(xr[i][j >> 1]) : bflo(xr[i][j >> 1]);
        y[j] = xv + s[j] * silu_f(f[j]);
      }
      v2u o; o.x = pk2(y[0], y[1]); o.y = pk2(y[2], y[3]);
      *(GAS v2u*)((GAS char*)out + (size_t)(cg * 128 + rh * 64 + m * 16 + r) * (DM * 2) + (c3 * 80 + n * 16 + g * 4) * 2) = o;
    }
  }
  STAMP(7);
  asm volatile(".Lcode_end_mark:");
}
}

extern "C" void kernel_launch(void* const* d_in, const int* in_sizes, int n_in, void* d_out, int out_size, void* d_ws, size_t ws_size,
                              hipStream_t stream) {
  (void)hipFuncSetAttribute((const void*)fused_kernel, hipFuncAttributeMaxDynamicSharedMemorySize, LDS_BYTES);
  fused_kernel<<<256, 512, LDS_BYTES, stream>>>((const u16*)d_in[0], (const u16*)d_in[1], (const u16*)d_in[2], (const u16*)d_in[4],
                                                (const u16*)d_in[5], (u16*)d_out, (char*)d_ws);
}
```

```cpp
#include <hip/hip_runtime.h>
#include <stdint.h>
#include <type_traits>
#include <utility>

#define GAS __attribute__((address_space(1)))
#define LAS __attribute__((address_space(3)))
typedef unsigned short u16;
typedef unsigned long long u64;
typedef __bf16 v2bf __attribute__((ext_vector_type(2)));
typedef __bf16 v4bf __attribute__((ext_vector_type(4)));
typedef __bf16 v8bf __attribute__((ext_vector_type(8)));
typedef float f32x2 __attribute__((ext_vector_type(2)));
typedef float f32x4 __attribute__((ext_vector_type(4)));
typedef unsigned v4u __attribute__((ext_vector_type(4)));
typedef unsigned v2u __attribute__((ext_vector_type(2)));
typedef int v4i __attribute__((ext_vector_type(4)));

namespace {
constexpr int SEQ = 2048, DM = 640, FF = 1728;
constexpr int NCT = 148;
constexpr int H_BYTES = 64 * NCT * 1024;
constexpr size_t CTRL_OFF = 16u << 20;
constexpr int EPOCH_OFF = 0, RDY_OFF = 2048, FLAG_OFF = 8192, STAT_OFF = 65536;
constexpr size_t BT_OFF = 12u << 20;
constexpr int BT_SLICE = 74 * 5120;
constexpr int A_BYTES = 144 * 128, B_BYTES = 64 * 512, ST_BYTES = A_BYTES + B_BYTES;
constexpr int CS_OFF = 3 * ST_BYTES, RS_OFF = CS_OFF + 1024;
constexpr int RDYL_OFF = CS_OFF + 6144, EPL_OFF = CS_OFF + 7168;
constexpr int DUMP_OFF = CS_OFF + 4096;
constexpr int IMG_LD = 488;
constexpr int RING_OFF = 71680;
constexpr int LDS_BYTES = 163840;
constexpr int OOB = 0x7FF00000;
constexpr unsigned SPIN_CAP = 1u << 16;
#define SGB 1
#define MAP3 1
#define POLL_SLEEP 8
#define STAMP(i)
#define SB __builtin_amdgcn_sched_barrier(0)
#define LDPOL2 " sc1"

__device__ __forceinline__ v4i mkrs(const void* p, int bytes) {
  u64 a = (u64)p; v4i r; r.x = (int)(unsigned)a; r.y = (int)((unsigned)(a >> 32) & 0xFFFFu); r.z = bytes; r.w = 0x00020000; return r;
}
__device__ __forceinline__ void dma16(v4i rs, unsigned lds, int voff, int soff) {
  asm volatile("s_mov_b32 m0, %0\n\ts_nop 0\n\tbuffer_load_dwordx4 %1, %2, %3 offen lds" ::"s"(lds), "v"(voff), "s"(rs), "s"(soff));
}
__device__ __forceinline__ void dma16_sc1(v4i rs, unsigned lds, int voff, int soff) {
  asm volatile("s_mov_b32 m0, %0\n\ts_nop 3\n\tbuffer_load_dwordx4 %1, %2, %3 offen" LDPOL2 " lds" ::"s"(lds), "v"(voff), "s"(rs), "s"(soff));
}
__device__ __forceinline__ void dma16_50(v4i rs, unsigned lds, int voff, int soff) {
  asm volatile("s_mov_b32 m0, %0\n\ts_mov_b32 exec_hi, 0x3ffff\n\tbuffer_load_dwordx4 %1, %2, %3 offen lds\n\ts_mov_b32 exec_hi, -1" ::"s"(lds), "v"(voff), "s"(rs), "s"(soff));
}
#define LDPOL " sc1"
#define STPOL " sc1"
template <int IMM> __device__ __forceinline__ v4u ld128_sc1(v4i rs, int voff, int soff) {
  v4u r; asm volatile("buffer_load_dwordx4 %0, %1, %2, %3 offen offset:%4" LDPOL : "=v"(r) : "v"(voff), "s"(rs), "s"(soff), "i"(IMM)); return r;
}
template <int IMM> __device__ __forceinline__ v4u ld128(v4i rs, int voff, int soff) {
  v4u r; asm volatile("buffer_load_dwordx4 %0, %1, %2, %3 offen offset:%4" : "=v"(r) : "v"(voff), "s"(rs), "s"(soff), "i"(IMM)); return r;
}
template <int IMM> __device__ __forceinline__ unsigned ld16(v4i rs, int voff, int soff) {
  unsigned r; asm volatile("buffer_load_ushort %0, %1, %2, %3 offen offset:%4" : "=v"(r) : "v"(voff), "s"(rs), "s"(soff), "i"(IMM)); return r;
}
__device__ __forceinline__ v2u ld64(v4i rs, int voff, int soff) {
  v2u r; asm volatile("buffer_load_dwordx2 %0, %1, %2, %3 offen" : "=v"(r) : "v"(voff), "s"(rs), "s"(soff)); return r;
}
__device__ __forceinline__ v2u ld64_sc1(v4i rs, int voff, int soff) {
  v2u r; asm volatile("buffer_load_dwordx2 %0, %1, %2, %3 offen sc1" : "=v"(r) : "v"(voff), "s"(rs), "s"(soff)); return r;
}
__device__ __forceinline__ v2u ld64_sc1_wait(v4i rs, int voff) {
  v2u r; asm volatile("buffer_load_dwordx2 %0, %1, %2, 0 offen sc1\n\ts_waitcnt vmcnt(0)" : "=&v"(r) : "v"(voff), "s"(rs) : "memory"); return r;
}
__device__ __forceinline__ void st128_sc1(v4i rs, int voff, int soff, v4u v) {
  asm volatile("buffer_store_dwordx4 %0, %1, %2, %3 offen" STPOL "\n\ts_nop 1" ::"v"(v), "v"(voff), "s"(rs), "s"(soff) : "memory");
}
__device__ __forceinline__ void st64_sc1(v4i rs, int voff, v2u v) {
  asm volatile("buffer_store_dwordx2 %0, %1, %2, 0 offen sc1" ::"v"(v), "v"(voff), "s"(rs) : "memory");
}
__device__ __forceinline__ void st32_sc1(v4i rs, int voff, unsigned v) {
  asm volatile("buffer_store_dword %0, %1, %2, 0 offen sc1" ::"v"(v), "v"(voff), "s"(rs) : "memory");
}
template <int N> __device__ __forceinline__ void waitbar() { asm volatile("s_waitcnt vmcnt(%0) lgkmcnt(0)\n\ts_barrier" ::"i"(N) : "memory"); }
__device__ __forceinline__ void ldsbar() { asm volatile("s_waitcnt lgkmcnt(0)\n\ts_barrier" ::: "memory"); }
__device__ __forceinline__ void drain() { asm volatile("s_waitcnt vmcnt(0)" ::: "memory"); }
__device__ __forceinline__ float bflo(unsigned u) { return __builtin_bit_cast(float, u << 16); }
__device__ __forceinline__ float bfhi(unsigned u) { return __builtin_bit_cast(float, u & 0xFFFF0000u); }
__device__ __forceinline__ unsigned pk2(float a, float b) {
  v2bf v; v.x = (__bf16)a; v.y = (__bf16)b; return __builtin_bit_cast(unsigned, v);
}
__device__ __forceinline__ float rbf(float a) { return (float)(__bf16)a; }
__device__ __forceinline__ float dot2(unsigned a, unsigned b, float c) {
  return __builtin_amdgcn_fdot2_f32_bf16(__builtin_bit_cast(v2bf, a), __builtin_bit_cast(v2bf, b), c, false);
}
__device__ __forceinline__ f32x4 mfma(v8bf a, v8bf b, f32x4 c) { return __builtin_amdgcn_mfma_f32_16x16x32_bf16(a, b, c, 0, 0, 0); }
__device__ __forceinline__ v8bf trfrag(LAS char* p, int second) {
  v4bf lo = __builtin_amdgcn_ds_read_tr16_b64_v4bf16((LAS v4bf*)p);
  v4bf hi = __builtin_amdgcn_ds_read_tr16_b64_v4bf16((LAS v4bf*)(p + second));
  return __builtin_shufflevector(lo, hi, 0, 1, 2, 3, 4, 5, 6, 7);
}
__device__ __forceinline__ float row8_sum(float v) {
  v += __builtin_bit_cast(float, __builtin_amdgcn_update_dpp(0, __builtin_bit_cast(int, v), 0xB1, 0xF, 0xF, false));
  v += __builtin_bit_cast(float, __builtin_amdgcn_update_dpp(0, __builtin_bit_cast(int, v), 0x4E, 0xF, 0xF, false));
  v += __builtin_bit_cast(float, __builtin_amdgcn_update_dpp(0, __builtin_bit_cast(int, v), 0x141, 0xF, 0xF, false));
  return v;
}
__device__ __forceinline__ float silu_f(float y) { return y * __builtin_amdgcn_rcpf(1.0f + __builtin_amdgcn_exp2f(y * -1.4426950408889634f)); }
template <class F, int... I> __device__ __forceinline__ void static_for_impl(F&& f, std::integer_sequence<int, I...>) { (f(std::integral_constant<int, I>{}), ...); }
template <int N, class F> __device__ __forceinline__ void static_for(F&& f) { static_for_impl(f, std::make_integer_sequence<int, N>{}); }

struct P1 {
  v4i rsA, rsB, rsH, rsC, rsT, rsPf;
  int avoff, asvoff, bvoff, ldb2;
  int pfso, bid;
  int w, wm, lane, grp, mem, rot;
  const char* bt;
  int tstart, ftile0;
  int isffn, htile0;
  unsigned tag, taghi;
  int k0, halo;
};
__device__ __forceinline__ int k0_of(int w) { return 9 * w + (w >= 1) + (w >= 2); }

template <int I> __device__ __forceinline__ void p1_req(const P1& c, unsigned base, int st) {
  if (I == 0 && c.halo) dma16(c.rsA, base + c.w * 1024, c.asvoff, st * 128);
  if (I < 2) dma16(c.rsA, base + (2 + c.w + 8 * I) * 1024, c.avoff, st * 128 + I * 64 * (DM * 2));
  else dma16(c.rsB, base + A_BYTES + (c.w + 8 * (I - 2)) * 1024, c.bvoff, (st * 64 + (I - 2) * 16) * c.ldb2);
}
template <int E> __device__ __forceinline__ void tail_req(const P1& c, unsigned lds0) {
  dma16_sc1(c.rsT, lds0 + RING_OFF + E * 8192 + c.w * 1024, c.lane * 16, (c.k0 + 6) * 5120 + E * 1024);
}

__device__ __forceinline__ void ldB5(v4u (&B)[5], v4i rsT, int voff, int so) {
  const int so2 = so == OOB ? OOB : so + 4096;
  B[0] = ld128_sc1<0>(rsT, voff, so); B[1] = ld128_sc1<1024>(rsT, voff, so); B[2] = ld128_sc1<2048>(rsT, voff, so);
  B[3] = ld128_sc1<3072>(rsT, voff, so); B[4] = ld128_sc1<0>(rsT, voff, so2);
}
#define TIE5(B) asm volatile("" : "+v"((B)[0]), "+v"((B)[1]), "+v"((B)[2]), "+v"((B)[3]), "+v"((B)[4]))

template <int NT, int NOWN, int NFOR>
__device__ __forceinline__ void phase1(P1& c, LAS char* smem, unsigned lds0, char* ws, v4u (&B)[6][5]) {
  constexpr int NB = NT + NFOR, NX = NOWN + NFOR;
  const int lane = c.lane, r = lane & 15, g = lane >> 4, q = r >> 2, p = lane & 3;
  int aad[2];
#pragma unroll
  for (int ks = 0; ks < 2; ks++) aad[ks] = r * 128 + (((4 * ks + g) ^ (r >> 1)) << 4);
  const int amain = (1 + c.wm * 4) * 2048;
  const int z = q | ((g & 1) << 2);
  int tile[NB], bad[NB];
#pragma unroll
  for (int n = 0; n < NB; n++) {
    if (n < NT) { int t = n + 2 * c.wm; t = t >= NT ? t - NT : t; tile[n] = c.tstart + t; }
    else tile[n] = c.ftile0 + 2 * (n - NT);
    bad[n] = A_BYTES + (8 * g + q) * 512 + (((tile[n] & 7) ^ z) << 5) + (tile[n] >> 3) * 256 + p * 8;
  }
  f32x4 acc[4][NT], accx[NX];
#pragma unroll
  for (int m = 0; m < 4; m++)
#pragma unroll
    for (int n = 0; n < NT; n++) acc[m][n] = f32x4{0, 0, 0, 0};
#pragma unroll
  for (int n = 0; n < NX; n++) accx[n] = f32x4{0, 0, 0, 0};
  v8bf af[2][4], as[2], bf[2][NB];
  const bool ones = (r == 0) || c.isffn;
  auto ldq = [&](int buf, LAS char* base, int ks, int i) {
    af[buf][i] = *(LAS v8bf*)(base + aad[ks] + amain + i * 2048);
#pragma unroll
    for (int n = 0; n < NB; n++) if ((n & 3) == i) bf[buf][n] = trfrag(base + bad[n] + ks * 16384, 2048);
    if (i == 3) as[buf] = *(LAS v8bf*)(base + aad[ks]);
  };
  auto row = [&](int buf, int m) {
#pragma unroll
    for (int n = 0; n < NT; n++) acc[m][n] = mfma(bf[buf][n], af[buf][m], acc[m][n]);
    if (m == 3) {
      v4u s = __builtin_bit_cast(v4u, as[buf]);
#pragma unroll
      for (int e = 0; e < 4; e++) s[e] = ones ? 0x3F803F80u : s[e];
      v8bf sv = __builtin_bit_cast(v8bf, s);
#pragma unroll
      for (int n = 0; n < NX; n++) accx[n] = mfma(bf[buf][n < NOWN ? n : NT + n - NOWN], sv, accx[n]);
    }
#pragma unroll
    for (int i = 0; i < NT; i++) {
      __builtin_amdgcn_sched_group_barrier(0x8, 1, 0); __builtin_amdgcn_sched_group_barrier(0x100, 2, 0); __builtin_amdgcn_sched_group_barrier(0x2, 1, 0);
    }
    SB;
  };
  auto halfstage = [&](int buf, bool rd, LAS char* rbase, int rks, auto&& rq0, auto&& rq1, auto&& rq2) {
    if (rd) { ldq(buf ^ 1, rbase, rks, 0); ldq(buf ^ 1, rbase, rks, 1); }
    row(buf, 0); rq0(); SB;
    if (rd) ldq(buf ^ 1, rbase, rks, 2);
    row(buf, 1); rq1(); SB;
    if (rd) ldq(buf ^ 1, rbase, rks, 3);
    row(buf, 2); rq2(); SB;
    row(buf, 3);
  };
  auto none = [&]() {};
  auto lst = [&](int i) {};
#pragma unroll
  for (int m = 0; m < 4; m++)
#pragma unroll
    for (int n = 0; n < NT; n++) asm volatile("" : "+v"(acc[m][n]));
#pragma unroll
  for (int n = 0; n < NX; n++) asm volatile("" : "+v"(accx[n]));
#pragma unroll
  for (int n = 0; n < NB; n++) asm volatile("" : "+v"(bad[n]));
  asm volatile("" : "+v"(aad[0]), "+v"(aad[1]));
  waitbar<9>();
  lst(0);
  STAMP(1);
  unsigned pfd = 0;
  auto special = [&](int st) {
    if (st == 0)
      asm volatile("buffer_load_dword %0, %1, %2, %3 offen" : "+v"(pfd) : "v"(lane * 128), "s"(c.rsPf), "s"(c.pfso));
    else if (st == 2) {
      const v2u ep = *(LAS v2u*)(smem + EPL_OFF);
      const u64 e = ((u64)(unsigned)__builtin_amdgcn_readfirstlane(ep.y) << 32) | (unsigned)__builtin_amdgcn_readfirstlane(ep.x);
      const u64 t = e * 0x5851F42D4C957F2Dull + 0x14057B7EF767814Full;
      c.tag = (unsigned)t; c.taghi = (unsigned)(t >> 32);
      if (c.w == 0) st32_sc1(c.rsC, lane == 0 ? RDY_OFF + c.bid * 4 : OOB, c.tag);
    } else if (st == 5) {
      if (c.w == 0) dma16_sc1(c.rsC, lds0 + RDYL_OFF, lane * 16, RDY_OFF);
    } else if (st == 6) {
      v4u rdy = *(LAS v4u*)(smem + RDYL_OFF + lane * 16);
      for (unsigned spins = 0;; spins++) {
        if (__all(rdy.x == c.tag && rdy.y == c.tag && rdy.z == c.tag && rdy.w == c.tag) || spins > SPIN_CAP) break;
        __builtin_amdgcn_s_sleep(POLL_SLEEP);
        asm volatile("buffer_load_dwordx4 %0, %1, %2, 0 offen offset:%3 sc1\n\ts_waitcnt vmcnt(0)" : "=&v"(rdy) : "v"(lane * 16), "s"(c.rsC), "i"(RDY_OFF) : "memory");
      }
      if (c.w < 2) dma16_sc1(c.rsC, lds0 + RS_OFF + c.w * 1024, lane * 16, STAT_OFF + c.grp * 2048 + c.w * 1024);
    }
  };
#pragma unroll
  for (int i = 0; i < 4; i++) ldq(0, smem, 0, i);
  int so = 0, po = 2 * ST_BYTES;
#pragma nounroll
  for (int st = 0; st < 7; st++) {
    const int no = so == 2 * ST_BYTES ? 0 : so + ST_BYTES;
    halfstage(0, true, smem + so, 1, [&]() { p1_req<3>(c, lds0 + po, st + 2); }, [&]() { p1_req<4>(c, lds0 + po, st + 2); }, [&]() { p1_req<5>(c, lds0 + po, st + 2); });
    waitbar<6>();
    SB;
    lst(st + 1);
    if ((0x65 >> st) & 1) special(st);
    SB;
    halfstage(1, true, smem + no, 0, [&]() { p1_req<0>(c, lds0 + so, st + 3); }, [&]() { p1_req<1>(c, lds0 + so, st + 3); }, [&]() { p1_req<2>(c, lds0 + so, st + 3); });
    po = so; so = no;
  }
  halfstage(0, true, smem + ST_BYTES, 1, [&]() { p1_req<3>(c, lds0, 9); }, [&]() { p1_req<4>(c, lds0, 9); }, [&]() { p1_req<5>(c, lds0, 9); });
  waitbar<6>(); SB; lst(8);
  halfstage(1, true, smem + 2 * ST_BYTES, 0, [&]() { tail_req<0>(c, lds0); }, [&]() { tail_req<1>(c, lds0); }, [&]() { tail_req<2>(c, lds0); });
  halfstage(0, true, smem + 2 * ST_BYTES, 1, none, none, none);
  waitbar<3>(); SB; lst(9);
  halfstage(1, true, smem, 0, [&]() { tail_req<3>(c, lds0); }, [&]() { tail_req<4>(c, lds0); }, [&]() { tail_req<5>(c, lds0); });
  halfstage(0, true, smem, 1, [&]() { tail_req<6>(c, lds0); }, [&]() { tail_req<7>(c, lds0); }, [&]() { tail_req<8>(c, lds0); });
  halfstage(1, false, smem, 0, [&]() { tail_req<9>(c, lds0); }, none, none);
  lst(10);
  STAMP(2);
#pragma unroll
  for (int n = 0; n < NX; n++)
    *(LAS f32x4*)(smem + (r == 0 ? CS_OFF + (tile[n < NOWN ? n : NT + n - NOWN] * 16 + 4 * g) * 4 : DUMP_OFF + lane * 16)) = accx[n];
  ldsbar();
  asm volatile("" ::"v"(pfd));
  STAMP(8);
  f32x2 rs[4];
#pragma unroll
  for (int m = 0; m < 4; m++) rs[m] = *(LAS f32x2*)(smem + RS_OFF + (16 + c.wm * 64 + m * 16 + r) * 8);
  auto ldB = [&](int s) { SB; ldB5(B[s], c.rsT, lane * 16, (c.k0 + s) * 5120); SB; };
  if (NT == 3) ldB(3);
  if (c.isffn) {
    float c1[4];
#pragma unroll
    for (int m = 0; m < 4; m++) c1[m] = __builtin_amdgcn_rcpf(rs[m].y) * -1.4426950408889634f;
#pragma unroll
    for (int n = 0; n < NT; n++) {
      ldB(n);
      const f32x4 cs = *(LAS f32x4*)(smem + CS_OFF + (tile[n] * 16 + 4 * g) * 4);
#pragma unroll
      for (int mp = 0; mp < 2; mp++) {
        v4u o;
#pragma unroll
        for (int h = 0; h < 2; h++) {
          const int m = 2 * mp + h;
          const f32x4 t = acc[m][n] - rs[m].x * cs;
          const f32x4 zz = t * c1[m];
          f32x4 e;
#pragma unroll
          for (int j = 0; j < 4; j++) e[j] = __builtin_amdgcn_exp2f(zz[j]);
          const f32x4 d = e * rs[m].y + rs[m].y;
          f32x4 rr;
#pragma unroll
          for (int j = 0; j < 4; j++) rr[j] = __builtin_amdgcn_rcpf(d[j]);
          const f32x4 y = t * rr;
          o[2 * h] = pk2(y[0], y[1]); o[2 * h + 1] = pk2(y[2], y[3]);
        }
        st128_sc1(c.rsH, lane * 16, ((c.grp * 4 + c.wm * 2 + mp) * NCT + c.htile0 + tile[n]) * 1024, o);
      }
    }
  } else {
    float ri[4];
#pragma unroll
    for (int m = 0; m < 4; m++) ri[m] = __builtin_amdgcn_rcpf(rs[m].y);
#pragma unroll
    for (int n = 0; n < NT; n++) {
      ldB(n);
      const f32x4 cs = *(LAS f32x4*)(smem + CS_OFF + (tile[n] * 16 + 4 * g) * 4);
#pragma unroll
      for (int m = 0; m < 4; m++) {
        const f32x4 y = (acc[m][n] - rs[m].x * cs) * ri[m];
        v2u o; o.x = pk2(y[0], y[1]); o.y = pk2(y[2], y[3]);
        *(LAS v2u*)(smem + (16 + c.wm * 64 + m * 16 + r) * IMG_LD + tile[n] * 32 + g * 8) = o;
      }
    }
    const f32x2 rh = *(LAS f32x2*)(smem + RS_OFF + r * 8);
    const float rih = __builtin_amdgcn_rcpf(rh.y);
#pragma unroll
    for (int n = 0; n < NX; n++) {
      const int t = tile[n < NOWN ? n : NT + n - NOWN];
      const f32x4 cs = *(LAS f32x4*)(smem + CS_OFF + (t * 16 + 4 * g) * 4);
      const f32x4 y = (accx[n] - rh.x * cs) * rih;
      v2u o; o.x = pk2(y[0], y[1]); o.y = pk2(y[2], y[3]);
      *(LAS v2u*)(smem + r * IMG_LD + t * 32 + g * 8) = o;
    }
    ldsbar();
  }
}

__device__ __forceinline__ void wait_flags(const P1& c, int off) {
  for (unsigned spins = 0;; spins++) {
    unsigned f;
    asm volatile("buffer_load_dword %0, %1, %2, 0 offen sc1\n\ts_waitcnt vmcnt(0)" : "=&v"(f) : "v"(off), "s"(c.rsC) : "memory");
    if (__all(off == OOB || f == c.tag) || spins > SPIN_CAP) break;
    __builtin_amdgcn_s_sleep(POLL_SLEEP);
  }
}
__device__ __forceinline__ void mma20(f32x4 (&acc)[4][5], const v4u (&b)[5], const v4u (&A)[4]) {
  v4u a[4];
#pragma unroll
  for (int h = 0; h < 2; h++) {
    const v4u l0 = A[2 * h], l1 = A[2 * h + 1];
    a[2 * h] = __builtin_shufflevector(l0, l1, 0, 1, 4, 5);
    a[2 * h + 1] = __builtin_shufflevector(l0, l1, 2, 3, 6, 7);
  }
  asm volatile("s_nop 1\n\t"
               "v_mfma_f32_16x16x32_bf16 %0, %20, %25, %0\n\t"
               "v_mfma_f32_16x16x32_bf16 %1, %21, %25, %1\n\t"
               "v_mfma_f32_16x16x32_bf16 %2, %22, %25, %2\n\t"
               "v_mfma_f32_16x16x32_bf16 %3, %23, %25, %3\n\t"
               "v_mfma_f32_16x16x32_bf16 %4, %24, %25, %4\n\t"
               "v_mfma_f32_16x16x32_bf16 %5, %20, %26, %5\n\t"
               "v_mfma_f32_16x16x32_bf16 %6, %21, %26, %6\n\t"
               "v_mfma_f32_16x16x32_bf16 %7, %22, %26, %7\n\t"
               "v_mfma_f32_16x16x32_bf16 %8, %23, %26, %8\n\t"
               "v_mfma_f32_16x16x32_bf16 %9, %24, %26, %9\n\t"
               "v_mfma_f32_16x16x32_bf16 %10, %20, %27, %10\n\t"
               "v_mfma_f32_16x16x32_bf16 %11, %21, %27, %11\n\t"
               "v_mfma_f32_16x16x32_bf16 %12, %22, %27, %12\n\t"
               "v_mfma_f32_16x16x32_bf16 %13, %23, %27, %13\n\t"
               "v_mfma_f32_16x16x32_bf16 %14, %24, %27, %14\n\t"
               "v_mfma_f32_16x16x32_bf16 %15, %20, %28, %15\n\t"
               "v_mfma_f32_16x16x32_bf16 %16, %21, %28, %16\n\t"
               "v_mfma_f32_16x16x32_bf16 %17, %22, %28, %17\n\t"
               "v_mfma_f32_16x16x32_bf16 %18, %23, %28, %18\n\t"
               "v_mfma_f32_16x16x32_bf16 %19, %24, %28, %19\n\t"
               : "+v"(acc[0][0]), "+v"(acc[0][1]), "+v"(acc[0][2]), "+v"(acc[0][3]), "+v"(acc[0][4]), "+v"(acc[1][0]), "+v"(acc[1][1]), "+v"(acc[1][2]), "+v"(acc[1][3]), "+v"(acc[1][4]), "+v"(acc[2][0]), "+v"(acc[2][1]), "+v"(acc[2][2]), "+v"(acc[2][3]), "+v"(acc[2][4]), "+v"(acc[3][0]), "+v"(acc[3][1]), "+v"(acc[3][2]), "+v"(acc[3][3]), "+v"(acc[3][4])
               : "v"(b[0]), "v"(b[1]), "v"(b[2]), "v"(b[3]), "v"(b[4]), "v"(a[0]), "v"(a[1]), "v"(a[2]), "v"(a[3]));
}
__device__ __forceinline__ void ldA4(v4u (&A)[4], v4i rsH, int voff, int so) {
  const int so2 = so == OOB ? OOB : so + NCT * 1024;
  A[0] = ld128_sc1<0>(rsH, voff, so); A[1] = ld128_sc1<1024>(rsH, voff, so);
  A[2] = ld128_sc1<0>(rsH, voff, so2); A[3] = ld128_sc1<1024>(rsH, voff, so2);
}
#define WAITA(A, N) asm volatile("s_waitcnt vmcnt(%4) lgkmcnt(0)" : "+v"((A)[0]), "+v"((A)[1]), "+v"((A)[2]), "+v"((A)[3]) : "i"(N) : "memory")
constexpr int p3wait(int s) {
  int issued = 2;
  for (int t = 0; t < s; t++) issued += t < 2 ? 2 : (t + 4 < 10 ? 1 : 0);
  return 4 * (issued - s - 1) + (s > 6 ? 3 : 0);
}

__global__ __launch_bounds__(512, 2) void fused_kernel(const u16* __restrict__ x, const u16* __restrict__ wqkv, const u16* __restrict__ wo,
                                                       const u16* __restrict__ w1, const u16* __restrict__ w2, u16* __restrict__ out,
                                                       char* __restrict__ ws) {
  extern __shared__ __attribute__((aligned(16))) char smem_[];
  LAS char* smem = (LAS char*)smem_;
  const unsigned lds0 = (unsigned)(size_t)smem;
  const int tid = threadIdx.x, lane = tid & 63, w = __builtin_amdgcn_readfirstlane(tid >> 6);
  const int bid = blockIdx.x, xcd = bid & 7, idx = bid >> 3, half = xcd & 1, j8 = idx & 7;
  const int grp = (xcd >> 1) * 4 + (idx >> 3), mem = half * 8 + j8;
  const int isffn = j8 >> 2, sub = half * 4 + (j8 & 3);
  STAMP(0);
  P1 c;
  c.rsA = mkrs(x, SEQ * DM * 2);
  c.rsB = mkrs(isffn ? w1 : wqkv, isffn ? DM * FF * 2 : DM * 3 * DM * 2);
  c.ldb2 = isffn ? FF * 2 : 3 * DM * 2;
  c.w = w; c.lane = lane; c.grp = grp; c.isffn = isffn; c.mem = mem;
  const int ftile0 = sub < 4 ? sub * 14 : 56 + (sub - 4) * 13;
  const int ntile = isffn ? (sub < 4 ? 14 : 13) : 15;
  {
    const int ar = lane >> 3, sw = ((lane & 7) ^ ((4 * w + (lane >> 4)) & 7)) << 4;
    c.avoff = (grp * 128 + 8 * w + ar) * (DM * 2) + sw;
    int hr = grp * 128 - 16 + 8 * w + ar; hr = hr < 0 ? 0 : hr;
    c.asvoff = hr * (DM * 2) + sw;
    const int kk = 2 * w + (lane >> 5), zz = (kk & 3) | (((kk >> 3) & 1) << 2), pc = lane & 31, seg = (pc >> 1) ^ zz;
    const int part = (seg >= 10) + (seg >= 5);
    const int col = isffn ? (ftile0 + seg) * 16 : part * (DM - 80) + sub * 80 + seg * 16;
    c.bvoff = seg < ntile ? kk * c.ldb2 + col * 2 + (pc & 1) * 16 : OOB;
  }
  c.halo = !isffn && w < 2;
  static_for<6>([&](auto I) { p1_req<decltype(I)::value>(c, lds0, 0); });
  c.rsH = mkrs(ws, H_BYTES);
  c.rsC = mkrs(ws + CTRL_OFF, 1 << 20);
  const int cg = (xcd >> 1) * 4 + half * 2 + (idx >> 4), rh = (idx >> 3) & 1, c3 = idx & 7;
  c.bt = ws + BT_OFF; c.bid = bid;
  c.rsT = mkrs(ws + BT_OFF + (size_t)c3 * BT_SLICE, BT_SLICE);
  c.wm = w >> 2;
  c.k0 = k0_of(w);
  c.tag = 0; c.taghi = 0;
  const int wn = w < 4 ? w : 7 - w;
  c.htile0 = 40 + ftile0;
  unsigned pre[40];
  v4u xs[10];
  const int sslot = w * 8 + (lane >> 3);
  const int kap = (7 - w) * 16 + grp;
  const bool dostat = isffn && w < 3, dotile = isffn && w >= 3 && kap < 74;
  if (dostat) {
    int rho = sub * 18 + (sslot < 18 ? sslot : 17);
    int row = grp * 128 - 16 + rho; row = row < 0 ? 0 : row;
    const int vo = row * (DM * 2) + (lane & 7) * 16;
    static_for<10>([&](auto I) { constexpr int i = decltype(I)::value; xs[i] = ld128<128 * i>(c.rsA, vo, 0); });
  } else if (dotile) {
    const v4i rw = mkrs(kap < 20 ? wo : w2, kap < 20 ? DM * DM * 2 : DM * FF * 2);
    int kq = kap;
    if (kap >= 20) asm volatile("s_sub_u32 %0, %0, 20" : "+s"(kq)::"scc");
    const int so = kq * 32 * (DM * 2);
    const int vo = (lane >> 4) * 4 * (DM * 2) + (sub * 80 + (lane & 15)) * 2;
    static_for<40>([&](auto I) { constexpr int i = decltype(I)::value, n = i >> 3, j = i & 7;
      pre[i] = ld16<(j & 3) * (DM * 2) + n * 32>(rw, vo, so + (j >> 2) * 16 * (DM * 2)); });
  }
  static_for<6>([&](auto I) { p1_req<decltype(I)::value>(c, lds0 + ST_BYTES, 1); });
  static_for<3>([&](auto I) { p1_req<decltype(I)::value>(c, lds0 + 2 * ST_BYTES, 2); });
  {
    u64 pc0; unsigned codelen;
    asm volatile("s_getpc_b64 %0\n.Lcode_begin_mark:\n\ts_mov_b32 %1, .Lcode_end_mark-.Lcode_begin_mark" : "=s"(pc0), "=s"(codelen));
    const int gw = (bid - 8) * 8 + w;
    if (bid < 8) { c.rsPf = mkrs((const void*)pc0, (int)codelen); c.pfso = w * 8192; }
    else if (gw < 300) { c.rsPf = mkrs(wqkv, DM * 3 * DM * 2); c.pfso = gw * 8192; }
    else if (gw < 570) { c.rsPf = mkrs(w1, DM * FF * 2); c.pfso = (gw - 300) * 8192; }
    else { c.rsPf = c.rsA; c.pfso = (gw - 570) * 8192; }
  }
  if (w == 7) dma16_sc1(c.rsC, lds0 + EPL_OFF, lane == 0 ? EPOCH_OFF : OOB, 0);
  if (dostat) {
    asm volatile("s_waitcnt vmcnt(9)" : "+v"(xs[0]), "+v"(xs[1]), "+v"(xs[2]), "+v"(xs[3]), "+v"(xs[4]), "+v"(xs[5]), "+v"(xs[6]), "+v"(xs[7]), "+v"(xs[8]), "+v"(xs[9])::"memory");
    float s = 0;
#pragma unroll
    for (int i = 0; i < 40; i++) s += bflo(xs[i >> 2][i & 3]) + bfhi(xs[i >> 2][i & 3]);
    const float mean = row8_sum(s) * (1.0f / DM);
    float v = 0;
#pragma unroll
    for (int i = 0; i < 40; i++) { const float a = bflo(xs[i >> 2][i & 3]) - mean, b = bfhi(xs[i >> 2][i & 3]) - mean; v += a * a + b * b; }
    const float var = row8_sum(v) * (1.0f / DM);
    const float sd = __builtin_sqrtf(rbf(rbf(var) + 1e-5f));
    v2u gr; gr.x = __builtin_bit_cast(unsigned, rbf(mean)); gr.y = __builtin_bit_cast(unsigned, rbf(sd));
    st64_sc1(c.rsC, ((lane & 7) == 0 && sslot < 18) ? STAT_OFF + grp * 2048 + (sub * 18 + sslot) * 8 : OOB, gr);
  } else if (dotile) {
    asm volatile("s_waitcnt vmcnt(9)" ::: "memory");
#pragma unroll
    for (int i = 0; i < 40; i += 10)
      asm volatile("" : "+v"(pre[i]), "+v"(pre[i + 1]), "+v"(pre[i + 2]), "+v"(pre[i + 3]), "+v"(pre[i + 4]), "+v"(pre[i + 5]), "+v"(pre[i + 6]), "+v"(pre[i + 7]), "+v"(pre[i + 8]), "+v"(pre[i + 9]));
#pragma unroll
    for (int n = 0; n < 5; n++) {
      v4u v;
#pragma unroll
      for (int e = 0; e < 4; e++) v[e] = pre[8 * n + 2 * e] | (pre[8 * n + 2 * e + 1] << 16);
      st128_sc1(mkrs(ws + BT_OFF, 8 * BT_SLICE), lane * 16, sub * BT_SLICE + (kap * 5 + n) * 1024, v);
    }
  }
  v4u B[6][5];
  {
    const int n4 = ntile - 12;
    c.tstart = wn <= n4 ? 4 * wn : 4 * n4 + 3 * (wn - n4);
    if (!isffn) c.ftile0 = 1 + 6 * c.wm;
    else { const int fi = (wn - n4) * 2 + c.wm; c.ftile0 = 1 + 2 * (n4 == 2 ? fi : (fi & 1)); }
    if (wn < n4) phase1<4, 1, 0>(c, smem, lds0, ws, B);
    else if (!isffn) phase1<3, 2, 3>(c, smem, lds0, ws, B);
    else phase1<3, 2, 1>(c, smem, lds0, ws, B);
  }
  STAMP(9);
  const int r = lane & 15, g = lane >> 4;
  const int k0 = c.k0;
  auto ldB = [&](int s) { SB; ldB5(B[s], c.rsT, lane * 16, (k0 + s) * 5120); SB; };
  if (isffn) { ldB(4); ldB(5); }
  if (!isffn) {
    const int row = (w & 1) * 64 + lane;
    const int pos = grp * 128 + row;
    LAS char* qp = smem + (row + 16) * IMG_LD + (w >> 1) * 40;
    unsigned qv[10];
#pragma unroll
    for (int i = 0; i < 5; i++) { const v2u t = *(LAS v2u*)(qp + i * 8); qv[2 * i] = t.x; qv[2 * i + 1] = t.y; }
    float lg[8];
#pragma unroll
    for (int k = 0; k < 8; k++) {
      if (k == 2) ldB(4);
      LAS char* kp = qp + 160 - 2 * k * IMG_LD;
      float a0 = 0, a1 = 0;
#pragma unroll
      for (int i = 0; i < 5; i++) { const v2u t = *(LAS v2u*)(kp + i * 8); a0 = dot2(qv[2 * i], t.x, a0); a1 = dot2(qv[2 * i + 1], t.y, a1); }
      lg[k] = pos - 2 * k >= 0 ? a0 + a1 : -4e9f;
    }
    ldB(5);
    float mx = lg[0];
#pragma unroll
    for (int k = 1; k < 8; k++) mx = fmaxf(mx, lg[k]);
    float sum = 0;
    constexpr float C = 0.22360679774997896f * 1.4426950408889634f;
#pragma unroll
    for (int k = 0; k < 8; k++) { lg[k] = __builtin_amdgcn_exp2f((lg[k] - mx) * C); sum += lg[k]; }
    const float inv = __builtin_amdgcn_rcpf(sum);
    float o[20];
#pragma unroll
    for (int d = 0; d < 20; d++) o[d] = 0;
#pragma unroll
    for (int k = 0; k < 8; k++) {
      const unsigned plo = pk2(lg[k] * inv, 0.f), phi = plo << 16;
      LAS char* vp = qp + 320 - 2 * k * IMG_LD;
#pragma unroll
      for (int i = 0; i < 5; i++) {
        const v2u t = *(LAS v2u*)(vp + i * 8);
        o[4 * i] = dot2(t.x, plo, o[4 * i]); o[4 * i + 1] = dot2(t.x, phi, o[4 * i + 1]);
        o[4 * i + 2] = dot2(t.y, plo, o[4 * i + 2]); o[4 * i + 3] = dot2(t.y, phi, o[4 * i + 3]);
      }
    }
#pragma unroll
    for (int i = 0; i < 5; i++) { v2u t; t.x = pk2(o[4 * i], o[4 * i + 1]); t.y = pk2(o[4 * i + 2], o[4 * i + 3]); *(LAS v2u*)(qp + i * 8) = t; }
    STAMP(11);
    ldsbar();
#pragma unroll
    for (int i = 0; i < 3; i++) {
      const int b0 = w + 8 * i, b = b0 < 20 ? b0 : 19, rp = b / 5, t = b - 5 * rp;
      LAS char* sp = smem + (16 + rp * 32 + r) * IMG_LD + t * 32 + g * 8;
      const v2u lo = *(LAS v2u*)sp, hi = *(LAS v2u*)(sp + 16 * IMG_LD);
      v4u v; v.x = lo.x; v.y = lo.y; v.z = hi.x; v.w = hi.y;
      st128_sc1(c.rsH, lane * 16, b0 < 20 ? ((grp * 4 + rp) * NCT + sub * 5 + t) * 1024 : OOB, v);
    }
    waitbar<0>();
  } else waitbar<10>();
  st32_sc1(c.rsC, tid == 0 ? FLAG_OFF + grp * 256 + mem * 4 : OOB, c.tag);
  STAMP(4);
  const int e_off = RING_OFF + w * 1024, l_off = w < 7 ? w * 10240 : CS_OFF;
#pragma unroll
  for (int e = 0; e < 10; e++) if (e < 5 || w < 2) dma16_sc1(c.rsT, lds0 + l_off + e * 1024, lane * 16, (k0 + 8) * 5120 + e * 1024);
  {
    int m;
    if (w < 2) m = lane < 4 ? w * 8 + lane : -1;
    else {
      const int t0 = 18 * (w - 2), t1 = t0 + 17;
      const int f0 = t0 < 56 ? t0 / 14 : 4 + (t0 - 56) / 13, f1 = t1 < 56 ? t1 / 14 : 4 + (t1 - 56) / 13, f = f0 + lane;
      m = f <= f1 ? (f >> 2) * 8 + 4 + (f & 3) : -1;
    }
    wait_flags(c, m >= 0 ? FLAG_OFF + cg * 256 + m * 4 : OOB);
  }
  STAMP(5);
  const int hso = ((cg * 4 + rh * 2) * NCT + 2 * k0) * 1024;
  v4u A[10][4];
  v2u xr[3];
  auto ldA = [&](int s) { ldA4(A[s], c.rsH, lane * 16, (s < 9 || w < 2) ? hso + s * 2048 : OOB); };
  ldA(0); ldA(1);
  f32x4 acc[4][5];
#pragma unroll
  for (int m = 0; m < 4; m++)
#pragma unroll
    for (int n = 0; n < 5; n++) acc[m][n] = f32x4{0, 0, 0, 0};
  static_for<10>([&](auto S) {
    constexpr int s = decltype(S)::value;
    WAITA(A[s], p3wait(s));
    if constexpr (s < 6) TIE5(B[s]);
    mma20(acc, B[s % 6], A[s]);
    SB;
    if constexpr (s < 2) { ldA(2 * s + 2); ldA(2 * s + 3); }
    else if constexpr (s + 4 < 10) ldA(s + 4);
    if constexpr (s == 6) {
#pragma unroll
      for (int i = 0; i < 3; i++) {
        const int tl = w + 8 * i, tt = tl < 20 ? tl : 19, m = tt / 5, n = tt - 5 * m;
        xr[i] = ld64(c.rsA, (cg * 128 + rh * 64 + m * 16 + r) * (DM * 2) + (c3 * 80 + n * 16 + g * 4) * 2, 0);
      }
    }
    if constexpr (s >= 4 && s < 8) {
#pragma unroll
      for (int n = 0; n < 5; n++) B[(s + 2) % 6][n] = *(LAS v4u*)(smem + (s < 6 ? e_off + ((s & 1) * 5 + n) * 8192 : l_off + ((s == 7 && w >= 2) ? 0 : (s & 1) * 5120) + n * 1024) + lane * 16);
    }
    SB;
  });
  STAMP(6);
  asm volatile("s_nop 7\n\ts_nop 3\n\ts_waitcnt vmcnt(0) lgkmcnt(0)" : "+v"(xr[0]), "+v"(xr[1]), "+v"(xr[2])::"memory");
#pragma unroll
  for (int m = 0; m < 4; m++)
#pragma unroll
    for (int n = 0; n < 5; n++) *(LAS f32x4*)(smem + (m < 2 ? e_off + (m * 5 + n) * 8192 : l_off + ((m & 1) * 5 + n) * 1024) + lane * 16) = acc[m][n];
  STAMP(12);
  ldsbar();
  STAMP(13);
  if (w == 0 && bid == 0) { v2u t; t.x = c.tag; t.y = c.taghi; st64_sc1(c.rsC, lane == 0 ? EPOCH_OFF : OOB, t); }
#pragma unroll
  for (int i = 0; i < 3; i++) {
    const int tl = w + 8 * i;
    if (tl < 20) {
      const int m = tl / 5, n = tl - 5 * m;
      LAS char* pp = smem + (tl < 10 ? RING_OFF + tl * 8192 : (tl - 10) * 1024) + lane * 16;
      const int ps = tl < 10 ? 1024 : 10240;
      const f32x4 s = *(LAS f32x4*)pp + *(LAS f32x4*)(pp + ps);
      const f32x4 f = ((*(LAS f32x4*)(pp + 2 * ps) + *(LAS f32x4*)(pp + 3 * ps)) + (*(LAS f32x4*)(pp + 4 * ps) + *(LAS f32x4*)(pp + 5 * ps))) +
                      (*(LAS f32x4*)(pp + 6 * ps) + *(LAS f32x4*)(pp + (tl < 10 ? 7 * 1024 : CS_OFF)));
      float y[4];
#pragma unroll
      for (int j = 0; j < 4; j++) {
        const float xv = (j & 1) ? bfhi(xr[i][j >> 1]) : bflo(xr[i][j >> 1]);
        y[j] = xv + s[j] * silu_f(f[j]);
      }
      v2u o; o.x = pk2(y[0], y[1]); o.y = pk2(y[2], y[3]);
      *(GAS v2u*)((GAS char*)out + (size_t)(cg * 128 + rh * 64 + m * 16 + r) * (DM * 2) + (c3 * 80 + n * 16 + g * 4) * 2) = o;
    }
  }
  STAMP(7);
  asm volatile(".Lcode_end_mark:");
}
}

extern "C" void kernel_launch(void* const* d_in, const int* in_sizes, int n_in, void* d_out, int out_size, void* d_ws, size_t ws_size,
                              hipStream_t stream) {
  (void)hipFuncSetAttribute((const void*)fused_kernel, hipFuncAttributeMaxDynamicSharedMemorySize, LDS_BYTES);
  fused_kernel<<<256, 512, LDS_BYTES, stream>>>((const u16*)d_in[0], (const u16*)d_in[1], (const u16*)d_in[2], (const u16*)d_in[4],
                                                (const u16*)d_in[5], (u16*)d_out, (char*)d_ws);
}
```

```cpp
#include <hip/hip_runtime.h>
#include <stdint.h>
#include <type_traits>
#include <utility>

#define GAS __attribute__((address_space(1)))
#define LAS __attribute__((address_space(3)))
typedef unsigned short u16;
typedef unsigned long long u64;
typedef __bf16 v2bf __attribute__((ext_vector_type(2)));
typedef __bf16 v4bf __attribute__((ext_vector_type(4)));
typedef __bf16 v8bf __attribute__((ext_vector_type(8)));
typedef float f32x2 __attribute__((ext_vector_type(2)));
typedef float f32x4 __attribute__((ext_vector_type(4)));
typedef unsigned v4u __attribute__((ext_vector_type(4)));
typedef unsigned v2u __attribute__((ext_vector_type(2)));
typedef int v4i __attribute__((ext_vector_type(4)));

namespace {
constexpr int SEQ = 2048, DM = 640, FF = 1728;
constexpr int NCT = 148;
constexpr int H_BYTES = 64 * NCT * 1024;
constexpr size_t CTRL_OFF = 16u << 20;
constexpr int EPOCH_OFF = 0, RDY_OFF = 2048, FLAG_OFF = 8192, STAT_OFF = 65536;
constexpr size_t BT_OFF = 12u << 20;
constexpr int BT_SLICE = 74 * 5120;
constexpr int A_BYTES = 144 * 128, B_BYTES = 64 * 512, ST_BYTES = A_BYTES + B_BYTES;
constexpr int CS_OFF = 3 * ST_BYTES, RS_OFF = CS_OFF + 1024;
constexpr int RDYL_OFF = CS_OFF + 6144, EPL_OFF = CS_OFF + 7168;
constexpr int DUMP_OFF = CS_OFF + 4096;
constexpr int IMG_LD = 488;
constexpr int RING_OFF = 71680;
constexpr int LDS_BYTES = 163840;
constexpr int OOB = 0x7FF00000;
constexpr unsigned SPIN_CAP = 1u << 16;
#define SGB 1
#define MAP3 1
#define POLL_SLEEP 8
#define STAMP(i)
#define SB __builtin_amdgcn_sched_barrier(0)
#define LDPOL2 " sc1"

__device__ __forceinline__ v4i mkrs(const void* p, int bytes) {
  u64 a = (u64)p; v4i r; r.x = (int)(unsigned)a; r.y = (int)((unsigned)(a >> 32) & 0xFFFFu); r.z = bytes; r.w = 0x00020000; return r;
}
__device__ __forceinline__ void dma16(v4i rs, unsigned lds, int voff, int soff) {
  asm volatile("s_mov_b32 m0, %0\n\ts_nop 0\n\tbuffer_load_dwordx4 %1, %2, %3 offen lds" ::"s"(lds), "v"(voff), "s"(rs), "s"(soff));
}
__device__ __forceinline__ void dma16_sc1(v4i rs, unsigned lds, int voff, int soff) {
  asm volatile("s_mov_b32 m0, %0\n\ts_nop 3\n\tbuffer_load_dwordx4 %1, %2, %3 offen" LDPOL2 " lds" ::"s"(lds), "v"(voff), "s"(rs), "s"(soff));
}
__device__ __forceinline__ void dma16_50(v4i rs, unsigned lds, int voff, int soff) {
  asm volatile("s_mov_b32 m0, %0\n\ts_mov_b32 exec_hi, 0x3ffff\n\tbuffer_load_dwordx4 %1, %2, %3 offen lds\n\ts_mov_b32 exec_hi, -1" ::"s"(lds), "v"(voff), "s"(rs), "s"(soff));
}
#define LDPOL " sc1"
#define STPOL " sc1"
template <int IMM> __device__ __forceinline__ v4u ld128_sc1(v4i rs, int voff, int soff) {
  v4u r; asm volatile("buffer_load_dwordx4 %0, %1, %2, %3 offen offset:%4" LDPOL : "=v"(r) : "v"(voff), "s"(rs), "s"(soff), "i"(IMM)); return r;
}
template <int IMM> __device__ __forceinline__ v4u ld128(v4i rs, int voff, int soff) {
  v4u r; asm volatile("buffer_load_dwordx4 %0, %1, %2, %3 offen offset:%4" : "=v"(r) : "v"(voff), "s"(rs), "s"(soff), "i"(IMM)); return r;
}
template <int IMM> __device__ __forceinline__ unsigned ld16(v4i rs, int voff, int soff) {
  unsigned r; asm volatile("buffer_load_ushort %0, %1, %2, %3 offen offset:%4" : "=v"(r) : "v"(voff), "s"(rs), "s"(soff), "i"(IMM)); return r;
}
__device__ __forceinline__ v2u ld64(v4i rs, int voff, int soff) {
  v2u r; asm volatile("buffer_load_dwordx2 %0, %1, %2, %3 offen" : "=v"(r) : "v"(voff), "s"(rs), "s"(soff)); return r;
}
__device__ __forceinline__ v2u ld64_sc1(v4i rs, int voff, int soff) {
  v2u r; asm volatile("buffer_load_dwordx2 %0, %1, %2, %3 offen sc1" : "=v"(r) : "v"(voff), "s"(rs), "s"(soff)); return r;
}
__device__ __forceinline__ v2u ld64_sc1_wait(v4i rs, int voff) {
  v2u r; asm volatile("buffer_load_dwordx2 %0, %1, %2, 0 offen sc1\n\ts_waitcnt vmcnt(0)" : "=&v"(r) : "v"(voff), "s"(rs) : "memory"); return r;
}
__device__ __forceinline__ void st128_sc1(v4i rs, int voff, int soff, v4u v) {
  asm volatile("buffer_store_dwordx4 %0, %1, %2, %3 offen" STPOL "\n\ts_nop 1" ::"v"(v), "v"(voff), "s"(rs), "s"(soff) : "memory");
}
__device__ __forceinline__ void st64_sc1(v4i rs, int voff, v2u v) {
  asm volatile("buffer_store_dwordx2 %0, %1, %2, 0 offen sc1" ::"v"(v), "v"(voff), "s"(rs) : "memory");
}
__device__ __forceinline__ void st32_sc1(v4i rs, int voff, unsigned v) {
  asm volatile("buffer_store_dword %0, %1, %2, 0 offen sc1" ::"v"(v), "v"(voff), "s"(rs) : "memory");
}
template <int N> __device__ __forceinline__ void waitbar() { asm volatile("s_waitcnt vmcnt(%0) lgkmcnt(0)\n\ts_barrier" ::"i"(N) : "memory"); }
__device__ __forceinline__ void ldsbar() { asm volatile("s_waitcnt lgkmcnt(0)\n\ts_barrier" ::: "memory"); }
__device__ __forceinline__ void drain() { asm volatile("s_waitcnt vmcnt(0)" ::: "memory"); }
__device__ __forceinline__ float bflo(unsigned u) { return __builtin_bit_cast(float, u << 16); }
__device__ __forceinline__ float bfhi(unsigned u) { return __builtin_bit_cast(float, u & 0xFFFF0000u); }
__device__ __forceinline__ unsigned pk2(float a, float b) {
  v2bf v; v.x = (__bf16)a; v.y = (__bf16)b; return __builtin_bit_cast(unsigned, v);
}
__device__ __forceinline__ float rbf(float a) { return (float)(__bf16)a; }
__device__ __forceinline__ float dot2(unsigned a, unsigned b, float c) {
  return __builtin_amdgcn_fdot2_f32_bf16(__builtin_bit_cast(v2bf, a), __builtin_bit_cast(v2bf, b), c, false);
}
__device__ __forceinline__ f32x4 mfma(v8bf a, v8bf b, f32x4 c) { return __builtin_amdgcn_mfma_f32_16x16x32_bf16(a, b, c, 0, 0, 0); }
__device__ __forceinline__ v8bf trfrag(LAS char* p, int second) {
  v4bf lo = __builtin_amdgcn_ds_read_tr16_b64_v4bf16((LAS v4bf*)p);
  v4bf hi = __builtin_amdgcn_ds_read_tr16_b64_v4bf16((LAS v4bf*)(p + second));
  return __builtin_shufflevector(lo, hi, 0, 1, 2, 3, 4, 5, 6, 7);
}
__device__ __forceinline__ float row8_sum(float v) {
  v += __builtin_bit_cast(float, __builtin_amdgcn_update_dpp(0, __builtin_bit_cast(int, v), 0xB1, 0xF, 0xF, false));
  v += __builtin_bit_cast(float, __builtin_amdgcn_update_dpp(0, __builtin_bit_cast(int, v), 0x4E, 0xF, 0xF, false));
  v += __builtin_bit_cast(float, __builtin_amdgcn_update_dpp(0, __builtin_bit_cast(int, v), 0x141, 0xF, 0xF, false));
  return v;
}
__device__ __forceinline__ float silu_f(float y) { return y * __builtin_amdgcn_rcpf(1.0f + __builtin_amdgcn_exp2f(y * -1.4426950408889634f)); }
template <class F, int... I> __device__ __forceinline__ void static_for_impl(F&& f, std::integer_sequence<int, I...>) { (f(std::integral_constant<int, I>{}), ...); }
template <int N, class F> __device__ __forceinline__ void static_for(F&& f) { static_for_impl(f, std::make_integer_sequence<int, N>{}); }

struct P1 {
  v4i rsA, rsB, rsH, rsC, rsT, rsPf;
  int avoff, asvoff, bvoff, ldb2;
  int pfso, bid;
  int w, wm, lane, grp, mem, rot;
  const char* bt;
  int tstart, ftile0;
  int isffn, htile0;
  unsigned tag, taghi;
  int k0, k0t, halo;
};
__device__ __forceinline__ int hk0(int hw) { return 20 + 14 * hw - ((hw + 1) >> 2); }

template <int I> __device__ __forceinline__ void p1_req(const P1& c, unsigned base, int st) {
  if (I == 0 && c.halo) dma16(c.rsA, base + c.w * 1024, c.asvoff, st * 128);
  if (I < 2) dma16(c.rsA, base + (2 + c.w + 8 * I) * 1024, c.avoff, st * 128 + I * 64 * (DM * 2));
  else dma16(c.rsB, base + A_BYTES + (c.w + 8 * (I - 2)) * 1024, c.bvoff, (st * 64 + (I - 2) * 16) * c.ldb2);
}
template <int E> __device__ __forceinline__ void tail_req(const P1& c, unsigned lds0) {
  dma16_sc1(c.rsT, lds0 + RING_OFF + E * 8192 + c.w * 1024, c.lane * 16, c.k0t * 5120 + E * 1024);
}

__device__ __forceinline__ void ldB5(v4u (&B)[5], v4i rsT, int voff, int so) {
  const int so2 = so == OOB ? OOB : so + 4096;
  B[0] = ld128_sc1<0>(rsT, voff, so); B[1] = ld128_sc1<1024>(rsT, voff, so); B[2] = ld128_sc1<2048>(rsT, voff, so);
  B[3] = ld128_sc1<3072>(rsT, voff, so); B[4] = ld128_sc1<0>(rsT, voff, so2);
}
#define TIE5(B) asm volatile("" : "+v"((B)[0]), "+v"((B)[1]), "+v"((B)[2]), "+v"((B)[3]), "+v"((B)[4]))

template <int NT, int NOWN, int NFOR>
__device__ __forceinline__ void phase1(P1& c, LAS char* smem, unsigned lds0, char* ws, v4u (&B)[5][5]) {
  constexpr int NB = NT + NFOR, NX = NOWN + NFOR;
  const int lane = c.lane, r = lane & 15, g = lane >> 4, q = r >> 2, p = lane & 3;
  int aad[2];
#pragma unroll
  for (int ks = 0; ks < 2; ks++) aad[ks] = r * 128 + (((4 * ks + g) ^ (r >> 1)) << 4);
  const int amain = (1 + c.wm * 4) * 2048;
  const int z = q | ((g & 1) << 2);
  int tile[NB], bad[NB];
#pragma unroll
  for (int n = 0; n < NB; n++) {
    if (n < NT) { int t = n + 2 * c.wm; t = t >= NT ? t - NT : t; tile[n] = c.tstart + t; }
    else tile[n] = c.ftile0 + 2 * (n - NT);
    bad[n] = A_BYTES + (8 * g + q) * 512 + (((tile[n] & 7) ^ z) << 5) + (tile[n] >> 3) * 256 + p * 8;
  }
  f32x4 acc[4][NT], accx[NX];
#pragma unroll
  for (int m = 0; m < 4; m++)
#pragma unroll
    for (int n = 0; n < NT; n++) acc[m][n] = f32x4{0, 0, 0, 0};
#pragma unroll
  for (int n = 0; n < NX; n++) accx[n] = f32x4{0, 0, 0, 0};
  v8bf af[2][4], as[2], bf[2][NB];
  const bool ones = (r == 0) || c.isffn;
  auto ldq = [&](int buf, LAS char* base, int ks, int i) {
    af[buf][i] = *(LAS v8bf*)(base + aad[ks] + amain + i * 2048);
#pragma unroll
    for (int n = 0; n < NB; n++) if ((n & 3) == i) bf[buf][n] = trfrag(base + bad[n] + ks * 16384, 2048);
    if (i == 3) as[buf] = *(LAS v8bf*)(base + aad[ks]);
  };
  auto row = [&](int buf, int m) {
#pragma unroll
    for (int n = 0; n < NT; n++) acc[m][n] = mfma(bf[buf][n], af[buf][m], acc[m][n]);
    if (m == 3) {
      v4u s = __builtin_bit_cast(v4u, as[buf]);
#pragma unroll
      for (int e = 0; e < 4; e++) s[e] = ones ? 0x3F803F80u : s[e];
      v8bf sv = __builtin_bit_cast(v8bf, s);
#pragma unroll
      for (int n = 0; n < NX; n++) accx[n] = mfma(bf[buf][n < NOWN ? n : NT + n - NOWN], sv, accx[n]);
    }
#pragma unroll
    for (int i = 0; i < NT; i++) {
      __builtin_amdgcn_sched_group_barrier(0x8, 1, 0); __builtin_amdgcn_sched_group_barrier(0x100, 2, 0); __builtin_amdgcn_sched_group_barrier(0x2, 1, 0);
    }
    SB;
  };
  auto halfstage = [&](int buf, bool rd, LAS char* rbase, int rks, auto&& rq0, auto&& rq1, auto&& rq2) {
    if (rd) { ldq(buf ^ 1, rbase, rks, 0); ldq(buf ^ 1, rbase, rks, 1); }
    row(buf, 0); rq0(); SB;
    if (rd) ldq(buf ^ 1, rbase, rks, 2);
    row(buf, 1); rq1(); SB;
    if (rd) ldq(buf ^ 1, rbase, rks, 3);
    row(buf, 2); rq2(); SB;
    row(buf, 3);
  };
  auto none = [&]() {};
  auto lst = [&](int i) {};
#pragma unroll
  for (int m = 0; m < 4; m++)
#pragma unroll
    for (int n = 0; n < NT; n++) asm volatile("" : "+v"(acc[m][n]));
#pragma unroll
  for (int n = 0; n < NX; n++) asm volatile("" : "+v"(accx[n]));
#pragma unroll
  for (int n = 0; n < NB; n++) asm volatile("" : "+v"(bad[n]));
  asm volatile("" : "+v"(aad[0]), "+v"(aad[1]));
  waitbar<9>();
  lst(0);
  STAMP(1);
  unsigned pfd = 0;
  auto special = [&](int st) {
    if (st == 0)
      asm volatile("buffer_load_dword %0, %1, %2, %3 offen" : "+v"(pfd) : "v"(lane * 128), "s"(c.rsPf), "s"(c.pfso));
    else if (st == 2) {
      const v2u ep = *(LAS v2u*)(smem + EPL_OFF);
      const u64 e = ((u64)(unsigned)__builtin_amdgcn_readfirstlane(ep.y) << 32) | (unsigned)__builtin_amdgcn_readfirstlane(ep.x);
      const u64 t = e * 0x5851F42D4C957F2Dull + 0x14057B7EF767814Full;
      c.tag = (unsigned)t; c.taghi = (unsigned)(t >> 32);
      if (c.w == 0) st32_sc1(c.rsC, lane == 0 ? RDY_OFF + c.bid * 4 : OOB, c.tag);
    } else if (st == 5) {
      if (c.w == 0) dma16_sc1(c.rsC, lds0 + RDYL_OFF, lane * 16, RDY_OFF);
    } else if (st == 6) {
      v4u rdy = *(LAS v4u*)(smem + RDYL_OFF + lane * 16);
      for (unsigned spins = 0;; spins++) {
        if (__all(rdy.x == c.tag && rdy.y == c.tag && rdy.z == c.tag && rdy.w == c.tag) || spins > SPIN_CAP) break;
        __builtin_amdgcn_s_sleep(POLL_SLEEP);
        asm volatile("buffer_load_dwordx4 %0, %1, %2, 0 offen offset:%3 sc1\n\ts_waitcnt vmcnt(0)" : "=&v"(rdy) : "v"(lane * 16), "s"(c.rsC), "i"(RDY_OFF) : "memory");
      }
      if (c.w < 2) dma16_sc1(c.rsC, lds0 + RS_OFF + c.w * 1024, lane * 16, STAT_OFF + c.grp * 2048 + c.w * 1024);
    }
  };
#pragma unroll
  for (int i = 0; i < 4; i++) ldq(0, smem, 0, i);
  int so = 0, po = 2 * ST_BYTES;
#pragma nounroll
  for (int st = 0; st < 7; st++) {
    const int no = so == 2 * ST_BYTES ? 0 : so + ST_BYTES;
    halfstage(0, true, smem + so, 1, [&]() { p1_req<3>(c, lds0 + po, st + 2); }, [&]() { p1_req<4>(c, lds0 + po, st + 2); }, [&]() { p1_req<5>(c, lds0 + po, st + 2); });
    waitbar<6>();
    SB;
    lst(st + 1);
    if ((0x65 >> st) & 1) special(st);
    SB;
    halfstage(1, true, smem + no, 0, [&]() { p1_req<0>(c, lds0 + so, st + 3); }, [&]() { p1_req<1>(c, lds0 + so, st + 3); }, [&]() { p1_req<2>(c, lds0 + so, st + 3); });
    po = so; so = no;
  }
  halfstage(0, true, smem + ST_BYTES, 1, [&]() { p1_req<3>(c, lds0, 9); }, [&]() { p1_req<4>(c, lds0, 9); }, [&]() { p1_req<5>(c, lds0, 9); });
  waitbar<6>(); SB; lst(8);
  halfstage(1, true, smem + 2 * ST_BYTES, 0, [&]() { tail_req<0>(c, lds0); }, [&]() { tail_req<1>(c, lds0); }, [&]() { tail_req<2>(c, lds0); });
  halfstage(0, true, smem + 2 * ST_BYTES, 1, none, none, none);
  waitbar<3>(); SB; lst(9);
  halfstage(1, true, smem, 0, [&]() { tail_req<3>(c, lds0); }, [&]() { tail_req<4>(c, lds0); }, [&]() { tail_req<5>(c, lds0); });
  halfstage(0, true, smem, 1, [&]() { tail_req<6>(c, lds0); }, [&]() { tail_req<7>(c, lds0); }, [&]() { tail_req<8>(c, lds0); });
  halfstage(1, false, smem, 0, [&]() { tail_req<9>(c, lds0); }, none, none);
  lst(10);
  STAMP(2);
#pragma unroll
  for (int n = 0; n < NX; n++)
    *(LAS f32x4*)(smem + (r == 0 ? CS_OFF + (tile[n < NOWN ? n : NT + n - NOWN] * 16 + 4 * g) * 4 : DUMP_OFF + lane * 16)) = accx[n];
  ldsbar();
  asm volatile("" ::"v"(pfd));
  STAMP(8);
  f32x2 rs[4];
#pragma unroll
  for (int m = 0; m < 4; m++) rs[m] = *(LAS f32x2*)(smem + RS_OFF + (16 + c.wm * 64 + m * 16 + r) * 8);
  auto ldB = [&](int s) { SB; ldB5(B[s], c.rsT, lane * 16, (c.k0 + s) * 5120); SB; };
  if (c.isffn) {
    float c1[4];
#pragma unroll
    for (int m = 0; m < 4; m++) c1[m] = __builtin_amdgcn_rcpf(rs[m].y) * -1.4426950408889634f;
#pragma unroll
    for (int n = 0; n < NT; n++) {
      const f32x4 cs = *(LAS f32x4*)(smem + CS_OFF + (tile[n] * 16 + 4 * g) * 4);
#pragma unroll
      for (int mp = 0; mp < 2; mp++) {
        v4u o;
#pragma unroll
        for (int h = 0; h < 2; h++) {
          const int m = 2 * mp + h;
          const f32x4 t = acc[m][n] - rs[m].x * cs;
          const f32x4 zz = t * c1[m];
          f32x4 e;
#pragma unroll
          for (int j = 0; j < 4; j++) e[j] = __builtin_amdgcn_exp2f(zz[j]);
          const f32x4 d = e * rs[m].y + rs[m].y;
          f32x4 rr;
#pragma unroll
          for (int j = 0; j < 4; j++) rr[j] = __builtin_amdgcn_rcpf(d[j]);
          const f32x4 y = t * rr;
          o[2 * h] = pk2(y[0], y[1]); o[2 * h + 1] = pk2(y[2], y[3]);
        }
        st128_sc1(c.rsH, lane * 16, ((c.grp * 4 + c.wm * 2 + mp) * NCT + c.htile0 + tile[n]) * 1024, o);
      }
    }
  } else {
    if (NT == 3) ldB(3);
    float ri[4];
#pragma unroll
    for (int m = 0; m < 4; m++) ri[m] = __builtin_amdgcn_rcpf(rs[m].y);
#pragma unroll
    for (int n = 0; n < NT; n++) {
      ldB(n);
      const f32x4 cs = *(LAS f32x4*)(smem + CS_OFF + (tile[n] * 16 + 4 * g) * 4);
#pragma unroll
      for (int m = 0; m < 4; m++) {
        const f32x4 y = (acc[m][n] - rs[m].x * cs) * ri[m];
        v2u o; o.x = pk2(y[0], y[1]); o.y = pk2(y[2], y[3]);
        *(LAS v2u*)(smem + (16 + c.wm * 64 + m * 16 + r) * IMG_LD + tile[n] * 32 + g * 8) = o;
      }
    }
    const f32x2 rh = *(LAS f32x2*)(smem + RS_OFF + r * 8);
    const float rih = __builtin_amdgcn_rcpf(rh.y);
#pragma unroll
    for (int n = 0; n < NX; n++) {
      const int t = tile[n < NOWN ? n : NT + n - NOWN];
      const f32x4 cs = *(LAS f32x4*)(smem + CS_OFF + (t * 16 + 4 * g) * 4);
      const f32x4 y = (accx[n] - rh.x * cs) * rih;
      v2u o; o.x = pk2(y[0], y[1]); o.y = pk2(y[2], y[3]);
      *(LAS v2u*)(smem + r * IMG_LD + t * 32 + g * 8) = o;
    }
    ldsbar();
  }
}

__device__ __forceinline__ void wait_flags(const P1& c, int off) {
  for (unsigned spins = 0;; spins++) {
    unsigned f;
    asm volatile("buffer_load_dword %0, %1, %2, 0 offen sc1\n\ts_waitcnt vmcnt(0)" : "=&v"(f) : "v"(off), "s"(c.rsC) : "memory");
    if (__all(off == OOB || f == c.tag) || spins > SPIN_CAP) break;
    __builtin_amdgcn_s_sleep(POLL_SLEEP);
  }
}
__device__ __forceinline__ void mfma_ip(f32x4& acc, const v4u& b, const v4u& a) {
  asm volatile("v_mfma_f32_16x16x32_bf16 %0, %1, %2, %0" : "+v"(acc) : "v"(b), "v"(a));
}
__device__ __forceinline__ void swap_pair(v4u& l0, v4u& l1) {
  unsigned a = l0.z, b = l1.x, c = l0.w, d = l1.y;
  asm volatile("v_swap_b32 %0, %1" : "+v"(a), "+v"(b));
  l0.z = a; l1.x = b;
  asm volatile("v_swap_b32 %0, %1" : "+v"(c), "+v"(d));
  l0.w = c; l1.y = d;
}
template <int I> __device__ __forceinline__ v4u ldA1(v4i rsH, int voff, int so) {
  return ld128_sc1<(I & 1) * 1024>(rsH, voff, I < 2 ? so : so + NCT * 1024);
}
template <int I> __device__ __forceinline__ v4u ldB1(v4i rsT, int voff, int so) {
  return ld128_sc1<(I & 3) * 1024>(rsT, voff, I < 4 ? so : so + 4096);
}
__device__ __forceinline__ void ldA4(v4u (&A)[4], v4i rsH, int voff, int so) {
  const int so2 = so == OOB ? OOB : so + NCT * 1024;
  A[0] = ld128_sc1<0>(rsH, voff, so); A[1] = ld128_sc1<1024>(rsH, voff, so);
  A[2] = ld128_sc1<0>(rsH, voff, so2); A[3] = ld128_sc1<1024>(rsH, voff, so2);
}
#define WAITA(A, N) asm volatile("s_waitcnt vmcnt(%4) lgkmcnt(0)" : "+v"((A)[0]), "+v"((A)[1]), "+v"((A)[2]), "+v"((A)[3]) : "i"(N) : "memory")
constexpr int h_na(int t) { return t == 1 ? 2 : (t + 3 < 14 ? 1 : 0); }
constexpr int h_a0(int t) { int n = 2; for (int i = 0; i < t; i++) n += h_na(i); return n; }
constexpr int hcnt(int t) { return 4 * h_na(t) + ((t >= 6 && t <= 10) ? 5 : 0) + (t == 8 ? 3 : 0); }
constexpr int hwait(int s) {
  int n = 0;
  if (s >= 9) { n = (s - 3 == 8 ? 3 : 0); for (int t = s - 2; t < s; t++) n += hcnt(t); return n; }
  if (s < 2) { n = 4 * (1 - s); for (int t = 0; t < s; t++) n += hcnt(t); return n; }
  int t0 = 0; while (h_a0(t0) + h_na(t0) <= s) t0++;
  n = hcnt(t0) - 4 * (s - h_a0(t0) + 1);
  for (int t = t0 + 1; t < s; t++) n += hcnt(t);
  return n;
}

__global__ __launch_bounds__(512, 2) void fused_kernel(const u16* __restrict__ x, const u16* __restrict__ wqkv, const u16* __restrict__ wo,
                                                       const u16* __restrict__ w1, const u16* __restrict__ w2, u16* __restrict__ out,
                                                       char* __restrict__ ws) {
  extern __shared__ __attribute__((aligned(16))) char smem_[];
  LAS char* smem = (LAS char*)smem_;
  const unsigned lds0 = (unsigned)(size_t)smem;
  const int tid = threadIdx.x, lane = tid & 63, w = __builtin_amdgcn_readfirstlane(tid >> 6);
  const int bid = blockIdx.x, xcd = bid & 7, idx = bid >> 3, half = xcd & 1, j8 = idx & 7;
  const int grp = (xcd >> 1) * 4 + (idx >> 3), mem = half * 8 + j8;
  const int isffn = j8 >> 2, sub = half * 4 + (j8 & 3);
  STAMP(0);
  u64 pc0; unsigned codelen, cpf = 0;
  asm volatile("s_getpc_b64 %0\n.Lcode_begin_mark:\n\ts_mov_b32 %1, .Lcode_end_mark-.Lcode_begin_mark" : "=s"(pc0), "=s"(codelen));
  if ((idx & 7) == 0) {
    const v4i rp = mkrs((const void*)pc0, (int)codelen);
    asm volatile("buffer_load_dword %0, %1, %2, %3 offen" : "=v"(cpf) : "v"(lane * 128), "s"(rp), "s"(w * 8192));
  }
  P1 c;
  c.rsA = mkrs(x, SEQ * DM * 2);
  c.rsB = mkrs(isffn ? w1 : wqkv, isffn ? DM * FF * 2 : DM * 3 * DM * 2);
  c.ldb2 = isffn ? FF * 2 : 3 * DM * 2;
  c.w = w; c.lane = lane; c.grp = grp; c.isffn = isffn; c.mem = mem;
  const int ftile0 = sub < 4 ? sub * 14 : 56 + (sub - 4) * 13;
  const int ntile = isffn ? (sub < 4 ? 14 : 13) : 15;
  {
    const int ar = lane >> 3, sw = ((lane & 7) ^ ((4 * w + (lane >> 4)) & 7)) << 4;
    c.avoff = (grp * 128 + 8 * w + ar) * (DM * 2) + sw;
    int hr = grp * 128 - 16 + 8 * w + ar; hr = hr < 0 ? 0 : hr;
    c.asvoff = hr * (DM * 2) + sw;
    const int kk = 2 * w + (lane >> 5), zz = (kk & 3) | (((kk >> 3) & 1) << 2), pc = lane & 31, seg = (pc >> 1) ^ zz;
    const int part = (seg >= 10) + (seg >= 5);
    const int col = isffn ? (ftile0 + seg) * 16 : part * (DM - 80) + sub * 80 + seg * 16;
    c.bvoff = seg < ntile ? kk * c.ldb2 + col * 2 + (pc & 1) * 16 : OOB;
  }
  c.halo = !isffn && w < 2;
  static_for<6>([&](auto I) { p1_req<decltype(I)::value>(c, lds0, 0); });
  c.rsH = mkrs(ws, H_BYTES);
  c.rsC = mkrs(ws + CTRL_OFF, 1 << 20);
  const int cg = (xcd >> 1) * 4 + half * 2 + (idx >> 4), rh = (idx >> 3) & 1, c3 = idx & 7;
  c.bt = ws + BT_OFF; c.bid = bid;
  c.rsT = mkrs(ws + BT_OFF + (size_t)c3 * BT_SLICE, BT_SLICE);
  c.wm = w >> 2;
  c.k0 = w < 4 ? 5 * w : hk0(w & 3);
  c.k0t = hk0(w & 3) + 5 + 2 * (w >> 2);
  c.tag = 0; c.taghi = 0;
  const int wn = w < 4 ? w : 7 - w;
  c.htile0 = 40 + ftile0;
  unsigned pre[40];
  v4u xs[10];
  const int sslot = w * 8 + (lane >> 3);
  const int kap = (7 - w) * 16 + grp;
  const bool dostat = isffn && w < 3, dotile = isffn && w >= 3 && kap < 74;
  if (dostat) {
    int rho = sub * 18 + (sslot < 18 ? sslot : 17);
    int row = grp * 128 - 16 + rho; row = row < 0 ? 0 : row;
    const int vo = row * (DM * 2) + (lane & 7) * 16;
    static_for<10>([&](auto I) { constexpr int i = decltype(I)::value; xs[i] = ld128<128 * i>(c.rsA, vo, 0); });
  } else if (dotile) {
    const v4i rw = mkrs(kap < 20 ? wo : w2, kap < 20 ? DM * DM * 2 : DM * FF * 2);
    int kq = kap;
    if (kap >= 20) asm volatile("s_sub_u32 %0, %0, 20" : "+s"(kq)::"scc");
    const int so = kq * 32 * (DM * 2);
    const int vo = (lane >> 4) * 4 * (DM * 2) + (sub * 80 + (lane & 15)) * 2;
    static_for<40>([&](auto I) { constexpr int i = decltype(I)::value, n = i >> 3, j = i & 7;
      pre[i] = ld16<(j & 3) * (DM * 2) + n * 32>(rw, vo, so + (j >> 2) * 16 * (DM * 2)); });
  }
  static_for<6>([&](auto I) { p1_req<decltype(I)::value>(c, lds0 + ST_BYTES, 1); });
  static_for<3>([&](auto I) { p1_req<decltype(I)::value>(c, lds0 + 2 * ST_BYTES, 2); });
  {
    const int gw = bid * 8 + w;
    if (gw < 300) { c.rsPf = mkrs(wqkv, DM * 3 * DM * 2); c.pfso = gw * 8192; }
    else if (gw < 570) { c.rsPf = mkrs(w1, DM * FF * 2); c.pfso = (gw - 300) * 8192; }
    else { c.rsPf = c.rsA; c.pfso = (gw - 570) * 8192; }
  }
  if (w == 7) dma16_sc1(c.rsC, lds0 + EPL_OFF, lane == 0 ? EPOCH_OFF : OOB, 0);
  if (dostat) {
    asm volatile("s_waitcnt vmcnt(9)" : "+v"(xs[0]), "+v"(xs[1]), "+v"(xs[2]), "+v"(xs[3]), "+v"(xs[4]), "+v"(xs[5]), "+v"(xs[6]), "+v"(xs[7]), "+v"(xs[8]), "+v"(xs[9])::"memory");
    float s = 0;
#pragma unroll
    for (int i = 0; i < 40; i++) s += bflo(xs[i >> 2][i & 3]) + bfhi(xs[i >> 2][i & 3]);
    const float mean = row8_sum(s) * (1.0f / DM);
    float v = 0;
#pragma unroll
    for (int i = 0; i < 40; i++) { const float a = bflo(xs[i >> 2][i & 3]) - mean, b = bfhi(xs[i >> 2][i & 3]) - mean; v += a * a + b * b; }
    const float var = row8_sum(v) * (1.0f / DM);
    const float sd = __builtin_sqrtf(rbf(rbf(var) + 1e-5f));
    v2u gr; gr.x = __builtin_bit_cast(unsigned, rbf(mean)); gr.y = __builtin_bit_cast(unsigned, rbf(sd));
    st64_sc1(c.rsC, ((lane & 7) == 0 && sslot < 18) ? STAT_OFF + grp * 2048 + (sub * 18 + sslot) * 8 : OOB, gr);
  } else if (dotile) {
    asm volatile("s_waitcnt vmcnt(9)" ::: "memory");
#pragma unroll
    for (int i = 0; i < 40; i += 10)
      asm volatile("" : "+v"(pre[i]), "+v"(pre[i + 1]), "+v"(pre[i + 2]), "+v"(pre[i + 3]), "+v"(pre[i + 4]), "+v"(pre[i + 5]), "+v"(pre[i + 6]), "+v"(pre[i + 7]), "+v"(pre[i + 8]), "+v"(pre[i + 9]));
#pragma unroll
    for (int n = 0; n < 5; n++) {
      v4u v;
#pragma unroll
      for (int e = 0; e < 4; e++) v[e] = pre[8 * n + 2 * e] | (pre[8 * n + 2 * e + 1] << 16);
      st128_sc1(mkrs(ws + BT_OFF, 8 * BT_SLICE), lane * 16, sub * BT_SLICE + (kap * 5 + n) * 1024, v);
    }
  }
  asm volatile("s_waitcnt vmcnt(9)" : "+v"(cpf)::"memory");
  v4u B[5][5];
  {
    const int n4 = ntile - 12;
    c.tstart = wn <= n4 ? 4 * wn : 4 * n4 + 3 * (wn - n4);
    if (!isffn) c.ftile0 = 1 + 6 * c.wm;
    else { const int fi = (wn - n4) * 2 + c.wm; c.ftile0 = 1 + 2 * (n4 == 2 ? fi : (fi & 1)); }
    if (wn < n4) phase1<4, 1, 0>(c, smem, lds0, ws, B);
    else if (!isffn) phase1<3, 2, 3>(c, smem, lds0, ws, B);
    else phase1<3, 2, 1>(c, smem, lds0, ws, B);
  }
  STAMP(9);
  const int r = lane & 15, g = lane >> 4;
  const int k0 = c.k0;
  auto ldB = [&](int s) { SB; ldB5(B[s], c.rsT, lane * 16, (k0 + s) * 5120); SB; };
  if (!isffn) {
    const int row = (w & 1) * 64 + lane;
    const int pos = grp * 128 + row;
    LAS char* qp = smem + (row + 16) * IMG_LD + (w >> 1) * 40;
    unsigned qv[10];
#pragma unroll
    for (int i = 0; i < 5; i++) { const v2u t = *(LAS v2u*)(qp + i * 8); qv[2 * i] = t.x; qv[2 * i + 1] = t.y; }
    float lg[8];
#pragma unroll
    for (int k = 0; k < 8; k++) {
      if (k == 2) ldB(4);
      LAS char* kp = qp + 160 - 2 * k * IMG_LD;
      float a0 = 0, a1 = 0;
#pragma unroll
      for (int i = 0; i < 5; i++) { const v2u t = *(LAS v2u*)(kp + i * 8); a0 = dot2(qv[2 * i], t.x, a0); a1 = dot2(qv[2 * i + 1], t.y, a1); }
      lg[k] = pos - 2 * k >= 0 ? a0 + a1 : -4e9f;
    }
    float mx = lg[0];
#pragma unroll
    for (int k = 1; k < 8; k++) mx = fmaxf(mx, lg[k]);
    float sum = 0;
    constexpr float C = 0.22360679774997896f * 1.4426950408889634f;
#pragma unroll
    for (int k = 0; k < 8; k++) { lg[k] = __builtin_amdgcn_exp2f((lg[k] - mx) * C); sum += lg[k]; }
    const float inv = __builtin_amdgcn_rcpf(sum);
    float o[20];
#pragma unroll
    for (int d = 0; d < 20; d++) o[d] = 0;
#pragma unroll
    for (int k = 0; k < 8; k++) {
      const unsigned plo = pk2(lg[k] * inv, 0.f), phi = plo << 16;
      LAS char* vp = qp + 320 - 2 * k * IMG_LD;
#pragma unroll
      for (int i = 0; i < 5; i++) {
        const v2u t = *(LAS v2u*)(vp + i * 8);
        o[4 * i] = dot2(t.x, plo, o[4 * i]); o[4 * i + 1] = dot2(t.x, phi, o[4 * i + 1]);
        o[4 * i + 2] = dot2(t.y, plo, o[4 * i + 2]); o[4 * i + 3] = dot2(t.y, phi, o[4 * i + 3]);
      }
    }
#pragma unroll
    for (int i = 0; i < 5; i++) { v2u t; t.x = pk2(o[4 * i], o[4 * i + 1]); t.y = pk2(o[4 * i + 2], o[4 * i + 3]); *(LAS v2u*)(qp + i * 8) = t; }
    STAMP(11);
    ldsbar();
#pragma unroll
    for (int i = 0; i < 3; i++) {
      const int b0 = w + 8 * i, b = b0 < 20 ? b0 : 19, rp = b / 5, t = b - 5 * rp;
      LAS char* sp = smem + (16 + rp * 32 + r) * IMG_LD + t * 32 + g * 8;
      const v2u lo = *(LAS v2u*)sp, hi = *(LAS v2u*)(sp + 16 * IMG_LD);
      v4u v; v.x = lo.x; v.y = lo.y; v.z = hi.x; v.w = hi.y;
      st128_sc1(c.rsH, lane * 16, b0 < 20 ? ((grp * 4 + rp) * NCT + sub * 5 + t) * 1024 : OOB, v);
    }
  }
  waitbar<0>();
  st32_sc1(c.rsC, tid == 0 ? FLAG_OFF + grp * 256 + mem * 4 : OOB, c.tag);
  STAMP(4);
  if (isffn) {
    if (w < 4) __builtin_amdgcn_s_sleep(40);
    ldB(0); ldB(1); ldB(2); ldB(3); ldB(4);
  }
  {
    int m;
    if (w < 4) m = lane < 2 ? (w >> 1) * 8 + (w & 1) * 2 + lane : -1;
    else {
      const int t0 = 2 * (k0 - 20), t1 = t0 + 27 - (w & 2);
      const int f0 = t0 < 56 ? t0 / 14 : 4 + (t0 - 56) / 13, f1 = t1 < 56 ? t1 / 14 : 4 + (t1 - 56) / 13, f = f0 + lane;
      m = f <= f1 ? (f >> 2) * 8 + 4 + (f & 3) : -1;
    }
    wait_flags(c, m >= 0 ? FLAG_OFF + cg * 256 + m * 4 : OOB);
  }
  STAMP(5);
  const int hso = ((cg * 4 + rh * 2) * NCT + 2 * k0) * 1024;
  v2u xr[3];
  auto ldx = [&]() {
#pragma unroll
    for (int i = 0; i < 3; i++) {
      const int tl = w + 8 * i, tt = tl < 20 ? tl : 19, m = tt / 5, n = tt - 5 * m;
      xr[i] = ld64(c.rsA, (cg * 128 + rh * 64 + m * 16 + r) * (DM * 2) + (c3 * 80 + n * 16 + g * 4) * 2, 0);
    }
  };
  auto put = [&](const f32x4 (&acc)[4][5], int p0, int p1, int stride) {
    STAMP(6);
    asm volatile("s_nop 7\n\ts_nop 3\n\ts_waitcnt vmcnt(0) lgkmcnt(0)" : "+v"(xr[0]), "+v"(xr[1]), "+v"(xr[2])::"memory");
#pragma unroll
    for (int m = 0; m < 4; m++)
#pragma unroll
      for (int n = 0; n < 5; n++) *(LAS f32x4*)(smem + (m < 2 ? p0 : p1) + ((m & 1) * 5 + n) * stride + lane * 16) = acc[m][n];
  };
  const int vo = lane * 16;
  if (w < 4) {
    v4u A[5][4];
    auto ldA = [&](int s) { ldA4(A[s], c.rsH, vo, hso + s * 2048); };
    ldA(0); ldA(1); ldA(2);
    f32x4 acc[4][5];
#pragma unroll
    for (int m = 0; m < 4; m++)
#pragma unroll
      for (int n = 0; n < 5; n++) acc[m][n] = f32x4{0, 0, 0, 0};
#pragma unroll
    for (int m = 0; m < 4; m++) TIE5(acc[m]);
#pragma unroll
    for (int s = 0; s < 5; s++) TIE5(B[s]);
    WAITA(A[0], 8); swap_pair(A[0][0], A[0][1]); swap_pair(A[0][2], A[0][3]);
    asm volatile("s_nop 1");
    static_for<5>([&](auto S) {
      constexpr int s = decltype(S)::value;
      static_for<20>([&](auto I) {
        constexpr int i = decltype(I)::value;
        mfma_ip(acc[i / 5][i % 5], B[s][i % 5], A[s][i / 5]);
        if constexpr ((s == 1 || s == 2) && i < 8 && (i & 1)) A[s + 2][i >> 1] = ldA1<(i >> 1)>(c.rsH, vo, hso + (s + 2) * 2048);
        if constexpr (s == 2 && i == 9) ldx();
        if constexpr (s < 4 && i == 13) WAITA(A[s + 1], (s == 0 ? 4 : s == 1 ? 4 : s == 2 ? 7 : 3));
        if constexpr (s < 4 && i == 14) swap_pair(A[s + 1][0], A[s + 1][1]);
        if constexpr (s < 4 && i == 15) swap_pair(A[s + 1][2], A[s + 1][3]);
      });
    });
    put(acc, w * 20480, w < 3 ? w * 20480 + 10240 : CS_OFF, 1024);
#pragma unroll
    for (int s = 0; s < 5; s++)
#pragma unroll
      for (int n = 0; n < 5; n++) asm volatile("" : "=v"(B[s][n]));
  }
  int w_h = w;
  asm volatile("" : "+s"(w_h));
  if (w_h >= 4) {
    const int hw = w & 3;
    v4u A[14][4], Bs[14][5];
    const int last = hw < 2 ? 0 : OOB;
    ldA4(A[0], c.rsH, vo, hso); ldA4(A[1], c.rsH, vo, hso + 2048);
    f32x4 acc[4][5];
#pragma unroll
    for (int m = 0; m < 4; m++)
#pragma unroll
      for (int n = 0; n < 5; n++) acc[m][n] = f32x4{0, 0, 0, 0};
#pragma unroll
    for (int m = 0; m < 4; m++) TIE5(acc[m]);
    LAS char* eb = smem + RING_OFF + hw * 1024 + lane * 16;
#pragma unroll
    for (int s = 0; s < 5; s++) {
      TIE5(B[s]);
#pragma unroll
      for (int n = 0; n < 5; n++) Bs[s][n] = B[s][n];
    }
    WAITA(A[0], 4); swap_pair(A[0][0], A[0][1]); swap_pair(A[0][2], A[0][3]);
    asm volatile("s_nop 1");
    static_for<14>([&](auto S) {
      constexpr int s = decltype(S)::value;
      static_for<20>([&](auto I) {
        constexpr int i = decltype(I)::value;
        mfma_ip(acc[i / 5][i % 5], Bs[s][i % 5], A[s][i / 5]);
        if constexpr (i < 4 * h_na(s)) {
          constexpr int k = h_a0(s) + (i >> 2);
          A[k][i & 3] = ldA1<(i & 3)>(c.rsH, k == 13 ? (vo | last) : vo, hso + k * 2048);
        }
        if constexpr (s >= 6 && s <= 10 && i >= 4 && i < 9) Bs[s + 3][i - 4] = ldB1<i - 4>(c.rsT, s == 10 ? (vo | last) : vo, (k0 + s + 3) * 5120);
        if constexpr (s == 8 && i == 9) ldx();
        if constexpr (s >= 4 && s < 8 && i >= 9 && i < 14) {
          SB; Bs[s + 1][i - 9] = *(LAS v4u*)(eb + ((s & 1) * 5 + i - 9) * 8192 + ((s - 4) >> 1) * 4096); SB;
        }
        if constexpr (s < 13 && i == 14) {
          asm volatile("s_waitcnt vmcnt(%4)" : "+v"(A[s + 1][0]), "+v"(A[s + 1][1]), "+v"(A[s + 1][2]), "+v"(A[s + 1][3]) : "i"(hwait(s + 1)) : "memory");
          if constexpr (s + 1 >= 9) TIE5(Bs[s + 1]);
        }
        if constexpr (s < 13 && i == 15) swap_pair(A[s + 1][0], A[s + 1][1]);
        if constexpr (s < 13 && i == 16) swap_pair(A[s + 1][2], A[s + 1][3]);
      });
    });
    put(acc, RING_OFF + hw * 1024, RING_OFF + hw * 1024 + 4096, 8192);
  }
  STAMP(12);
  ldsbar();
  STAMP(13);
  if (w == 0 && bid == 0) { v2u t; t.x = c.tag; t.y = c.taghi; st64_sc1(c.rsC, lane == 0 ? EPOCH_OFF : OOB, t); }
#pragma unroll
  for (int i = 0; i < 3; i++) {
    const int tl = w + 8 * i;
    if (tl < 20) {
      const int m = tl / 5, n = tl - 5 * m;
      LAS char* ps = smem + tl * 1024 + lane * 16;
      LAS char* pf = smem + RING_OFF + (tl < 10 ? tl * 8192 : (tl - 10) * 8192 + 4096) + lane * 16;
      const f32x4 s = (*(LAS f32x4*)ps + *(LAS f32x4*)(ps + 20480)) + (*(LAS f32x4*)(ps + 40960) + *(LAS f32x4*)(ps + (tl < 10 ? 61440 : CS_OFF - 10240)));
      const f32x4 f = (*(LAS f32x4*)pf + *(LAS f32x4*)(pf + 1024)) + (*(LAS f32x4*)(pf + 2048) + *(LAS f32x4*)(pf + 3072));
      float y[4];
#pragma unroll
      for (int j = 0; j < 4; j++) {
        const float xv = (j & 1) ? bfhi(xr[i][j >> 1]) : bflo(xr[i][j >> 1]);
        y[j] = xv + s[j] * silu_f(f[j]);
      }
      v2u o; o.x = pk2(y[0], y[1]); o.y = pk2(y[2], y[3]);
      *(GAS v2u*)((GAS char*)out + (size_t)(cg * 128 + rh * 64 + m * 16 + r) * (DM * 2) + (c3 * 80 + n * 16 + g * 4) * 2) = o;
    }
  }
  STAMP(7);
  asm volatile(".Lcode_end_mark:");
}
}

extern "C" void kernel_launch(void* const* d_in, const int* in_sizes, int n_in, void* d_out, int out_size, void* d_ws, size_t ws_size,
                              hipStream_t stream) {
  (void)hipFuncSetAttribute((const void*)fused_kernel, hipFuncAttributeMaxDynamicSharedMemorySize, LDS_BYTES);
  fused_kernel<<<256, 512, LDS_BYTES, stream>>>((const u16*)d_in[0], (const u16*)d_in[1], (const u16*)d_in[2], (const u16*)d_in[4],
                                                (const u16*)d_in[5], (u16*)d_out, (char*)d_ws);
}
```

```cpp
#include <hip/hip_runtime.h>
#include <stdint.h>
#include <type_traits>
#include <utility>

#define GAS __attribute__((address_space(1)))
#define LAS __attribute__((address_space(3)))
typedef unsigned short u16;
typedef unsigned long long u64;
typedef __bf16 v2bf __attribute__((ext_vector_type(2)));
typedef __bf16 v4bf __attribute__((ext_vector_type(4)));
typedef __bf16 v8bf __attribute__((ext_vector_type(8)));
typedef float f32x2 __attribute__((ext_vector_type(2)));
typedef float f32x4 __attribute__((ext_vector_type(4)));
typedef unsigned v4u __attribute__((ext_vector_type(4)));
typedef unsigned v2u __attribute__((ext_vector_type(2)));
typedef int v4i __attribute__((ext_vector_type(4)));

namespace {
constexpr int SEQ = 2048, DM = 640, FF = 1728;
constexpr int NCT = 148;
constexpr int H_BYTES = 64 * NCT * 1024;
constexpr size_t CTRL_OFF = 16u << 20;
constexpr int EPOCH_OFF = 0, RDY_OFF = 2048, FLAG_OFF = 8192, STAT_OFF = 65536;
constexpr size_t BT_OFF = 12u << 20;
constexpr int BT_SLICE = 74 * 5120;
constexpr int A_BYTES = 144 * 128, B_BYTES = 64 * 512, ST_BYTES = A_BYTES + B_BYTES;
constexpr int CS_OFF = 3 * ST_BYTES, RS_OFF = CS_OFF + 1024;
constexpr int RDYL_OFF = CS_OFF + 6144, EPL_OFF = CS_OFF + 7168;
constexpr int DUMP_OFF = CS_OFF + 4096;
constexpr int IMG_LD = 488;
constexpr int RING_OFF = 71680;
constexpr int LDS_BYTES = 163840;
constexpr int OOB = 0x7FF00000;
constexpr unsigned SPIN_CAP = 1u << 16;
#define SGB 1
#define NCLS 3
#define PFMASK 0x42
#define MAP3 1
#define POLL_SLEEP 8
#define STAMP(i)
#define SB __builtin_amdgcn_sched_barrier(0)
#define LDPOL2 " sc1"

__device__ __forceinline__ v4i mkrs(const void* p, int bytes) {
  u64 a = (u64)p; v4i r; r.x = (int)(unsigned)a; r.y = (int)((unsigned)(a >> 32) & 0xFFFFu); r.z = bytes; r.w = 0x00020000; return r;
}
__device__ __forceinline__ void dma16(v4i rs, unsigned lds, int voff, int soff) {
  asm volatile("s_mov_b32 m0, %0\n\ts_nop 0\n\tbuffer_load_dwordx4 %1, %2, %3 offen lds" ::"s"(lds), "v"(voff), "s"(rs), "s"(soff));
}
__device__ __forceinline__ void dma16_sc1(v4i rs, unsigned lds, int voff, int soff) {
  asm volatile("s_mov_b32 m0, %0\n\ts_nop 3\n\tbuffer_load_dwordx4 %1, %2, %3 offen" LDPOL2 " lds" ::"s"(lds), "v"(voff), "s"(rs), "s"(soff));
}
__device__ __forceinline__ void dma16_50(v4i rs, unsigned lds, int voff, int soff) {
  asm volatile("s_mov_b32 m0, %0\n\ts_mov_b32 exec_hi, 0x3ffff\n\tbuffer_load_dwordx4 %1, %2, %3 offen lds\n\ts_mov_b32 exec_hi, -1" ::"s"(lds), "v"(voff), "s"(rs), "s"(soff));
}
#define LDPOL " sc1"
#define STPOL " sc1"
template <int IMM> __device__ __forceinline__ v4u ld128_sc1(v4i rs, int voff, int soff) {
  v4u r; asm volatile("buffer_load_dwordx4 %0, %1, %2, %3 offen offset:%4" LDPOL : "=v"(r) : "v"(voff), "s"(rs), "s"(soff), "i"(IMM)); return r;
}
template <int IMM> __device__ __forceinline__ v4u ld128(v4i rs, int voff, int soff) {
  v4u r; asm volatile("buffer_load_dwordx4 %0, %1, %2, %3 offen offset:%4" : "=v"(r) : "v"(voff), "s"(rs), "s"(soff), "i"(IMM)); return r;
}
template <int IMM> __device__ __forceinline__ unsigned ld16(v4i rs, int voff, int soff) {
  unsigned r; asm volatile("buffer_load_ushort %0, %1, %2, %3 offen offset:%4" : "=v"(r) : "v"(voff), "s"(rs), "s"(soff), "i"(IMM)); return r;
}
__device__ __forceinline__ v2u ld64(v4i rs, int voff, int soff) {
  v2u r; asm volatile("buffer_load_dwordx2 %0, %1, %2, %3 offen" : "=v"(r) : "v"(voff), "s"(rs), "s"(soff)); return r;
}
__device__ __forceinline__ v2u ld64_sc1(v4i rs, int voff, int soff) {
  v2u r; asm volatile("buffer_load_dwordx2 %0, %1, %2, %3 offen sc1" : "=v"(r) : "v"(voff), "s"(rs), "s"(soff)); return r;
}
__device__ __forceinline__ v2u ld64_sc1_wait(v4i rs, int voff) {
  v2u r; asm volatile("buffer_load_dwordx2 %0, %1, %2, 0 offen sc1\n\ts_waitcnt vmcnt(0)" : "=&v"(r) : "v"(voff), "s"(rs) : "memory"); return r;
}
__device__ __forceinline__ void st128_sc1(v4i rs, int voff, int soff, v4u v) {
  asm volatile("buffer_store_dwordx4 %0, %1, %2, %3 offen" STPOL "\n\ts_nop 1" ::"v"(v), "v"(voff), "s"(rs), "s"(soff) : "memory");
}
__device__ __forceinline__ void st64_sc1(v4i rs, int voff, v2u v) {
  asm volatile("buffer_store_dwordx2 %0, %1, %2, 0 offen sc1" ::"v"(v), "v"(voff), "s"(rs) : "memory");
}
__device__ __forceinline__ void st32_sc1(v4i rs, int voff, unsigned v) {
  asm volatile("buffer_store_dword %0, %1, %2, 0 offen sc1" ::"v"(v), "v"(voff), "s"(rs) : "memory");
}
template <int N> __device__ __forceinline__ void waitbar() { asm volatile("s_waitcnt vmcnt(%0) lgkmcnt(0)\n\ts_barrier" ::"i"(N) : "memory"); }
__device__ __forceinline__ void ldsbar() { asm volatile("s_waitcnt lgkmcnt(0)\n\ts_barrier" ::: "memory"); }
__device__ __forceinline__ void drain() { asm volatile("s_waitcnt vmcnt(0)" ::: "memory"); }
__device__ __forceinline__ float bflo(unsigned u) { return __builtin_bit_cast(float, u << 16); }
__device__ __forceinline__ float bfhi(unsigned u) { return __builtin_bit_cast(float, u & 0xFFFF0000u); }
__device__ __forceinline__ unsigned pk2(float a, float b) {
  v2bf v; v.x = (__bf16)a; v.y = (__bf16)b; return __builtin_bit_cast(unsigned, v);
}
__device__ __forceinline__ float rbf(float a) { return (float)(__bf16)a; }
__device__ __forceinline__ float dot2(unsigned a, unsigned b, float c) {
  return __builtin_amdgcn_fdot2_f32_bf16(__builtin_bit_cast(v2bf, a), __builtin_bit_cast(v2bf, b), c, false);
}
__device__ __forceinline__ f32x4 mfma(v8bf a, v8bf b, f32x4 c) { return __builtin_amdgcn_mfma_f32_16x16x32_bf16(a, b, c, 0, 0, 0); }
__device__ __forceinline__ v8bf trfrag(LAS char* p, int second) {
  v4bf lo = __builtin_amdgcn_ds_read_tr16_b64_v4bf16((LAS v4bf*)p);
  v4bf hi = __builtin_amdgcn_ds_read_tr16_b64_v4bf16((LAS v4bf*)(p + second));
  return __builtin_shufflevector(lo, hi, 0, 1, 2, 3, 4, 5, 6, 7);
}
__device__ __forceinline__ float row8_sum(float v) {
  v += __builtin_bit_cast(float, __builtin_amdgcn_update_dpp(0, __builtin_bit_cast(int, v), 0xB1, 0xF, 0xF, false));
  v += __builtin_bit_cast(float, __builtin_amdgcn_update_dpp(0, __builtin_bit_cast(int, v), 0x4E, 0xF, 0xF, false));
  v += __builtin_bit_cast(float, __builtin_amdgcn_update_dpp(0, __builtin_bit_cast(int, v), 0x141, 0xF, 0xF, false));
  return v;
}
__device__ __forceinline__ float silu_f(float y) { return y * __builtin_amdgcn_rcpf(1.0f + __builtin_amdgcn_exp2f(y * -1.4426950408889634f)); }
template <class F, int... I> __device__ __forceinline__ void static_for_impl(F&& f, std::integer_sequence<int, I...>) { (f(std::integral_constant<int, I>{}), ...); }
template <int N, class F> __device__ __forceinline__ void static_for(F&& f) { static_for_impl(f, std::make_integer_sequence<int, N>{}); }

struct P1 {
  v4i rsA, rsB, rsH, rsC, rsT;
  int avoff, asvoff, bvoff, ldb2;
  int pfso, bid;
  int w, wm, lane, grp, mem, rot;
  const char* bt;
  int tstart, ftile0;
  int isffn, htile0;
  unsigned tag, taghi;
  int k0, k0t, halo, side;
  unsigned cpf;
};
__device__ __forceinline__ int hk0(int hw) { return 20 + 14 * hw - ((hw + 1) >> 2); }

template <int I> __device__ __forceinline__ void p1_req(const P1& c, unsigned base, int st) {
  st += c.rot; st = st >= 10 ? st - 10 : st;
  if (I == 0 && c.halo) dma16(c.rsA, base + c.w * 1024, c.asvoff, st * 128);
  if (I < 2) dma16(c.rsA, base + (2 + c.w + 8 * I) * 1024, c.avoff, st * 128 + I * 64 * (DM * 2));
  else dma16(c.rsB, base + A_BYTES + (c.w + 8 * (I - 2)) * 1024, c.bvoff, (st * 64 + (I - 2) * 16) * c.ldb2);
}
template <int E> __device__ __forceinline__ void tail_req(const P1& c, unsigned lds0) {
  dma16_sc1(c.rsT, lds0 + RING_OFF + E * 8192 + c.w * 1024, c.lane * 16, c.k0t * 5120 + E * 1024);
}

__device__ __forceinline__ void ldB5(v4u (&B)[5], v4i rsT, int voff, int so) {
  const int so2 = so == OOB ? OOB : so + 4096;
  B[0] = ld128_sc1<0>(rsT, voff, so); B[1] = ld128_sc1<1024>(rsT, voff, so); B[2] = ld128_sc1<2048>(rsT, voff, so);
  B[3] = ld128_sc1<3072>(rsT, voff, so); B[4] = ld128_sc1<0>(rsT, voff, so2);
}
#define TIE5(B) asm volatile("" : "+v"((B)[0]), "+v"((B)[1]), "+v"((B)[2]), "+v"((B)[3]), "+v"((B)[4]))

template <int I> __device__ __forceinline__ v4u ldB1(v4i rsT, int voff, int so) {
  return ld128_sc1<(I & 3) * 1024>(rsT, voff, I < 4 ? so : so + 4096);
}
template <int NT, int NOWN, int NFOR>
__device__ __forceinline__ void phase1(P1& c, LAS char* smem, unsigned lds0, char* ws, v4u (&B)[5][5]) {
  constexpr int NB = NT + NFOR, NX = NOWN + NFOR;
  const int lane = c.lane, r = lane & 15, g = lane >> 4, q = r >> 2, p = lane & 3;
  int aad[2];
#pragma unroll
  for (int ks = 0; ks < 2; ks++) aad[ks] = r * 128 + (((4 * ks + g) ^ (r >> 1)) << 4);
  const int amain = (1 + c.wm * 4) * 2048;
  const int z = q | ((g & 1) << 2);
  int tile[NB], bad[NB];
#pragma unroll
  for (int n = 0; n < NB; n++) {
    if (n < NT) { int t = n + 2 * c.wm; t = t >= NT ? t - NT : t; tile[n] = c.tstart + t; }
    else tile[n] = c.ftile0 + 2 * (n - NT);
    bad[n] = A_BYTES + (8 * g + q) * 512 + (((tile[n] & 7) ^ z) << 5) + (tile[n] >> 3) * 256 + p * 8;
  }
  f32x4 acc[4][NT], accx[NX];
#pragma unroll
  for (int m = 0; m < 4; m++)
#pragma unroll
    for (int n = 0; n < NT; n++) acc[m][n] = f32x4{0, 0, 0, 0};
#pragma unroll
  for (int n = 0; n < NX; n++) accx[n] = f32x4{0, 0, 0, 0};
  v8bf af[2][4], as[2], bf[2][NB];
  const bool ones = (r == 0) || c.isffn;
  auto ldq = [&](int buf, LAS char* base, int ks, int i) {
    af[buf][i] = *(LAS v8bf*)(base + aad[ks] + amain + i * 2048);
#pragma unroll
    for (int n = 0; n < NB; n++) if ((n & 3) == i) bf[buf][n] = trfrag(base + bad[n] + ks * 16384, 2048);
    if (i == 3) as[buf] = *(LAS v8bf*)(base + aad[ks]);
  };
  auto row = [&](int buf, int m) {
#pragma unroll
    for (int n = 0; n < NT; n++) acc[m][n] = mfma(bf[buf][n], af[buf][m], acc[m][n]);
    if (m == 3) {
      v4u s = __builtin_bit_cast(v4u, as[buf]);
#pragma unroll
      for (int e = 0; e < 4; e++) s[e] = ones ? 0x3F803F80u : s[e];
      v8bf sv = __builtin_bit_cast(v8bf, s);
#pragma unroll
      for (int n = 0; n < NX; n++) accx[n] = mfma(bf[buf][n < NOWN ? n : NT + n - NOWN], sv, accx[n]);
    }
#pragma unroll
    for (int i = 0; i < NT; i++) {
      __builtin_amdgcn_sched_group_barrier(0x8, 1, 0); __builtin_amdgcn_sched_group_barrier(0x100, 2, 0); __builtin_amdgcn_sched_group_barrier(0x2, 1, 0);
    }
    SB;
  };
  auto halfstage = [&](int buf, bool rd, LAS char* rbase, int rks, auto&& rq0, auto&& rq1, auto&& rq2) {
    if (rd) { ldq(buf ^ 1, rbase, rks, 0); ldq(buf ^ 1, rbase, rks, 1); }
    row(buf, 0); rq0(); SB;
    if (rd) ldq(buf ^ 1, rbase, rks, 2);
    row(buf, 1); rq1(); SB;
    if (rd) ldq(buf ^ 1, rbase, rks, 3);
    row(buf, 2); rq2(); SB;
    row(buf, 3);
  };
  auto none = [&]() {};
  auto lst = [&](int i) {};
#pragma unroll
  for (int m = 0; m < 4; m++)
#pragma unroll
    for (int n = 0; n < NT; n++) asm volatile("" : "+v"(acc[m][n]));
#pragma unroll
  for (int n = 0; n < NX; n++) asm volatile("" : "+v"(accx[n]));
#pragma unroll
  for (int n = 0; n < NB; n++) asm volatile("" : "+v"(bad[n]));
  asm volatile("" : "+v"(aad[0]), "+v"(aad[1]));
  if (c.side) ldsbar(); else waitbar<9>();
  asm volatile("" ::"v"(c.cpf));
  lst(0);
  STAMP(1);
  auto special = [&](int st) {
    if (st == 1) {
      const v2u ep = *(LAS v2u*)(smem + EPL_OFF);
      const u64 e = ((u64)(unsigned)__builtin_amdgcn_readfirstlane(ep.y) << 32) | (unsigned)__builtin_amdgcn_readfirstlane(ep.x);
      const u64 t = e * 0x5851F42D4C957F2Dull + 0x14057B7EF767814Full;
      c.tag = (unsigned)t; c.taghi = (unsigned)(t >> 32);
      if (c.w == 0) st32_sc1(c.rsC, lane == 0 ? RDY_OFF + c.bid * 4 : OOB, c.tag);
    } else if (st == 6) {
      if (c.w == 0) dma16_sc1(c.rsC, lds0 + RDYL_OFF, lane * 16, RDY_OFF);
    } else if (st == 7) {
      v4u rdy = *(LAS v4u*)(smem + RDYL_OFF + lane * 16);
      for (unsigned spins = 0;; spins++) {
        if (__all(rdy.x == c.tag && rdy.y == c.tag && rdy.z == c.tag && rdy.w == c.tag) || spins > SPIN_CAP) break;
        __builtin_amdgcn_s_sleep(POLL_SLEEP);
        asm volatile("buffer_load_dwordx4 %0, %1, %2, 0 offen offset:%3 sc1\n\ts_waitcnt vmcnt(0)" : "=&v"(rdy) : "v"(lane * 16), "s"(c.rsC), "i"(RDY_OFF) : "memory");
      }
      if (c.w < 2) dma16_sc1(c.rsC, lds0 + RS_OFF + c.w * 1024, lane * 16, STAT_OFF + c.grp * 2048 + c.w * 1024);
    }
  };
#pragma unroll
  for (int i = 0; i < 4; i++) ldq(0, smem, 0, i);
  int so = 0, po = 2 * ST_BYTES;
#pragma nounroll
  for (int st = 0; st < 7; st++) {
    const int no = so == 2 * ST_BYTES ? 0 : so + ST_BYTES;
    halfstage(0, true, smem + so, 1, [&]() { p1_req<3>(c, lds0 + po, st + 2); }, [&]() { p1_req<4>(c, lds0 + po, st + 2); }, [&]() { p1_req<5>(c, lds0 + po, st + 2); });
    waitbar<6>();
    SB;
    lst(st + 1);
    if ((PFMASK >> st) & 1) special(st);
    SB;
    halfstage(1, true, smem + no, 0, [&]() { p1_req<0>(c, lds0 + so, st + 3); }, [&]() { p1_req<1>(c, lds0 + so, st + 3); }, [&]() { p1_req<2>(c, lds0 + so, st + 3); });
    po = so; so = no;
  }
  halfstage(0, true, smem + ST_BYTES, 1, [&]() { p1_req<3>(c, lds0, 9); }, [&]() { p1_req<4>(c, lds0, 9); }, [&]() { p1_req<5>(c, lds0, 9); });
  waitbar<6>(); SB; lst(8);
  special(7); SB;
  halfstage(1, true, smem + 2 * ST_BYTES, 0, [&]() { tail_req<0>(c, lds0); }, [&]() { tail_req<1>(c, lds0); }, [&]() { tail_req<2>(c, lds0); });
  halfstage(0, true, smem + 2 * ST_BYTES, 1, none, none, none);
  waitbar<3>(); SB; lst(9);
  halfstage(1, true, smem, 0, [&]() { tail_req<3>(c, lds0); }, [&]() { tail_req<4>(c, lds0); }, [&]() { tail_req<5>(c, lds0); });
  halfstage(0, true, smem, 1, [&]() { tail_req<6>(c, lds0); }, [&]() { tail_req<7>(c, lds0); }, [&]() { tail_req<8>(c, lds0); });
  halfstage(1, false, smem, 0, [&]() { tail_req<9>(c, lds0); }, none, none);
  lst(10);
  STAMP(2);
#pragma unroll
  for (int n = 0; n < NX; n++)
    *(LAS f32x4*)(smem + (r == 0 ? CS_OFF + (tile[n < NOWN ? n : NT + n - NOWN] * 16 + 4 * g) * 4 : DUMP_OFF + lane * 16)) = accx[n];
  ldsbar();
  STAMP(8);
  f32x2 rs[4];
#pragma unroll
  for (int m = 0; m < 4; m++) rs[m] = *(LAS f32x2*)(smem + RS_OFF + (16 + c.wm * 64 + m * 16 + r) * 8);
  auto ldB = [&](int s) { SB; ldB5(B[s], c.rsT, lane * 16, (c.k0 + s) * 5120); SB; };
  if (c.isffn) {
    float c1[4];
#pragma unroll
    for (int m = 0; m < 4; m++) c1[m] = __builtin_amdgcn_rcpf(rs[m].y) * -1.4426950408889634f;
#pragma unroll
    for (int n = 0; n < NT; n++) {
      const f32x4 cs = *(LAS f32x4*)(smem + CS_OFF + (tile[n] * 16 + 4 * g) * 4);
#pragma unroll
      for (int mp = 0; mp < 2; mp++) {
        v4u o;
#pragma unroll
        for (int h = 0; h < 2; h++) {
          const int m = 2 * mp + h;
          const f32x4 t = acc[m][n] - rs[m].x * cs;
          const f32x4 zz = t * c1[m];
          f32x4 e;
#pragma unroll
          for (int j = 0; j < 4; j++) e[j] = __builtin_amdgcn_exp2f(zz[j]);
          const f32x4 d = e * rs[m].y + rs[m].y;
          f32x4 rr;
#pragma unroll
          for (int j = 0; j < 4; j++) rr[j] = __builtin_amdgcn_rcpf(d[j]);
          const f32x4 y = t * rr;
          o[2 * h] = pk2(y[0], y[1]); o[2 * h + 1] = pk2(y[2], y[3]);
        }
        st128_sc1(c.rsH, lane * 16, ((c.grp * 4 + c.wm * 2 + mp) * NCT + c.htile0 + tile[n]) * 1024, o);
      }
    }
  } else {
    float ri[4];
#pragma unroll
    for (int m = 0; m < 4; m++) ri[m] = __builtin_amdgcn_rcpf(rs[m].y);
    f32x4 cs[NB];
#pragma unroll
    for (int n = 0; n < NB; n++) cs[n] = *(LAS f32x4*)(smem + CS_OFF + (tile[n] * 16 + 4 * g) * 4);
    const f32x2 rh = *(LAS f32x2*)(smem + RS_OFF + r * 8);
    const float rih = __builtin_amdgcn_rcpf(rh.y);
    SB;
    static_for<4 * NT + NX>([&](auto U) {
      constexpr int u = decltype(U)::value;
      if constexpr (u < 15) B[u / 5][u % 5] = ldB1<u % 5>(c.rsT, lane * 16, (c.k0 + u / 5) * 5120);
      if constexpr (u < 4 * NT) {
        constexpr int n = u / 4, m = u % 4;
        const f32x4 y = (acc[m][n] - rs[m].x * cs[n]) * ri[m];
        v2u o; o.x = pk2(y[0], y[1]); o.y = pk2(y[2], y[3]);
        *(LAS v2u*)(smem + (16 + c.wm * 64 + m * 16 + r) * IMG_LD + tile[n] * 32 + g * 8) = o;
      } else {
        constexpr int x = u - 4 * NT, n = x < NOWN ? x : NT + x - NOWN;
        const f32x4 y = (accx[x] - rh.x * cs[n]) * rih;
        v2u o; o.x = pk2(y[0], y[1]); o.y = pk2(y[2], y[3]);
        *(LAS v2u*)(smem + r * IMG_LD + tile[n] * 32 + g * 8) = o;
      }
      SB;
    });
    ldsbar();
  }
}

__device__ __forceinline__ void wait_flags(const P1& c, int off) {
  for (unsigned spins = 0;; spins++) {
    unsigned f;
    asm volatile("buffer_load_dword %0, %1, %2, 0 offen sc1\n\ts_waitcnt vmcnt(0)" : "=&v"(f) : "v"(off), "s"(c.rsC) : "memory");
    if (__all(off == OOB || f == c.tag) || spins > SPIN_CAP) break;
    __builtin_amdgcn_s_sleep(POLL_SLEEP);
  }
}
__device__ __forceinline__ void mfma_ip(f32x4& acc, const v4u& b, const v4u& a) {
  asm volatile("v_mfma_f32_16x16x32_bf16 %0, %1, %2, %0" : "+v"(acc) : "v"(b), "v"(a));
}
__device__ __forceinline__ void swap_pair(v4u& l0, v4u& l1) {
  unsigned a = l0.z, b = l1.x, c = l0.w, d = l1.y;
  asm volatile("v_swap_b32 %0, %1" : "+v"(a), "+v"(b));
  l0.z = a; l1.x = b;
  asm volatile("v_swap_b32 %0, %1" : "+v"(c), "+v"(d));
  l0.w = c; l1.y = d;
}
template <int I> __device__ __forceinline__ v4u ldA1(v4i rsH, int voff, int so) {
  return ld128_sc1<(I & 1) * 1024>(rsH, voff, I < 2 ? so : so + NCT * 1024);
}
__device__ __forceinline__ void ldA4(v4u (&A)[4], v4i rsH, int voff, int so) {
  const int so2 = so == OOB ? OOB : so + NCT * 1024;
  A[0] = ld128_sc1<0>(rsH, voff, so); A[1] = ld128_sc1<1024>(rsH, voff, so);
  A[2] = ld128_sc1<0>(rsH, voff, so2); A[3] = ld128_sc1<1024>(rsH, voff, so2);
}
#define WAITA(A, N) asm volatile("s_waitcnt vmcnt(%4) lgkmcnt(0)" : "+v"((A)[0]), "+v"((A)[1]), "+v"((A)[2]), "+v"((A)[3]) : "i"(N) : "memory")
constexpr int h_na(int t) { return t == 1 ? 2 : (t + 3 < 14 ? 1 : 0); }
constexpr int h_a0(int t) { int n = 2; for (int i = 0; i < t; i++) n += h_na(i); return n; }
constexpr int hcnt(int t) { return 4 * h_na(t) + ((t >= 6 && t <= 10) ? 5 : 0) + (t == 8 ? 3 : 0); }
constexpr int hwait(int s) {
  int n = 0;
  if (s >= 9) { n = (s - 3 == 8 ? 3 : 0); for (int t = s - 2; t < s; t++) n += hcnt(t); return n; }
  if (s < 2) { n = 4 * (1 - s); for (int t = 0; t < s; t++) n += hcnt(t); return n; }
  int t0 = 0; while (h_a0(t0) + h_na(t0) <= s) t0++;
  n = hcnt(t0) - 4 * (s - h_a0(t0) + 1);
  for (int t = t0 + 1; t < s; t++) n += hcnt(t);
  return n;
}

__global__ __launch_bounds__(512, 2) void fused_kernel(const u16* __restrict__ x, const u16* __restrict__ wqkv, const u16* __restrict__ wo,
                                                       const u16* __restrict__ w1, const u16* __restrict__ w2, u16* __restrict__ out,
                                                       char* __restrict__ ws) {
  extern __shared__ __attribute__((aligned(16))) char smem_[];
  LAS char* smem = (LAS char*)smem_;
  const unsigned lds0 = (unsigned)(size_t)smem;
  const int tid = threadIdx.x, lane = tid & 63, w = __builtin_amdgcn_readfirstlane(tid >> 6);
  const int bid = blockIdx.x, xcd = bid & 7, idx = bid >> 3, half = xcd & 1, j8 = idx & 7;
  const int grp = (xcd >> 1) * 4 + (idx >> 3), mem = half * 8 + j8;
  const int isffn = j8 >> 2, sub = half * 4 + (j8 & 3);
  STAMP(0);
  unsigned cpf;
  {
    u64 pc0; unsigned codelen;
    asm volatile("s_getpc_b64 %0\n.Lcode_begin_mark:\n\ts_mov_b32 %1, .Lcode_end_mark-.Lcode_begin_mark" : "=s"(pc0), "=s"(codelen));
    const v4i rp = mkrs((const void*)pc0, (int)codelen);
    asm volatile("buffer_load_dword %0, %1, %2, %3 offen" : "=v"(cpf) : "v"(lane < 2 ? lane * 128 : OOB), "s"(rp), "s"((idx * 8 + w) * 256));
  }
  P1 c;
  c.rsA = mkrs(x, SEQ * DM * 2);
  c.rsB = mkrs(isffn ? w1 : wqkv, isffn ? DM * FF * 2 : DM * 3 * DM * 2);
  c.ldb2 = isffn ? FF * 2 : 3 * DM * 2;
  c.w = w; c.lane = lane; c.grp = grp; c.isffn = isffn; c.mem = mem;
  const int ftile0 = sub < 4 ? sub * 14 : 56 + (sub - 4) * 13;
  const int ntile = isffn ? (sub < 4 ? 14 : 13) : 15;
  {
    const int ar = lane >> 3, sw = ((lane & 7) ^ ((4 * w + (lane >> 4)) & 7)) << 4;
    c.avoff = (grp * 128 + 8 * w + ar) * (DM * 2) + sw;
    int hr = grp * 128 - 16 + 8 * w + ar; hr = hr < 0 ? 0 : hr;
    c.asvoff = hr * (DM * 2) + sw;
    const int kk = 2 * w + (lane >> 5), zz = (kk & 3) | (((kk >> 3) & 1) << 2), pc = lane & 31, seg = (pc >> 1) ^ zz;
    const int part = (seg >= 10) + (seg >= 5);
    const int col = isffn ? (ftile0 + seg) * 16 : part * (DM - 80) + sub * 80 + seg * 16;
    c.bvoff = seg < ntile ? kk * c.ldb2 + col * 2 + (pc & 1) * 16 : OOB;
  }
  c.halo = !isffn && w < 2;
  { const int cl = (j8 + grp + half) % NCLS; c.rot = cl * 10 / NCLS; }
  static_for<6>([&](auto I) { p1_req<decltype(I)::value>(c, lds0, 0); });
  c.rsH = mkrs(ws, H_BYTES);
  c.rsC = mkrs(ws + CTRL_OFF, 1 << 20);
  const int cg = (xcd >> 1) * 4 + half * 2 + (idx >> 4), rh = (idx >> 3) & 1, c3 = idx & 7;
  c.bt = ws + BT_OFF; c.bid = bid;
  c.rsT = mkrs(ws + BT_OFF + (size_t)c3 * BT_SLICE, BT_SLICE);
  c.wm = w >> 2;
  c.k0 = w < 4 ? 5 * w : hk0(w & 3);
  c.k0t = hk0(w & 3) + 5 + 2 * (w >> 2);
  c.tag = 0; c.taghi = 0;
  const int wn = w < 4 ? w : 7 - w;
  c.htile0 = 40 + ftile0;
  const int tsc = w == 3 ? CS_OFF : 2 * ST_BYTES + A_BYTES + 8192 + (w - 4) * 5120;
  v4u xs[10];
  const int sslot = w * 8 + (lane >> 3);
  const int kap = (7 - w) * 16 + grp;
  const bool dostat = isffn && w < 3, dotile = isffn && w >= 3 && kap < 74;
  if (dostat) {
    int rho = sub * 18 + (sslot < 18 ? sslot : 17);
    int row = grp * 128 - 16 + rho; row = row < 0 ? 0 : row;
    const int vo = row * (DM * 2) + (lane & 7) * 16;
    static_for<10>([&](auto I) { constexpr int i = decltype(I)::value; xs[i] = ld128<128 * i>(c.rsA, vo, 0); });
  } else if (dotile) {
    const v4i rw = mkrs(kap < 20 ? wo : w2, kap < 20 ? DM * DM * 2 : DM * FF * 2);
    int kq = kap;
    if (kap >= 20) asm volatile("s_sub_u32 %0, %0, 20" : "+s"(kq)::"scc");
    const int so = kq * 32 * (DM * 2);
#pragma unroll
    for (int i = 0; i < 5; i++) {
      const int pc = 64 * i + lane, rr = pc / 10;
      dma16(rw, lds0 + tsc + i * 1024, rr * (DM * 2) + sub * 160 + (pc - 10 * rr) * 16, so);
    }
  }
  static_for<6>([&](auto I) { p1_req<decltype(I)::value>(c, lds0 + ST_BYTES, 1); });
  static_for<3>([&](auto I) { p1_req<decltype(I)::value>(c, lds0 + 2 * ST_BYTES, 2); });
  c.side = dostat || dotile; c.cpf = cpf;
  if (c.side) asm volatile("s_waitcnt vmcnt(9)" ::: "memory");
  if (w == 7) dma16_sc1(c.rsC, lds0 + EPL_OFF, lane == 0 ? EPOCH_OFF : OOB, 0);
  if (dostat) {
    asm volatile("" : "+v"(xs[0]), "+v"(xs[1]), "+v"(xs[2]), "+v"(xs[3]), "+v"(xs[4]), "+v"(xs[5]), "+v"(xs[6]), "+v"(xs[7]), "+v"(xs[8]), "+v"(xs[9])::"memory");
    float s0 = 0, s1 = 0, q0 = 0, q1 = 0;
#pragma unroll
    for (int i = 0; i < 40; i += 2) {
      const unsigned a = xs[i >> 2][i & 3], b = xs[i >> 2][(i & 3) + 1];
      s0 = dot2(a, 0x3F803F80u, s0); s1 = dot2(b, 0x3F803F80u, s1); q0 = dot2(a, a, q0); q1 = dot2(b, b, q1);
    }
    const float mean = row8_sum(s0 + s1) * (1.0f / DM);
    const float var = fmaxf(row8_sum(q0 + q1) * (1.0f / DM) - mean * mean, 0.f);
    const float sd = __builtin_sqrtf(rbf(rbf(var) + 1e-5f));
    v2u gr; gr.x = __builtin_bit_cast(unsigned, rbf(mean)); gr.y = __builtin_bit_cast(unsigned, rbf(sd));
    st64_sc1(c.rsC, ((lane & 7) == 0 && sslot < 18) ? STAT_OFF + grp * 2048 + (sub * 18 + sslot) * 8 : OOB, gr);
  } else if (dotile) {
    LAS char* tb = smem + tsc + ((lane >> 4) * 4 + ((lane & 15) >> 2)) * 160 + (lane & 3) * 8;
#pragma unroll
    for (int n = 0; n < 5; n++)
      st128_sc1(mkrs(ws + BT_OFF, 8 * BT_SLICE), lane * 16, sub * BT_SLICE + (kap * 5 + n) * 1024, __builtin_bit_cast(v4u, trfrag(tb + n * 32, 16 * 160)));
  }
  v4u B[5][5];
  {
    const int n4 = ntile - 12;
    c.tstart = wn <= n4 ? 4 * wn : 4 * n4 + 3 * (wn - n4);
    if (!isffn) c.ftile0 = 1 + 6 * c.wm;
    else { const int fi = (wn - n4) * 2 + c.wm; c.ftile0 = 1 + 2 * (n4 == 2 ? fi : (fi & 1)); }
    if (wn < n4) phase1<4, 1, 0>(c, smem, lds0, ws, B);
    else if (!isffn) phase1<3, 2, 3>(c, smem, lds0, ws, B);
    else phase1<3, 2, 1>(c, smem, lds0, ws, B);
  }
  STAMP(9);
  const int r = lane & 15, g = lane >> 4;
  const int k0 = c.k0;
  auto ldB = [&](int s) { SB; ldB5(B[s], c.rsT, lane * 16, (k0 + s) * 5120); SB; };
  if (!isffn) {
    const int row = (w & 1) * 64 + lane;
    const int pos = grp * 128 + row;
    LAS char* qp = smem + (row + 16) * IMG_LD + (w >> 1) * 40;
    unsigned qv[10];
#pragma unroll
    for (int i = 0; i < 5; i++) { const v2u t = *(LAS v2u*)(qp + i * 8); qv[2 * i] = t.x; qv[2 * i + 1] = t.y; }
    float lg[8];
    static_for<8>([&](auto K) {
      constexpr int k = decltype(K)::value;
      if constexpr (k < 5) B[3][k] = ldB1<k>(c.rsT, lane * 16, (k0 + 3) * 5120);
      LAS char* kp = qp + 160 - 2 * k * IMG_LD;
      float a0 = 0, a1 = 0;
#pragma unroll
      for (int i = 0; i < 5; i++) { const v2u t = *(LAS v2u*)(kp + i * 8); a0 = dot2(qv[2 * i], t.x, a0); a1 = dot2(qv[2 * i + 1], t.y, a1); }
      lg[k] = pos - 2 * k >= 0 ? a0 + a1 : -4e9f;
    });
    float mx = lg[0];
#pragma unroll
    for (int k = 1; k < 8; k++) mx = fmaxf(mx, lg[k]);
    float sum = 0;
    constexpr float C = 0.22360679774997896f * 1.4426950408889634f;
#pragma unroll
    for (int k = 0; k < 8; k++) { lg[k] = __builtin_amdgcn_exp2f((lg[k] - mx) * C); sum += lg[k]; }
    const float inv = __builtin_amdgcn_rcpf(sum);
    float o[20];
#pragma unroll
    for (int d = 0; d < 20; d++) o[d] = 0;
    static_for<8>([&](auto K) {
      constexpr int k = decltype(K)::value;
      if constexpr (k < 5) B[4][k] = ldB1<k>(c.rsT, lane * 16, (k0 + 4) * 5120);
      const unsigned plo = pk2(lg[k] * inv, 0.f), phi = plo << 16;
      LAS char* vp = qp + 320 - 2 * k * IMG_LD;
#pragma unroll
      for (int i = 0; i < 5; i++) {
        const v2u t = *(LAS v2u*)(vp + i * 8);
        o[4 * i] = dot2(t.x, plo, o[4 * i]); o[4 * i + 1] = dot2(t.x, phi, o[4 * i + 1]);
        o[4 * i + 2] = dot2(t.y, plo, o[4 * i + 2]); o[4 * i + 3] = dot2(t.y, phi, o[4 * i + 3]);
      }
    });
#pragma unroll
    for (int i = 0; i < 5; i++) { v2u t; t.x = pk2(o[4 * i], o[4 * i + 1]); t.y = pk2(o[4 * i + 2], o[4 * i + 3]); *(LAS v2u*)(qp + i * 8) = t; }
    STAMP(11);
    ldsbar();
#pragma unroll
    for (int i = 0; i < 3; i++) {
      const int b0 = w + 8 * i, b = b0 < 20 ? b0 : 19, rp = b / 5, t = b - 5 * rp;
      LAS char* sp = smem + (16 + rp * 32 + r) * IMG_LD + t * 32 + g * 8;
      const v2u lo = *(LAS v2u*)sp, hi = *(LAS v2u*)(sp + 16 * IMG_LD);
      v4u v; v.x = lo.x; v.y = lo.y; v.z = hi.x; v.w = hi.y;
      st128_sc1(c.rsH, lane * 16, b0 < 20 ? ((grp * 4 + rp) * NCT + sub * 5 + t) * 1024 : OOB, v);
    }
  }
  waitbar<0>();
  st32_sc1(c.rsC, tid == 0 ? FLAG_OFF + grp * 256 + mem * 4 : OOB, c.tag);
  STAMP(4);
  if (isffn) {
    if (w < 4) __builtin_amdgcn_s_sleep(40);
    ldB(0); ldB(1); ldB(2); ldB(3); ldB(4);
  }
  {
    int m;
    if (w < 4) m = lane < 2 ? (w >> 1) * 8 + (w & 1) * 2 + lane : -1;
    else {
      const int t0 = 2 * (k0 - 20), t1 = t0 + 27 - (w & 2);
      const int f0 = t0 < 56 ? t0 / 14 : 4 + (t0 - 56) / 13, f1 = t1 < 56 ? t1 / 14 : 4 + (t1 - 56) / 13, f = f0 + lane;
      m = f <= f1 ? (f >> 2) * 8 + 4 + (f & 3) : -1;
    }
    wait_flags(c, m >= 0 ? FLAG_OFF + cg * 256 + m * 4 : OOB);
  }
  STAMP(5);
  const int hso = ((cg * 4 + rh * 2) * NCT + 2 * k0) * 1024;
  v2u xr[3];
  auto ldx = [&]() {
#pragma unroll
    for (int i = 0; i < 3; i++) {
      const int tl = w + 8 * i, tt = tl < 20 ? tl : 19, m = tt / 5, n = tt - 5 * m;
      xr[i] = ld64(c.rsA, (cg * 128 + rh * 64 + m * 16 + r) * (DM * 2) + (c3 * 80 + n * 16 + g * 4) * 2, 0);
    }
  };
  auto put = [&](const f32x4 (&acc)[4][5], int p0, int p1, int stride) {
    STAMP(6);
    asm volatile("s_nop 7\n\ts_nop 3\n\ts_waitcnt vmcnt(0) lgkmcnt(0)" : "+v"(xr[0]), "+v"(xr[1]), "+v"(xr[2])::"memory");
#pragma unroll
    for (int m = 0; m < 4; m++)
#pragma unroll
      for (int n = 0; n < 5; n++) *(LAS f32x4*)(smem + (m < 2 ? p0 : p1) + ((m & 1) * 5 + n) * stride + lane * 16) = acc[m][n];
  };
  const int vo = lane * 16;
  if (w < 4) {
    v4u A[5][4];
    auto ldA = [&](int s) { ldA4(A[s], c.rsH, vo, hso + s * 2048); };
    ldA(0); ldA(1); ldA(2);
    f32x4 acc[4][5];
#pragma unroll
    for (int m = 0; m < 4; m++)
#pragma unroll
      for (int n = 0; n < 5; n++) acc[m][n] = f32x4{0, 0, 0, 0};
#pragma unroll
    for (int m = 0; m < 4; m++) TIE5(acc[m]);
#pragma unroll
    for (int s = 0; s < 5; s++) TIE5(B[s]);
    WAITA(A[0], 8); swap_pair(A[0][0], A[0][1]); swap_pair(A[0][2], A[0][3]);
    asm volatile("s_nop 1");
    static_for<5>([&](auto S) {
      constexpr int s = decltype(S)::value;
      static_for<20>([&](auto I) {
        constexpr int i = decltype(I)::value;
        mfma_ip(acc[i / 5][i % 5], B[s][i % 5], A[s][i / 5]);
        if constexpr ((s == 1 || s == 2) && i < 8 && (i & 1)) A[s + 2][i >> 1] = ldA1<(i >> 1)>(c.rsH, vo, hso + (s + 2) * 2048);
        if constexpr (s == 2 && i == 9) ldx();
        if constexpr (s < 4 && i == 13) WAITA(A[s + 1], (s == 0 ? 4 : s == 1 ? 4 : s == 2 ? 7 : 3));
        if constexpr (s < 4 && i == 14) swap_pair(A[s + 1][0], A[s + 1][1]);
        if constexpr (s < 4 && i == 15) swap_pair(A[s + 1][2], A[s + 1][3]);
      });
    });
    put(acc, w * 20480, w < 3 ? w * 20480 + 10240 : CS_OFF, 1024);
#pragma unroll
    for (int s = 0; s < 5; s++)
#pragma unroll
      for (int n = 0; n < 5; n++) asm volatile("" : "=v"(B[s][n]));
  }
  int w_h = w;
  asm volatile("" : "+s"(w_h));
  if (w_h >= 4) {
    const int hw = w & 3;
    v4u A[14][4], Bs[14][5];
    const int last = hw < 2 ? 0 : OOB;
    ldA4(A[0], c.rsH, vo, hso); ldA4(A[1], c.rsH, vo, hso + 2048);
    f32x4 acc[4][5];
#pragma unroll
    for (int m = 0; m < 4; m++)
#pragma unroll
      for (int n = 0; n < 5; n++) acc[m][n] = f32x4{0, 0, 0, 0};
#pragma unroll
    for (int m = 0; m < 4; m++) TIE5(acc[m]);
    LAS char* eb = smem + RING_OFF + hw * 1024 + lane * 16;
#pragma unroll
    for (int s = 0; s < 5; s++) {
      TIE5(B[s]);
#pragma unroll
      for (int n = 0; n < 5; n++) Bs[s][n] = B[s][n];
    }
    WAITA(A[0], 4); swap_pair(A[0][0], A[0][1]); swap_pair(A[0][2], A[0][3]);
    asm volatile("s_nop 1");
    static_for<14>([&](auto S) {
      constexpr int s = decltype(S)::value;
      static_for<20>([&](auto I) {
        constexpr int i = decltype(I)::value;
        mfma_ip(acc[i / 5][i % 5], Bs[s][i % 5], A[s][i / 5]);
        if constexpr (i < 4 * h_na(s)) {
          constexpr int k = h_a0(s) + (i >> 2);
          A[k][i & 3] = ldA1<(i & 3)>(c.rsH, k == 13 ? (vo | last) : vo, hso + k * 2048);
        }
        if constexpr (s >= 6 && s <= 10 && i >= 4 && i < 9) Bs[s + 3][i - 4] = ldB1<i - 4>(c.rsT, s == 10 ? (vo | last) : vo, (k0 + s + 3) * 5120);
        if constexpr (s == 8 && i == 9) ldx();
        if constexpr (s >= 4 && s < 8 && i >= 9 && i < 14) {
          SB; Bs[s + 1][i - 9] = *(LAS v4u*)(eb + ((s & 1) * 5 + i - 9) * 8192 + ((s - 4) >> 1) * 4096); SB;
        }
        if constexpr (s < 13 && i == 14) {
          asm volatile("s_waitcnt vmcnt(%4)" : "+v"(A[s + 1][0]), "+v"(A[s + 1][1]), "+v"(A[s + 1][2]), "+v"(A[s + 1][3]) : "i"(hwait(s + 1)) : "memory");
          if constexpr (s + 1 >= 9) TIE5(Bs[s + 1]);
        }
        if constexpr (s < 13 && i == 15) swap_pair(A[s + 1][0], A[s + 1][1]);
        if constexpr (s < 13 && i == 16) swap_pair(A[s + 1][2], A[s + 1][3]);
      });
    });
    put(acc, RING_OFF + hw * 1024, RING_OFF + hw * 1024 + 4096, 8192);
  }
  STAMP(12);
  ldsbar();
  STAMP(13);
  if (w == 0 && bid == 0) { v2u t; t.x = c.tag; t.y = c.taghi; st64_sc1(c.rsC, lane == 0 ? EPOCH_OFF : OOB, t); }
#pragma unroll
  for (int i = 0; i < 3; i++) {
    const int tl = w + 8 * i;
    if (tl < 20) {
      const int m = tl / 5, n = tl - 5 * m;
      LAS char* ps = smem + tl * 1024 + lane * 16;
      LAS char* pf = smem + RING_OFF + (tl < 10 ? tl * 8192 : (tl - 10) * 8192 + 4096) + lane * 16;
      const f32x4 s = (*(LAS f32x4*)ps + *(LAS f32x4*)(ps + 20480)) + (*(LAS f32x4*)(ps + 40960) + *(LAS f32x4*)(ps + (tl < 10 ? 61440 : CS_OFF - 10240)));
      const f32x4 f = (*(LAS f32x4*)pf + *(LAS f32x4*)(pf + 1024)) + (*(LAS f32x4*)(pf + 2048) + *(LAS f32x4*)(pf + 3072));
      float y[4];
#pragma unroll
      for (int j = 0; j < 4; j++) {
        const float xv = (j & 1) ? bfhi(xr[i][j >> 1]) : bflo(xr[i][j >> 1]);
        y[j] = xv + s[j] * silu_f(f[j]);
      }
      v2u o; o.x = pk2(y[0], y[1]); o.y = pk2(y[2], y[3]);
      *(GAS v2u*)((GAS char*)out + (size_t)(cg * 128 + rh * 64 + m * 16 + r) * (DM * 2) + (c3 * 80 + n * 16 + g * 4) * 2) = o;
    }
  }
  STAMP(7);
  asm volatile(".Lcode_end_mark:");
}
}

extern "C" void kernel_launch(void* const* d_in, const int* in_sizes, int n_in, void* d_out, int out_size, void* d_ws, size_t ws_size,
                              hipStream_t stream) {
  (void)hipFuncSetAttribute((const void*)fused_kernel, hipFuncAttributeMaxDynamicSharedMemorySize, LDS_BYTES);
  fused_kernel<<<256, 512, LDS_BYTES, stream>>>((const u16*)d_in[0], (const u16*)d_in[1], (const u16*)d_in[2], (const u16*)d_in[4],
                                                (const u16*)d_in[5], (u16*)d_out, (char*)d_ws);
}
```

```cpp
#include <hip/hip_runtime.h>
#include <stdint.h>
#include <type_traits>
#include <utility>

#define GAS __attribute__((address_space(1)))
#define LAS __attribute__((address_space(3)))
typedef unsigned short u16;
typedef unsigned long long u64;
typedef __bf16 v2bf __attribute__((ext_vector_type(2)));
typedef __bf16 v4bf __attribute__((ext_vector_type(4)));
typedef __bf16 v8bf __attribute__((ext_vector_type(8)));
typedef float f32x2 __attribute__((ext_vector_type(2)));
typedef float f32x4 __attribute__((ext_vector_type(4)));
typedef unsigned v4u __attribute__((ext_vector_type(4)));
typedef unsigned v2u __attribute__((ext_vector_type(2)));
typedef int v4i __attribute__((ext_vector_type(4)));

namespace {
constexpr int SEQ = 2048, DM = 640, FF = 1728;
constexpr int NCT = 148;
constexpr int H_BYTES = 64 * NCT * 1024;
constexpr size_t CTRL_OFF = 16u << 20;
constexpr int EPOCH_OFF = 0, RDY_OFF = 2048, FLAG_OFF = 8192, STAT_OFF = 65536;
constexpr size_t BT_OFF = 12u << 20;
constexpr int BT_SLICE = 74 * 5120;
constexpr int A_BYTES = 144 * 128, B_BYTES = 64 * 512, ST_BYTES = A_BYTES + B_BYTES;
constexpr int CS_OFF = 3 * ST_BYTES, RS_OFF = CS_OFF + 1024;
constexpr int RDYL_OFF = CS_OFF + 6144, EPL_OFF = CS_OFF + 7168;
constexpr int DUMP_OFF = CS_OFF + 4096;
constexpr int IMG_LD = 488;
constexpr int RING_OFF = 71680;
constexpr int LDS_BYTES = 163840;
constexpr int OOB = 0x7FF00000;
constexpr unsigned SPIN_CAP = 1u << 16;
#define SGB 1
#define NCLS 3
#define PFMASK 0x42
#define MAP3 1
#define POLL_SLEEP 8
#define STAMP(i)
#define SB __builtin_amdgcn_sched_barrier(0)
#define LDPOL2 " sc1"

__device__ __forceinline__ v4i mkrs(const void* p, int bytes) {
  u64 a = (u64)p; v4i r; r.x = (int)(unsigned)a; r.y = (int)((unsigned)(a >> 32) & 0xFFFFu); r.z = bytes; r.w = 0x00020000; return r;
}
__device__ __forceinline__ void dma16(v4i rs, unsigned lds, int voff, int soff) {
  asm volatile("s_mov_b32 m0, %0\n\ts_nop 0\n\tbuffer_load_dwordx4 %1, %2, %3 offen lds" ::"s"(lds), "v"(voff), "s"(rs), "s"(soff));
}
__device__ __forceinline__ void dma16_sc1(v4i rs, unsigned lds, int voff, int soff) {
  asm volatile("s_mov_b32 m0, %0\n\ts_nop 3\n\tbuffer_load_dwordx4 %1, %2, %3 offen" LDPOL2 " lds" ::"s"(lds), "v"(voff), "s"(rs), "s"(soff));
}
__device__ __forceinline__ void dma16_50(v4i rs, unsigned lds, int voff, int soff) {
  asm volatile("s_mov_b32 m0, %0\n\ts_mov_b32 exec_hi, 0x3ffff\n\tbuffer_load_dwordx4 %1, %2, %3 offen lds\n\ts_mov_b32 exec_hi, -1" ::"s"(lds), "v"(voff), "s"(rs), "s"(soff));
}
#define LDPOL " sc1"
#define STPOL " sc1"
template <int IMM> __device__ __forceinline__ v4u ld128_sc1(v4i rs, int voff, int soff) {
  v4u r; asm volatile("buffer_load_dwordx4 %0, %1, %2, %3 offen offset:%4" LDPOL : "=v"(r) : "v"(voff), "s"(rs), "s"(soff), "i"(IMM)); return r;
}
template <int IMM> __device__ __forceinline__ v4u ld128(v4i rs, int voff, int soff) {
  v4u r; asm volatile("buffer_load_dwordx4 %0, %1, %2, %3 offen offset:%4" : "=v"(r) : "v"(voff), "s"(rs), "s"(soff), "i"(IMM)); return r;
}
template <int IMM> __device__ __forceinline__ unsigned ld16(v4i rs, int voff, int soff) {
  unsigned r; asm volatile("buffer_load_ushort %0, %1, %2, %3 offen offset:%4" : "=v"(r) : "v"(voff), "s"(rs), "s"(soff), "i"(IMM)); return r;
}
__device__ __forceinline__ v2u ld64(v4i rs, int voff, int soff) {
  v2u r; asm volatile("buffer_load_dwordx2 %0, %1, %2, %3 offen" : "=v"(r) : "v"(voff), "s"(rs), "s"(soff)); return r;
}
__device__ __forceinline__ v2u ld64_sc1(v4i rs, int voff, int soff) {
  v2u r; asm volatile("buffer_load_dwordx2 %0, %1, %2, %3 offen sc1" : "=v"(r) : "v"(voff), "s"(rs), "s"(soff)); return r;
}
__device__ __forceinline__ v2u ld64_sc1_wait(v4i rs, int voff) {
  v2u r; asm volatile("buffer_load_dwordx2 %0, %1, %2, 0 offen sc1\n\ts_waitcnt vmcnt(0)" : "=&v"(r) : "v"(voff), "s"(rs) : "memory"); return r;
}
__device__ __forceinline__ void st128_sc1(v4i rs, int voff, int soff, v4u v) {
  asm volatile("buffer_store_dwordx4 %0, %1, %2, %3 offen" STPOL "\n\ts_nop 1" ::"v"(v), "v"(voff), "s"(rs), "s"(soff) : "memory");
}
__device__ __forceinline__ void st64_sc1(v4i rs, int voff, v2u v) {
  asm volatile("buffer_store_dwordx2 %0, %1, %2, 0 offen sc1" ::"v"(v), "v"(voff), "s"(rs) : "memory");
}
__device__ __forceinline__ void st32_sc1(v4i rs, int voff, unsigned v) {
  asm volatile("buffer_store_dword %0, %1, %2, 0 offen sc1" ::"v"(v), "v"(voff), "s"(rs) : "memory");
}
template <int N> __device__ __forceinline__ void waitbar() { asm volatile("s_waitcnt vmcnt(%0) lgkmcnt(0)\n\ts_barrier" ::"i"(N) : "memory"); }
__device__ __forceinline__ void ldsbar() { asm volatile("s_waitcnt lgkmcnt(0)\n\ts_barrier" ::: "memory"); }
__device__ __forceinline__ void drain() { asm volatile("s_waitcnt vmcnt(0)" ::: "memory"); }
__device__ __forceinline__ float bflo(unsigned u) { return __builtin_bit_cast(float, u << 16); }
__device__ __forceinline__ float bfhi(unsigned u) { return __builtin_bit_cast(float, u & 0xFFFF0000u); }
__device__ __forceinline__ unsigned pk2(float a, float b) {
  v2bf v; v.x = (__bf16)a; v.y = (__bf16)b; return __builtin_bit_cast(unsigned, v);
}
__device__ __forceinline__ float rbf(float a) { return (float)(__bf16)a; }
__device__ __forceinline__ float dot2(unsigned a, unsigned b, float c) {
  return __builtin_amdgcn_fdot2_f32_bf16(__builtin_bit_cast(v2bf, a), __builtin_bit_cast(v2bf, b), c, false);
}
__device__ __forceinline__ f32x4 mfma(v8bf a, v8bf b, f32x4 c) { return __builtin_amdgcn_mfma_f32_16x16x32_bf16(a, b, c, 0, 0, 0); }
__device__ __forceinline__ v8bf trfrag(LAS char* p, int second) {
  v4bf lo = __builtin_amdgcn_ds_read_tr16_b64_v4bf16((LAS v4bf*)p);
  v4bf hi = __builtin_amdgcn_ds_read_tr16_b64_v4bf16((LAS v4bf*)(p + second));
  return __builtin_shufflevector(lo, hi, 0, 1, 2, 3, 4, 5, 6, 7);
}
__device__ __forceinline__ float row8_sum(float v) {
  v += __builtin_bit_cast(float, __builtin_amdgcn_update_dpp(0, __builtin_bit_cast(int, v), 0xB1, 0xF, 0xF, false));
  v += __builtin_bit_cast(float, __builtin_amdgcn_update_dpp(0, __builtin_bit_cast(int, v), 0x4E, 0xF, 0xF, false));
  v += __builtin_bit_cast(float, __builtin_amdgcn_update_dpp(0, __builtin_bit_cast(int, v), 0x141, 0xF, 0xF, false));
  return v;
}
__device__ __forceinline__ float silu_f(float y) { return y * __builtin_amdgcn_rcpf(1.0f + __builtin_amdgcn_exp2f(y * -1.4426950408889634f)); }
template <class F, int... I> __device__ __forceinline__ void static_for_impl(F&& f, std::integer_sequence<int, I...>) { (f(std::integral_constant<int, I>{}), ...); }
template <int N, class F> __device__ __forceinline__ void static_for(F&& f) { static_for_impl(f, std::make_integer_sequence<int, N>{}); }

struct P1 {
  v4i rsA, rsB, rsH, rsC, rsT;
  int avoff, asvoff, bvoff, ldb2;
  int pfso, bid;
  int w, wm, lane, grp, mem, rot;
  const char* bt;
  int tstart, ftile0;
  int isffn, htile0;
  unsigned tag, taghi;
  int k0, k0t, halo, side;
  unsigned cpf;
};
__device__ __forceinline__ int hk0(int hw) { return 20 + 14 * hw - ((hw + 1) >> 2); }

template <int I> __device__ __forceinline__ void p1_req(const P1& c, unsigned base, int st) {
  st += c.rot; st = st >= 10 ? st - 10 : st;
  if (I == 0 && c.halo) dma16(c.rsA, base + c.w * 1024, c.asvoff, st * 128);
  if (I < 2) dma16(c.rsA, base + (2 + c.w + 8 * I) * 1024, c.avoff, st * 128 + I * 64 * (DM * 2));
  else dma16(c.rsB, base + A_BYTES + (c.w + 8 * (I - 2)) * 1024, c.bvoff, (st * 64 + (I - 2) * 16) * c.ldb2);
}
template <int E> __device__ __forceinline__ void tail_req(const P1& c, unsigned lds0) {
  dma16_sc1(c.rsT, lds0 + RING_OFF + E * 8192 + c.w * 1024, c.lane * 16, c.k0t * 5120 + E * 1024);
}

__device__ __forceinline__ void ldB5(v4u (&B)[5], v4i rsT, int voff, int so) {
  const int so2 = so == OOB ? OOB : so + 4096;
  B[0] = ld128_sc1<0>(rsT, voff, so); B[1] = ld128_sc1<1024>(rsT, voff, so); B[2] = ld128_sc1<2048>(rsT, voff, so);
  B[3] = ld128_sc1<3072>(rsT, voff, so); B[4] = ld128_sc1<0>(rsT, voff, so2);
}
#define TIE5(B) asm volatile("" : "+v"((B)[0]), "+v"((B)[1]), "+v"((B)[2]), "+v"((B)[3]), "+v"((B)[4]))

template <int I> __device__ __forceinline__ v4u ldB1(v4i rsT, int voff, int so) {
  return ld128_sc1<(I & 3) * 1024>(rsT, voff, I < 4 ? so : so + 4096);
}
template <int NT, int NOWN, int NFOR>
__device__ __forceinline__ void phase1(P1& c, LAS char* smem, unsigned lds0, char* ws, v4u (&B)[5][5]) {
  constexpr int NB = NT + NFOR, NX = NOWN + NFOR;
  const int lane = c.lane, r = lane & 15, g = lane >> 4, q = r >> 2, p = lane & 3;
  int aad[2];
#pragma unroll
  for (int ks = 0; ks < 2; ks++) aad[ks] = r * 128 + (((4 * ks + g) ^ (r >> 1)) << 4);
  const int amain = (1 + c.wm * 4) * 2048;
  const int z = q | ((g & 1) << 2);
  int tile[NB], bad[NB];
#pragma unroll
  for (int n = 0; n < NB; n++) {
    if (n < NT) { int t = n + 2 * c.wm; t = t >= NT ? t - NT : t; tile[n] = c.tstart + t; }
    else tile[n] = c.ftile0 + 2 * (n - NT);
    bad[n] = A_BYTES + (8 * g + q) * 512 + (((tile[n] & 7) ^ z) << 5) + (tile[n] >> 3) * 256 + p * 8;
  }
  f32x4 acc[4][NT], accx[NX];
#pragma unroll
  for (int m = 0; m < 4; m++)
#pragma unroll
    for (int n = 0; n < NT; n++) acc[m][n] = f32x4{0, 0, 0, 0};
#pragma unroll
  for (int n = 0; n < NX; n++) accx[n] = f32x4{0, 0, 0, 0};
  v8bf af[2][4], as[2], bf[2][NB];
  const bool ones = (r == 0) || c.isffn;
  auto ldq = [&](int buf, LAS char* base, int ks, int i) {
    af[buf][i] = *(LAS v8bf*)(base + aad[ks] + amain + i * 2048);
#pragma unroll
    for (int n = 0; n < NB; n++) if ((n & 3) == i) bf[buf][n] = trfrag(base + bad[n] + ks * 16384, 2048);
    if (i == 3) as[buf] = *(LAS v8bf*)(base + aad[ks]);
  };
  auto row = [&](int buf, int m) {
#pragma unroll
    for (int n = 0; n < NT; n++) acc[m][n] = mfma(bf[buf][n], af[buf][m], acc[m][n]);
    if (m == 3) {
      v4u s = __builtin_bit_cast(v4u, as[buf]);
#pragma unroll
      for (int e = 0; e < 4; e++) s[e] = ones ? 0x3F803F80u : s[e];
      v8bf sv = __builtin_bit_cast(v8bf, s);
#pragma unroll
      for (int n = 0; n < NX; n++) accx[n] = mfma(bf[buf][n < NOWN ? n : NT + n - NOWN], sv, accx[n]);
    }
#pragma unroll
    for (int i = 0; i < NT; i++) {
      __builtin_amdgcn_sched_group_barrier(0x8, 1, 0); __builtin_amdgcn_sched_group_barrier(0x100, 2, 0); __builtin_amdgcn_sched_group_barrier(0x2, 1, 0);
    }
    SB;
  };
  auto halfstage = [&](int buf, bool rd, LAS char* rbase, int rks, auto&& rq0, auto&& rq1, auto&& rq2) {
    if (rd) { ldq(buf ^ 1, rbase, rks, 0); ldq(buf ^ 1, rbase, rks, 1); }
    row(buf, 0); rq0(); SB;
    if (rd) ldq(buf ^ 1, rbase, rks, 2);
    row(buf, 1); rq1(); SB;
    if (rd) ldq(buf ^ 1, rbase, rks, 3);
    row(buf, 2); rq2(); SB;
    row(buf, 3);
  };
  auto none = [&]() {};
  auto lst = [&](int i) {};
#pragma unroll
  for (int m = 0; m < 4; m++)
#pragma unroll
    for (int n = 0; n < NT; n++) asm volatile("" : "+v"(acc[m][n]));
#pragma unroll
  for (int n = 0; n < NX; n++) asm volatile("" : "+v"(accx[n]));
#pragma unroll
  for (int n = 0; n < NB; n++) asm volatile("" : "+v"(bad[n]));
  asm volatile("" : "+v"(aad[0]), "+v"(aad[1]));
  if (c.side) ldsbar(); else waitbar<9>();
  asm volatile("" ::"v"(c.cpf));
  lst(0);
  STAMP(1);
  auto special = [&](int st) {
    if (st == 1) {
      const v2u ep = *(LAS v2u*)(smem + EPL_OFF);
      const u64 e = ((u64)(unsigned)__builtin_amdgcn_readfirstlane(ep.y) << 32) | (unsigned)__builtin_amdgcn_readfirstlane(ep.x);
      const u64 t = e * 0x5851F42D4C957F2Dull + 0x14057B7EF767814Full;
      c.tag = (unsigned)t; c.taghi = (unsigned)(t >> 32);
      if (c.w == 0) st32_sc1(c.rsC, lane == 0 ? RDY_OFF + c.bid * 4 : OOB, c.tag);
    } else if (st == 6) {
      if (c.w == 0) dma16_sc1(c.rsC, lds0 + RDYL_OFF, lane * 16, RDY_OFF);
    } else if (st == 7) {
      v4u rdy = *(LAS v4u*)(smem + RDYL_OFF + lane * 16);
      for (unsigned spins = 0;; spins++) {
        if (__all(rdy.x == c.tag && rdy.y == c.tag && rdy.z == c.tag && rdy.w == c.tag) || spins > SPIN_CAP) break;
        __builtin_amdgcn_s_sleep(POLL_SLEEP);
        asm volatile("buffer_load_dwordx4 %0, %1, %2, 0 offen offset:%3 sc1\n\ts_waitcnt vmcnt(0)" : "=&v"(rdy) : "v"(lane * 16), "s"(c.rsC), "i"(RDY_OFF) : "memory");
      }
      if (c.w < 2) dma16_sc1(c.rsC, lds0 + RS_OFF + c.w * 1024, lane * 16, STAT_OFF + c.grp * 2048 + c.w * 1024);
    }
  };
#pragma unroll
  for (int i = 0; i < 4; i++) ldq(0, smem, 0, i);
  int so = 0, po = 2 * ST_BYTES;
#pragma nounroll
  for (int st = 0; st < 7; st++) {
    const int no = so == 2 * ST_BYTES ? 0 : so + ST_BYTES;
    halfstage(0, true, smem + so, 1, [&]() { p1_req<3>(c, lds0 + po, st + 2); }, [&]() { p1_req<4>(c, lds0 + po, st + 2); }, [&]() { p1_req<5>(c, lds0 + po, st + 2); });
    waitbar<6>();
    SB;
    lst(st + 1);
    if ((PFMASK >> st) & 1) special(st);
    SB;
    halfstage(1, true, smem + no, 0, [&]() { p1_req<0>(c, lds0 + so, st + 3); }, [&]() { p1_req<1>(c, lds0 + so, st + 3); }, [&]() { p1_req<2>(c, lds0 + so, st + 3); });
    po = so; so = no;
  }
  halfstage(0, true, smem + ST_BYTES, 1, [&]() { p1_req<3>(c, lds0, 9); }, [&]() { p1_req<4>(c, lds0, 9); }, [&]() { p1_req<5>(c, lds0, 9); });
  waitbar<6>(); SB; lst(8);
  special(7); SB;
  halfstage(1, true, smem + 2 * ST_BYTES, 0, [&]() { tail_req<0>(c, lds0); }, [&]() { tail_req<1>(c, lds0); }, [&]() { tail_req<2>(c, lds0); });
  halfstage(0, true, smem + 2 * ST_BYTES, 1, none, none, none);
  waitbar<3>(); SB; lst(9);
  halfstage(1, true, smem, 0, [&]() { tail_req<3>(c, lds0); }, [&]() { tail_req<4>(c, lds0); }, [&]() { tail_req<5>(c, lds0); });
  halfstage(0, true, smem, 1, [&]() { tail_req<6>(c, lds0); }, [&]() { tail_req<7>(c, lds0); }, [&]() { tail_req<8>(c, lds0); });
  halfstage(1, false, smem, 0, [&]() { tail_req<9>(c, lds0); }, none, none);
  lst(10);
  STAMP(2);
#pragma unroll
  for (int n = 0; n < NX; n++)
    *(LAS f32x4*)(smem + (r == 0 ? CS_OFF + (tile[n < NOWN ? n : NT + n - NOWN] * 16 + 4 * g) * 4 : DUMP_OFF + lane * 16)) = accx[n];
  ldsbar();
  STAMP(8);
  f32x2 rs[4];
#pragma unroll
  for (int m = 0; m < 4; m++) rs[m] = *(LAS f32x2*)(smem + RS_OFF + (16 + c.wm * 64 + m * 16 + r) * 8);
  auto ldB = [&](int s) { SB; ldB5(B[s], c.rsT, lane * 16, (c.k0 + s) * 5120); SB; };
  if (c.isffn) {
    float c1[4];
#pragma unroll
    for (int m = 0; m < 4; m++) c1[m] = __builtin_amdgcn_rcpf(rs[m].y) * -1.4426950408889634f;
#pragma unroll
    for (int n = 0; n < NT; n++) {
      const f32x4 cs = *(LAS f32x4*)(smem + CS_OFF + (tile[n] * 16 + 4 * g) * 4);
#pragma unroll
      for (int mp = 0; mp < 2; mp++) {
        v4u o;
#pragma unroll
        for (int h = 0; h < 2; h++) {
          const int m = 2 * mp + h;
          const f32x4 t = acc[m][n] - rs[m].x * cs;
          const f32x4 zz = t * c1[m];
          f32x4 e;
#pragma unroll
          for (int j = 0; j < 4; j++) e[j] = __builtin_amdgcn_exp2f(zz[j]);
          const f32x4 d = e * rs[m].y + rs[m].y;
          f32x4 rr;
#pragma unroll
          for (int j = 0; j < 4; j++) rr[j] = __builtin_amdgcn_rcpf(d[j]);
          const f32x4 y = t * rr;
          o[2 * h] = pk2(y[0], y[1]); o[2 * h + 1] = pk2(y[2], y[3]);
        }
        st128_sc1(c.rsH, lane * 16, ((c.grp * 4 + c.wm * 2 + mp) * NCT + c.htile0 + tile[n]) * 1024, o);
      }
    }
  } else {
    float ri[4];
#pragma unroll
    for (int m = 0; m < 4; m++) ri[m] = __builtin_amdgcn_rcpf(rs[m].y);
    f32x4 cs[NB];
#pragma unroll
    for (int n = 0; n < NB; n++) cs[n] = *(LAS f32x4*)(smem + CS_OFF + (tile[n] * 16 + 4 * g) * 4);
    const f32x2 rh = *(LAS f32x2*)(smem + RS_OFF + r * 8);
    const float rih = __builtin_amdgcn_rcpf(rh.y);
    SB;
    static_for<4 * NT + NX>([&](auto U) {
      constexpr int u = decltype(U)::value;
      if constexpr (u < 15) B[u / 5][u % 5] = ldB1<u % 5>(c.rsT, lane * 16, (c.k0 + u / 5) * 5120);
      if constexpr (u < 4 * NT) {
        constexpr int n = u / 4, m = u % 4;
        const f32x4 y = (acc[m][n] - rs[m].x * cs[n]) * ri[m];
        v2u o; o.x = pk2(y[0], y[1]); o.y = pk2(y[2], y[3]);
        *(LAS v2u*)(smem + (16 + c.wm * 64 + m * 16 + r) * IMG_LD + tile[n] * 32 + g * 8) = o;
      } else {
        constexpr int x = u - 4 * NT, n = x < NOWN ? x : NT + x - NOWN;
        const f32x4 y = (accx[x] - rh.x * cs[n]) * rih;
        v2u o; o.x = pk2(y[0], y[1]); o.y = pk2(y[2], y[3]);
        *(LAS v2u*)(smem + r * IMG_LD + tile[n] * 32 + g * 8) = o;
      }
      SB;
    });
    ldsbar();
  }
}

__device__ __forceinline__ void wait_flags(const P1& c, int off) {
  for (unsigned spins = 0;; spins++) {
    unsigned f;
    asm volatile("buffer_load_dword %0, %1, %2, 0 offen sc1\n\ts_waitcnt vmcnt(0)" : "=&v"(f) : "v"(off), "s"(c.rsC) : "memory");
    if (__all(off == OOB || f == c.tag) || spins > SPIN_CAP) break;
    __builtin_amdgcn_s_sleep(POLL_SLEEP);
  }
}
__device__ __forceinline__ void mfma_ip(f32x4& acc, const v4u& b, const v4u& a) {
  asm volatile("v_mfma_f32_16x16x32_bf16 %0, %1, %2, %0" : "+v"(acc) : "v"(b), "v"(a));
}
__device__ __forceinline__ void swap_pair(v4u& l0, v4u& l1) {
  unsigned a = l0.z, b = l1.x, c = l0.w, d = l1.y;
  asm volatile("v_swap_b32 %0, %1" : "+v"(a), "+v"(b));
  l0.z = a; l1.x = b;
  asm volatile("v_swap_b32 %0, %1" : "+v"(c), "+v"(d));
  l0.w = c; l1.y = d;
}
template <int I> __device__ __forceinline__ v4u ldA1(v4i rsH, int voff, int so) {
  return ld128_sc1<(I & 1) * 1024>(rsH, voff, I < 2 ? so : so + NCT * 1024);
}
__device__ __forceinline__ void ldA4(v4u (&A)[4], v4i rsH, int voff, int so) {
  const int so2 = so == OOB ? OOB : so + NCT * 1024;
  A[0] = ld128_sc1<0>(rsH, voff, so); A[1] = ld128_sc1<1024>(rsH, voff, so);
  A[2] = ld128_sc1<0>(rsH, voff, so2); A[3] = ld128_sc1<1024>(rsH, voff, so2);
}
#define WAITA(A, N) asm volatile("s_waitcnt vmcnt(%4) lgkmcnt(0)" : "+v"((A)[0]), "+v"((A)[1]), "+v"((A)[2]), "+v"((A)[3]) : "i"(N) : "memory")
constexpr int h_na(int t) { return t == 1 ? 2 : (t + 3 < 14 ? 1 : 0); }
constexpr int h_a0(int t) { int n = 2; for (int i = 0; i < t; i++) n += h_na(i); return n; }
constexpr int hcnt(int t) { return 4 * h_na(t) + ((t >= 6 && t <= 10) ? 5 : 0) + (t == 8 ? 3 : 0); }
constexpr int hwait(int s) {
  int n = 0;
  if (s >= 9) { n = (s - 3 == 8 ? 3 : 0); for (int t = s - 2; t < s; t++) n += hcnt(t); return n; }
  if (s < 2) { n = 4 * (1 - s); for (int t = 0; t < s; t++) n += hcnt(t); return n; }
  int t0 = 0; while (h_a0(t0) + h_na(t0) <= s) t0++;
  n = hcnt(t0) - 4 * (s - h_a0(t0) + 1);
  for (int t = t0 + 1; t < s; t++) n += hcnt(t);
  return n;
}

typedef unsigned s8u __attribute__((ext_vector_type(8)));
__global__ __launch_bounds__(512, 2) void fused_kernel(const u16* x_, const u16* wqkv_, const u16* wo_, const u16* w1_, const u16* w2_, u16* out_, char* ws_) {
  extern __shared__ __attribute__((aligned(16))) char smem_[];
  LAS char* smem = (LAS char*)smem_;
  const unsigned lds0 = (unsigned)(size_t)smem;
  s8u ka; v4u kb; v2u kc;
  asm volatile("s_load_dwordx8 %0, %3, 0x0\n\ts_load_dwordx4 %1, %3, 0x20\n\ts_load_dwordx2 %2, %3, 0x30"
               : "=&s"(ka), "=&s"(kb), "=&s"(kc) : "s"(__builtin_amdgcn_kernarg_segment_ptr()));
  SB;
  const int tid = threadIdx.x, lane = tid & 63, w = __builtin_amdgcn_readfirstlane(tid >> 6);
  const int bid = blockIdx.x, xcd = bid & 7, idx = bid >> 3, half = xcd & 1, j8 = idx & 7;
  const int grp = (xcd >> 1) * 4 + (idx >> 3), mem = half * 8 + j8;
  const int isffn = j8 >> 2, sub = half * 4 + (j8 & 3);
  unsigned cpf;
  {
    u64 pc0; unsigned codelen;
    asm volatile("s_getpc_b64 %0\n.Lcode_begin_mark:\n\ts_mov_b32 %1, .Lcode_end_mark-.Lcode_begin_mark" : "=s"(pc0), "=s"(codelen));
    const v4i rp = mkrs((const void*)pc0, (int)codelen);
    asm volatile("buffer_load_dword %0, %1, %2, %3 offen" : "=v"(cpf) : "v"(lane < 2 ? lane * 128 : OOB), "s"(rp), "s"((idx * 8 + w) * 256));
  }
  P1 c;
  c.ldb2 = (3 * DM - isffn * (3 * DM - FF)) * 2;
  c.w = w; c.lane = lane; c.grp = grp; c.isffn = isffn; c.mem = mem;
  const int ftile0 = sub * 14 - half * (j8 & 3);
  const int ntile = 15 - isffn * (1 + half);
  {
    const int ar = lane >> 3, sw = ((lane & 7) ^ ((4 * w + (lane >> 4)) & 7)) << 4;
    c.avoff = (grp * 128 + 8 * w + ar) * (DM * 2) + sw;
    int hr = grp * 128 - 16 + 8 * w + ar; hr = hr < 0 ? 0 : hr;
    c.asvoff = hr * (DM * 2) + sw;
    const int kk = 2 * w + (lane >> 5), zz = (kk & 3) | (((kk >> 3) & 1) << 2), pc = lane & 31, seg = (pc >> 1) ^ zz;
    const int part = (seg * 13) >> 6;
    const int col = seg * 16 + (isffn ? ftile0 * 16 : sub * 80) + (isffn ? 0 : DM - 80) * part;
    const int bo = kk * c.ldb2 + col * 2 + (pc & 1) * 16;
    c.bvoff = seg < ntile ? bo : OOB;
  }
  c.halo = !isffn && w < 2;
  { const int cl = (j8 + grp + half) % NCLS; c.rot = cl * 10 / NCLS; }
  asm volatile("" : "+v"(c.avoff), "+v"(c.asvoff), "+v"(c.bvoff), "+s"(c.rot));
  asm volatile("s_waitcnt lgkmcnt(0)" : "+s"(ka), "+s"(kb), "+s"(kc));
  auto ptr = [](unsigned lo, unsigned hi) { return (char*)(((u64)hi << 32) | lo); };
  const u16* x = (const u16*)ptr(ka[0], ka[1]); const u16* wqkv = (const u16*)ptr(ka[2], ka[3]); const u16* wo = (const u16*)ptr(ka[4], ka[5]);
  const u16* w1 = (const u16*)ptr(ka[6], ka[7]); const u16* w2 = (const u16*)ptr(kb[0], kb[1]); u16* out = (u16*)ptr(kb[2], kb[3]);
  char* ws = ptr(kc[0], kc[1]);
  c.rsA = mkrs(x, SEQ * DM * 2);
  c.rsB = mkrs(isffn ? w1 : wqkv, DM * c.ldb2);
  static_for<6>([&](auto I) { p1_req<decltype(I)::value>(c, lds0, 0); });
  STAMP(0);
  c.rsH = mkrs(ws, H_BYTES);
  c.rsC = mkrs(ws + CTRL_OFF, 1 << 20);
  const int cg = (xcd >> 1) * 4 + half * 2 + (idx >> 4), rh = (idx >> 3) & 1, c3 = idx & 7;
  c.bt = ws + BT_OFF; c.bid = bid;
  c.rsT = mkrs(ws + BT_OFF + (size_t)c3 * BT_SLICE, BT_SLICE);
  c.wm = w >> 2;
  c.k0 = w < 4 ? 5 * w : hk0(w & 3);
  c.k0t = hk0(w & 3) + 5 + 2 * (w >> 2);
  c.tag = 0; c.taghi = 0;
  const int wn = w < 4 ? w : 7 - w;
  c.htile0 = 40 + ftile0;
  const int tsc = w == 3 ? CS_OFF : 2 * ST_BYTES + A_BYTES + 8192 + (w - 4) * 5120;
  v4u xs[10];
  const int sslot = w * 8 + (lane >> 3);
  const int kap = (7 - w) * 16 + grp;
  const bool dostat = isffn && w < 3, dotile = isffn && w >= 3 && kap < 74;
  if (dostat) {
    int rho = sub * 18 + (sslot < 18 ? sslot : 17);
    int row = grp * 128 - 16 + rho; row = row < 0 ? 0 : row;
    const int vo = row * (DM * 2) + (lane & 7) * 16;
    static_for<10>([&](auto I) { constexpr int i = decltype(I)::value; xs[i] = ld128<128 * i>(c.rsA, vo, 0); });
  } else if (dotile) {
    const v4i rw = mkrs(kap < 20 ? wo : w2, kap < 20 ? DM * DM * 2 : DM * FF * 2);
    int kq = kap;
    if (kap >= 20) asm volatile("s_sub_u32 %0, %0, 20" : "+s"(kq)::"scc");
    const int so = kq * 32 * (DM * 2);
#pragma unroll
    for (int i = 0; i < 5; i++) {
      const int pc = 64 * i + lane, rr = pc / 10;
      dma16(rw, lds0 + tsc + i * 1024, rr * (DM * 2) + sub * 160 + (pc - 10 * rr) * 16, so);
    }
  }
  static_for<6>([&](auto I) { p1_req<decltype(I)::value>(c, lds0 + ST_BYTES, 1); });
  static_for<3>([&](auto I) { p1_req<decltype(I)::value>(c, lds0 + 2 * ST_BYTES, 2); });
  c.side = dostat || dotile; c.cpf = cpf;
  if (c.side) asm volatile("s_waitcnt vmcnt(9)" ::: "memory");
  if (w == 7) dma16_sc1(c.rsC, lds0 + EPL_OFF, lane == 0 ? EPOCH_OFF : OOB, 0);
  if (dostat) {
    asm volatile("" : "+v"(xs[0]), "+v"(xs[1]), "+v"(xs[2]), "+v"(xs[3]), "+v"(xs[4]), "+v"(xs[5]), "+v"(xs[6]), "+v"(xs[7]), "+v"(xs[8]), "+v"(xs[9])::"memory");
    float s0 = 0, s1 = 0, q0 = 0, q1 = 0;
#pragma unroll
    for (int i = 0; i < 40; i += 2) {
      const unsigned a = xs[i >> 2][i & 3], b = xs[i >> 2][(i & 3) + 1];
      s0 = dot2(a, 0x3F803F80u, s0); s1 = dot2(b, 0x3F803F80u, s1); q0 = dot2(a, a, q0); q1 = dot2(b, b, q1);
    }
    const float mean = row8_sum(s0 + s1) * (1.0f / DM);
    const float var = fmaxf(row8_sum(q0 + q1) * (1.0f / DM) - mean * mean, 0.f);
    const float sd = __builtin_sqrtf(rbf(rbf(var) + 1e-5f));
    v2u gr; gr.x = __builtin_bit_cast(unsigned, rbf(mean)); gr.y = __builtin_bit_cast(unsigned, rbf(sd));
    st64_sc1(c.rsC, ((lane & 7) == 0 && sslot < 18) ? STAT_OFF + grp * 2048 + (sub * 18 + sslot) * 8 : OOB, gr);
  } else if (dotile) {
    LAS char* tb = smem + tsc + ((lane >> 4) * 4 + ((lane & 15) >> 2)) * 160 + (lane & 3) * 8;
#pragma unroll
    for (int n = 0; n < 5; n++)
      st128_sc1(mkrs(ws + BT_OFF, 8 * BT_SLICE), lane * 16, sub * BT_SLICE + (kap * 5 + n) * 1024, __builtin_bit_cast(v4u, trfrag(tb + n * 32, 16 * 160)));
  }
  v4u B[5][5];
  {
    const int n4 = ntile - 12;
    c.tstart = wn <= n4 ? 4 * wn : 4 * n4 + 3 * (wn - n4);
    if (!isffn) c.ftile0 = 1 + 6 * c.wm;
    else { const int fi = (wn - n4) * 2 + c.wm; c.ftile0 = 1 + 2 * (n4 == 2 ? fi : (fi & 1)); }
    if (wn < n4) phase1<4, 1, 0>(c, smem, lds0, ws, B);
    else if (!isffn) phase1<3, 2, 3>(c, smem, lds0, ws, B);
    else phase1<3, 2, 1>(c, smem, lds0, ws, B);
  }
  STAMP(9);
  const int r = lane & 15, g = lane >> 4;
  const int k0 = c.k0;
  auto ldB = [&](int s) { SB; ldB5(B[s], c.rsT, lane * 16, (k0 + s) * 5120); SB; };
  if (!isffn) {
    const int row = (w & 1) * 64 + lane;
    const int pos = grp * 128 + row;
    LAS char* qp = smem + (row + 16) * IMG_LD + (w >> 1) * 40;
    unsigned qv[10];
#pragma unroll
    for (int i = 0; i < 5; i++) { const v2u t = *(LAS v2u*)(qp + i * 8); qv[2 * i] = t.x; qv[2 * i + 1] = t.y; }
    float lg[8];
    static_for<8>([&](auto K) {
      constexpr int k = decltype(K)::value;
      if constexpr (k < 5) B[3][k] = ldB1<k>(c.rsT, lane * 16, (k0 + 3) * 5120);
      LAS char* kp = qp + 160 - 2 * k * IMG_LD;
      float a0 = 0, a1 = 0;
#pragma unroll
      for (int i = 0; i < 5; i++) { const v2u t = *(LAS v2u*)(kp + i * 8); a0 = dot2(qv[2 * i], t.x, a0); a1 = dot2(qv[2 * i + 1], t.y, a1); }
      lg[k] = pos - 2 * k >= 0 ? a0 + a1 : -4e9f;
    });
    float mx = lg[0];
#pragma unroll
    for (int k = 1; k < 8; k++) mx = fmaxf(mx, lg[k]);
    float sum = 0;
    constexpr float C = 0.22360679774997896f * 1.4426950408889634f;
#pragma unroll
    for (int k = 0; k < 8; k++) { lg[k] = __builtin_amdgcn_exp2f((lg[k] - mx) * C); sum += lg[k]; }
    const float inv = __builtin_amdgcn_rcpf(sum);
    float o[20];
#pragma unroll
    for (int d = 0; d < 20; d++) o[d] = 0;
    static_for<8>([&](auto K) {
      constexpr int k = decltype(K)::value;
      if constexpr (k < 5) B[4][k] = ldB1<k>(c.rsT, lane * 16, (k0 + 4) * 5120);
      const unsigned plo = pk2(lg[k] * inv, 0.f), phi = plo << 16;
      LAS char* vp = qp + 320 - 2 * k * IMG_LD;
#pragma unroll
      for (int i = 0; i < 5; i++) {
        const v2u t = *(LAS v2u*)(vp + i * 8);
        o[4 * i] = dot2(t.x, plo, o[4 * i]); o[4 * i + 1] = dot2(t.x, phi, o[4 * i + 1]);
        o[4 * i + 2] = dot2(t.y, plo, o[4 * i + 2]); o[4 * i + 3] = dot2(t.y, phi, o[4 * i + 3]);
      }
    });
#pragma unroll
    for (int i = 0; i < 5; i++) { v2u t; t.x = pk2(o[4 * i], o[4 * i + 1]); t.y = pk2(o[4 * i + 2], o[4 * i + 3]); *(LAS v2u*)(qp + i * 8) = t; }
    STAMP(11);
    ldsbar();
#pragma unroll
    for (int i = 0; i < 3; i++) {
      const int b0 = w + 8 * i, b = b0 < 20 ? b0 : 19, rp = b / 5, t = b - 5 * rp;
      LAS char* sp = smem + (16 + rp * 32 + r) * IMG_LD + t * 32 + g * 8;
      const v2u lo = *(LAS v2u*)sp, hi = *(LAS v2u*)(sp + 16 * IMG_LD);
      v4u v; v.x = lo.x; v.y = lo.y; v.z = hi.x; v.w = hi.y;
      st128_sc1(c.rsH, lane * 16, b0 < 20 ? ((grp * 4 + rp) * NCT + sub * 5 + t) * 1024 : OOB, v);
    }
  }
  waitbar<0>();
  st32_sc1(c.rsC, tid == 0 ? FLAG_OFF + grp * 256 + mem * 4 : OOB, c.tag);
  STAMP(4);
  if (isffn) {
    if (w < 4) __builtin_amdgcn_s_sleep(40);
    ldB(0); ldB(1); ldB(2); ldB(3); ldB(4);
  }
  {
    int m;
    if (w < 4) m = lane < 2 ? (w >> 1) * 8 + (w & 1) * 2 + lane : -1;
    else {
      const int t0 = 2 * (k0 - 20), t1 = t0 + 27 - (w & 2);
      const int f0 = t0 < 56 ? t0 / 14 : 4 + (t0 - 56) / 13, f1 = t1 < 56 ? t1 / 14 : 4 + (t1 - 56) / 13, f = f0 + lane;
      m = f <= f1 ? (f >> 2) * 8 + 4 + (f & 3) : -1;
    }
    wait_flags(c, m >= 0 ? FLAG_OFF + cg * 256 + m * 4 : OOB);
  }
  STAMP(5);
  const int hso = ((cg * 4 + rh * 2) * NCT + 2 * k0) * 1024;
  v2u xr[3];
  auto ldx = [&]() {
#pragma unroll
    for (int i = 0; i < 3; i++) {
      const int tl = w + 8 * i, tt = tl < 20 ? tl : 19, m = tt / 5, n = tt - 5 * m;
      xr[i] = ld64(c.rsA, (cg * 128 + rh * 64 + m * 16 + r) * (DM * 2) + (c3 * 80 + n * 16 + g * 4) * 2, 0);
    }
  };
  auto put = [&](const f32x4 (&acc)[4][5], int p0, int p1, int stride) {
    STAMP(6);
    asm volatile("s_nop 7\n\ts_nop 3\n\ts_waitcnt vmcnt(0) lgkmcnt(0)" : "+v"(xr[0]), "+v"(xr[1]), "+v"(xr[2])::"memory");
#pragma unroll
    for (int m = 0; m < 4; m++)
#pragma unroll
      for (int n = 0; n < 5; n++) *(LAS f32x4*)(smem + (m < 2 ? p0 : p1) + ((m & 1) * 5 + n) * stride + lane * 16) = acc[m][n];
  };
  const int vo = lane * 16;
  if (w < 4) {
    v4u A[5][4];
    auto ldA = [&](int s) { ldA4(A[s], c.rsH, vo, hso + s * 2048); };
    ldA(0); ldA(1); ldA(2);
    f32x4 acc[4][5];
#pragma unroll
    for (int m = 0; m < 4; m++)
#pragma unroll
      for (int n = 0; n < 5; n++) acc[m][n] = f32x4{0, 0, 0, 0};
#pragma unroll
    for (int m = 0; m < 4; m++) TIE5(acc[m]);
#pragma unroll
    for (int s = 0; s < 5; s++) TIE5(B[s]);
    WAITA(A[0], 8); swap_pair(A[0][0], A[0][1]); swap_pair(A[0][2], A[0][3]);
    asm volatile("s_nop 1");
    static_for<5>([&](auto S) {
      constexpr int s = decltype(S)::value;
      static_for<20>([&](auto I) {
        constexpr int i = decltype(I)::value;
        mfma_ip(acc[i / 5][i % 5], B[s][i % 5], A[s][i / 5]);
        if constexpr ((s == 1 || s == 2) && i < 8 && (i & 1)) A[s + 2][i >> 1] = ldA1<(i >> 1)>(c.rsH, vo, hso + (s + 2) * 2048);
        if constexpr (s == 2 && i == 9) ldx();
        if constexpr (s < 4 && i == 13) WAITA(A[s + 1], (s == 0 ? 4 : s == 1 ? 4 : s == 2 ? 7 : 3));
        if constexpr (s < 4 && i == 14) swap_pair(A[s + 1][0], A[s + 1][1]);
        if constexpr (s < 4 && i == 15) swap_pair(A[s + 1][2], A[s + 1][3]);
      });
    });
    put(acc, w * 20480, w < 3 ? w * 20480 + 10240 : CS_OFF, 1024);
#pragma unroll
    for (int s = 0; s < 5; s++)
#pragma unroll
      for (int n = 0; n < 5; n++) asm volatile("" : "=v"(B[s][n]));
  }
  int w_h = w;
  asm volatile("" : "+s"(w_h));
  if (w_h >= 4) {
    const int hw = w & 3;
    v4u A[14][4], Bs[14][5];
    const int last = hw < 2 ? 0 : OOB;
    ldA4(A[0], c.rsH, vo, hso); ldA4(A[1], c.rsH, vo, hso + 2048);
    f32x4 acc[4][5];
#pragma unroll
    for (int m = 0; m < 4; m++)
#pragma unroll
      for (int n = 0; n < 5; n++) acc[m][n] = f32x4{0, 0, 0, 0};
#pragma unroll
    for (int m = 0; m < 4; m++) TIE5(acc[m]);
    LAS char* eb = smem + RING_OFF + hw * 1024 + lane * 16;
#pragma unroll
    for (int s = 0; s < 5; s++) {
      TIE5(B[s]);
#pragma unroll
      for (int n = 0; n < 5; n++) Bs[s][n] = B[s][n];
    }
    WAITA(A[0], 4); swap_pair(A[0][0], A[0][1]); swap_pair(A[0][2], A[0][3]);
    asm volatile("s_nop 1");
    static_for<14>([&](auto S) {
      constexpr int s = decltype(S)::value;
      static_for<20>([&](auto I) {
        constexpr int i = decltype(I)::value;
        mfma_ip(acc[i / 5][i % 5], Bs[s][i % 5], A[s][i / 5]);
        if constexpr (i < 4 * h_na(s)) {
          constexpr int k = h_a0(s) + (i >> 2);
          A[k][i & 3] = ldA1<(i & 3)>(c.rsH, k == 13 ? (vo | last) : vo, hso + k * 2048);
        }
        if constexpr (s >= 6 && s <= 10 && i >= 4 && i < 9) Bs[s + 3][i - 4] = ldB1<i - 4>(c.rsT, s == 10 ? (vo | last) : vo, (k0 + s + 3) * 5120);
        if constexpr (s == 8 && i == 9) ldx();
        if constexpr (s >= 4 && s < 8 && i >= 9 && i < 14) {
          SB; Bs[s + 1][i - 9] = *(LAS v4u*)(eb + ((s & 1) * 5 + i - 9) * 8192 + ((s - 4) >> 1) * 4096); SB;
        }
        if constexpr (s < 13 && i == 14) {
          asm volatile("s_waitcnt vmcnt(%4)" : "+v"(A[s + 1][0]), "+v"(A[s + 1][1]), "+v"(A[s + 1][2]), "+v"(A[s + 1][3]) : "i"(hwait(s + 1)) : "memory");
          if constexpr (s + 1 >= 9) TIE5(Bs[s + 1]);
        }
        if constexpr (s < 13 && i == 15) swap_pair(A[s + 1][0], A[s + 1][1]);
        if constexpr (s < 13 && i == 16) swap_pair(A[s + 1][2], A[s + 1][3]);
      });
    });
    put(acc, RING_OFF + hw * 1024, RING_OFF + hw * 1024 + 4096, 8192);
  }
  STAMP(12);
  ldsbar();
  STAMP(13);
  if (w == 0 && bid == 0) { v2u t; t.x = c.tag; t.y = c.taghi; st64_sc1(c.rsC, lane == 0 ? EPOCH_OFF : OOB, t); }
#pragma unroll
  for (int i = 0; i < 3; i++) {
    const int tl = w + 8 * i;
    if (tl < 20) {
      const int m = tl / 5, n = tl - 5 * m;
      LAS char* ps = smem + tl * 1024 + lane * 16;
      LAS char* pf = smem + RING_OFF + (tl < 10 ? tl * 8192 : (tl - 10) * 8192 + 4096) + lane * 16;
      const f32x4 s = (*(LAS f32x4*)ps + *(LAS f32x4*)(ps + 20480)) + (*(LAS f32x4*)(ps + 40960) + *(LAS f32x4*)(ps + (tl < 10 ? 61440 : CS_OFF - 10240)));
      const f32x4 f = (*(LAS f32x4*)pf + *(LAS f32x4*)(pf + 1024)) + (*(LAS f32x4*)(pf + 2048) + *(LAS f32x4*)(pf + 3072));
      float y[4];
#pragma unroll
      for (int j = 0; j < 4; j++) {
        const float xv = (j & 1) ? bfhi(xr[i][j >> 1]) : bflo(xr[i][j >> 1]);
        y[j] = xv + s[j] * silu_f(f[j]);
      }
      v2u o; o.x = pk2(y[0], y[1]); o.y = pk2(y[2], y[3]);
      *(GAS v2u*)((GAS char*)out + (size_t)(cg * 128 + rh * 64 + m * 16 + r) * (DM * 2) + (c3 * 80 + n * 16 + g * 4) * 2) = o;
    }
  }
  STAMP(7);
  asm volatile(".Lcode_end_mark:");
}
}

extern "C" void kernel_launch(void* const* d_in, const int* in_sizes, int n_in, void* d_out, int out_size, void* d_ws, size_t ws_size,
                              hipStream_t stream) {
  (void)hipFuncSetAttribute((const void*)fused_kernel, hipFuncAttributeMaxDynamicSharedMemorySize, LDS_BYTES);
  fused_kernel<<<256, 512, LDS_BYTES, stream>>>((const u16*)d_in[0], (const u16*)d_in[1], (const u16*)d_in[2], (const u16*)d_in[4],
                                                (const u16*)d_in[5], (u16*)d_out, (char*)d_ws);
}
```

```cpp
#include <hip/hip_runtime.h>
#include <stdint.h>
#include <type_traits>
#include <utility>

#define GAS __attribute__((address_space(1)))
#define LAS __attribute__((address_space(3)))
typedef unsigned short u16;
typedef unsigned long long u64;
typedef __bf16 v2bf __attribute__((ext_vector_type(2)));
typedef __bf16 v4bf __attribute__((ext_vector_type(4)));
typedef __bf16 v8bf __attribute__((ext_vector_type(8)));
typedef float f32x2 __attribute__((ext_vector_type(2)));
typedef float f32x4 __attribute__((ext_vector_type(4)));
typedef unsigned v4u __attribute__((ext_vector_type(4)));
typedef unsigned v2u __attribute__((ext_vector_type(2)));
typedef int v4i __attribute__((ext_vector_type(4)));

namespace {
constexpr int SEQ = 2048, DM = 640, FF = 1728;
constexpr int NCT = 148;
constexpr int H_BYTES = 64 * NCT * 1024;
constexpr size_t CTRL_OFF = 16u << 20;
constexpr int EPOCH_OFF = 0, RDY_OFF = 2048, FLAG_OFF = 8192, STAT_OFF = 65536;
constexpr size_t BT_OFF = 12u << 20;
constexpr int BT_SLICE = 74 * 5120;
constexpr int A_BYTES = 144 * 128, B_BYTES = 64 * 512, ST_BYTES = A_BYTES + B_BYTES;
constexpr int CS_OFF = 3 * ST_BYTES, RS_OFF = CS_OFF + 1024;
constexpr int RDYL_OFF = CS_OFF + 6144, EPL_OFF = CS_OFF + 7168;
constexpr int DUMP_OFF = CS_OFF + 4096;
constexpr int IMG_LD = 488;
constexpr int RING_OFF = 71680;
constexpr int LDS_BYTES = 163840;
constexpr int OOB = 0x7FF00000;
constexpr unsigned SPIN_CAP = 1u << 16;
#define SGB 1
#define NCLS 3
#define PFMASK 0x42
#define MAP3 1
#define POLL_SLEEP 8
#define STAMP(i)
#define SB __builtin_amdgcn_sched_barrier(0)
#define LDPOL2 " sc1"

__device__ __forceinline__ v4i mkrs(const void* p, int bytes) {
  u64 a = (u64)p; v4i r; r.x = (int)(unsigned)a; r.y = (int)((unsigned)(a >> 32) & 0xFFFFu); r.z = bytes; r.w = 0x00020000; return r;
}
__device__ __forceinline__ void dma16(v4i rs, unsigned lds, int voff, int soff) {
  asm volatile("s_mov_b32 m0, %0\n\ts_nop 0\n\tbuffer_load_dwordx4 %1, %2, %3 offen lds" ::"s"(lds), "v"(voff), "s"(rs), "s"(soff));
}
__device__ __forceinline__ void dma16_sc1(v4i rs, unsigned lds, int voff, int soff) {
  asm volatile("s_mov_b32 m0, %0\n\ts_nop 3\n\tbuffer_load_dwordx4 %1, %2, %3 offen" LDPOL2 " lds" ::"s"(lds), "v"(voff), "s"(rs), "s"(soff));
}
__device__ __forceinline__ void dma16_50(v4i rs, unsigned lds, int voff, int soff) {
  asm volatile("s_mov_b32 m0, %0\n\ts_mov_b32 exec_hi, 0x3ffff\n\tbuffer_load_dwordx4 %1, %2, %3 offen lds\n\ts_mov_b32 exec_hi, -1" ::"s"(lds), "v"(voff), "s"(rs), "s"(soff));
}
#define LDPOL " sc1"
#define STPOL " sc1"
template <int IMM> __device__ __forceinline__ v4u ld128_sc1(v4i rs, int voff, int soff) {
  v4u r; asm volatile("buffer_load_dwordx4 %0, %1, %2, %3 offen offset:%4" LDPOL : "=v"(r) : "v"(voff), "s"(rs), "s"(soff), "i"(IMM)); return r;
}
template <int IMM> __device__ __forceinline__ v4u ld128(v4i rs, int voff, int soff) {
  v4u r; asm volatile("buffer_load_dwordx4 %0, %1, %2, %3 offen offset:%4" : "=v"(r) : "v"(voff), "s"(rs), "s"(soff), "i"(IMM)); return r;
}
template <int IMM> __device__ __forceinline__ unsigned ld16(v4i rs, int voff, int soff) {
  unsigned r; asm volatile("buffer_load_ushort %0, %1, %2, %3 offen offset:%4" : "=v"(r) : "v"(voff), "s"(rs), "s"(soff), "i"(IMM)); return r;
}
__device__ __forceinline__ v2u ld64(v4i rs, int voff, int soff) {
  v2u r; asm volatile("buffer_load_dwordx2 %0, %1, %2, %3 offen" : "=v"(r) : "v"(voff), "s"(rs), "s"(soff)); return r;
}
__device__ __forceinline__ v2u ld64_sc1(v4i rs, int voff, int soff) {
  v2u r; asm volatile("buffer_load_dwordx2 %0, %1, %2, %3 offen sc1" : "=v"(r) : "v"(voff), "s"(rs), "s"(soff)); return r;
}
__device__ __forceinline__ v2u ld64_sc1_wait(v4i rs, int voff) {
  v2u r; asm volatile("buffer_load_dwordx2 %0, %1, %2, 0 offen sc1\n\ts_waitcnt vmcnt(0)" : "=&v"(r) : "v"(voff), "s"(rs) : "memory"); return r;
}
__device__ __forceinline__ void st128_sc1(v4i rs, int voff, int soff, v4u v) {
  asm volatile("buffer_store_dwordx4 %0, %1, %2, %3 offen" STPOL "\n\ts_nop 1" ::"v"(v), "v"(voff), "s"(rs), "s"(soff) : "memory");
}
__device__ __forceinline__ void st64_sc1(v4i rs, int voff, v2u v) {
  asm volatile("buffer_store_dwordx2 %0, %1, %2, 0 offen sc1" ::"v"(v), "v"(voff), "s"(rs) : "memory");
}
__device__ __forceinline__ void st32_sc1(v4i rs, int voff, unsigned v) {
  asm volatile("buffer_store_dword %0, %1, %2, 0 offen sc1" ::"v"(v), "v"(voff), "s"(rs) : "memory");
}
template <int N> __device__ __forceinline__ void waitbar() { asm volatile("s_waitcnt vmcnt(%0) lgkmcnt(0)\n\ts_barrier" ::"i"(N) : "memory"); }
__device__ __forceinline__ void ldsbar() { asm volatile("s_waitcnt lgkmcnt(0)\n\ts_barrier" ::: "memory"); }
__device__ __forceinline__ void drain() { asm volatile("s_waitcnt vmcnt(0)" ::: "memory"); }
__device__ __forceinline__ float bflo(unsigned u) { return __builtin_bit_cast(float, u << 16); }
__device__ __forceinline__ float bfhi(unsigned u) { return __builtin_bit_cast(float, u & 0xFFFF0000u); }
__device__ __forceinline__ unsigned pk2(float a, float b) {
  v2bf v; v.x = (__bf16)a; v.y = (__bf16)b; return __builtin_bit_cast(unsigned, v);
}
__device__ __forceinline__ float rbf(float a) { return (float)(__bf16)a; }
__device__ __forceinline__ float dot2(unsigned a, unsigned b, float c) {
  return __builtin_amdgcn_fdot2_f32_bf16(__builtin_bit_cast(v2bf, a), __builtin_bit_cast(v2bf, b), c, false);
}
__device__ __forceinline__ f32x4 mfma(v8bf a, v8bf b, f32x4 c) { return __builtin_amdgcn_mfma_f32_16x16x32_bf16(a, b, c, 0, 0, 0); }
__device__ __forceinline__ v8bf trfrag(LAS char* p, int second) {
  v4bf lo = __builtin_amdgcn_ds_read_tr16_b64_v4bf16((LAS v4bf*)p);
  v4bf hi = __builtin_amdgcn_ds_read_tr16_b64_v4bf16((LAS v4bf*)(p + second));
  return __builtin_shufflevector(lo, hi, 0, 1, 2, 3, 4, 5, 6, 7);
}
__device__ __forceinline__ float row8_sum(float v) {
  v += __builtin_bit_cast(float, __builtin_amdgcn_update_dpp(0, __builtin_bit_cast(int, v), 0xB1, 0xF, 0xF, false));
  v += __builtin_bit_cast(float, __builtin_amdgcn_update_dpp(0, __builtin_bit_cast(int, v), 0x4E, 0xF, 0xF, false));
  v += __builtin_bit_cast(float, __builtin_amdgcn_update_dpp(0, __builtin_bit_cast(int, v), 0x141, 0xF, 0xF, false));
  return v;
}
__device__ __forceinline__ float silu_f(float y) { return y * __builtin_amdgcn_rcpf(1.0f + __builtin_amdgcn_exp2f(y * -1.4426950408889634f)); }
template <class F, int... I> __device__ __forceinline__ void static_for_impl(F&& f, std::integer_sequence<int, I...>) { (f(std::integral_constant<int, I>{}), ...); }
template <int N, class F> __device__ __forceinline__ void static_for(F&& f) { static_for_impl(f, std::make_integer_sequence<int, N>{}); }

struct P1 {
  v4i rsA, rsB, rsH, rsC, rsT;
  int avoff, asvoff, bvoff, ldb2;
  int pfso, bid;
  int w, wm, lane, grp, mem, rot;
  const char* bt;
  int tstart, ftile0;
  int isffn, htile0;
  unsigned tag, taghi;
  int k0, k0t, halo, side;
  unsigned cpf;
};
__device__ __forceinline__ int hk0(int hw) { return 20 + 14 * hw - ((hw + 1) >> 2); }

template <int I> __device__ __forceinline__ void p1_req(const P1& c, unsigned base, int st) {
  st += c.rot; st = st >= 10 ? st - 10 : st;
  if (I == 0 && c.halo) dma16(c.rsA, base + c.w * 1024, c.asvoff, st * 128);
  if (I < 2) dma16(c.rsA, base + (2 + c.w + 8 * I) * 1024, c.avoff, st * 128 + I * 64 * (DM * 2));
  else dma16(c.rsB, base + A_BYTES + (c.w + 8 * (I - 2)) * 1024, c.bvoff, (st * 64 + (I - 2) * 16) * c.ldb2);
}
template <int E> __device__ __forceinline__ void tail_req(const P1& c, unsigned lds0) {
  dma16_sc1(c.rsT, lds0 + RING_OFF + E * 8192 + c.w * 1024, c.lane * 16, c.k0t * 5120 + E * 1024);
}

__device__ __forceinline__ void ldB5(v4u (&B)[5], v4i rsT, int voff, int so) {
  const int so2 = so == OOB ? OOB : so + 4096;
  B[0] = ld128_sc1<0>(rsT, voff, so); B[1] = ld128_sc1<1024>(rsT, voff, so); B[2] = ld128_sc1<2048>(rsT, voff, so);
  B[3] = ld128_sc1<3072>(rsT, voff, so); B[4] = ld128_sc1<0>(rsT, voff, so2);
}
#define TIE5(B) asm volatile("" : "+v"((B)[0]), "+v"((B)[1]), "+v"((B)[2]), "+v"((B)[3]), "+v"((B)[4]))

template <int I> __device__ __forceinline__ v4u ldB1(v4i rsT, int voff, int so) {
  return ld128_sc1<(I & 3) * 1024>(rsT, voff, I < 4 ? so : so + 4096);
}
template <int NT, int NOWN, int NFOR>
__device__ __forceinline__ void phase1(P1& c, LAS char* smem, unsigned lds0, char* ws, v4u (&B)[5][5]) {
  constexpr int NB = NT + NFOR, NX = NOWN + NFOR;
  const int lane = c.lane, r = lane & 15, g = lane >> 4, q = r >> 2, p = lane & 3;
  int aad[2];
#pragma unroll
  for (int ks = 0; ks < 2; ks++) aad[ks] = r * 128 + (((4 * ks + g) ^ (r >> 1)) << 4);
  const int amain = (1 + c.wm * 4) * 2048;
  const int z = q | ((g & 1) << 2);
  int tile[NB], bad[NB];
#pragma unroll
  for (int n = 0; n < NB; n++) {
    if (n < NT) { int t = n + 2 * c.wm; t = t >= NT ? t - NT : t; tile[n] = c.tstart + t; }
    else tile[n] = c.ftile0 + 2 * (n - NT);
    bad[n] = A_BYTES + (8 * g + q) * 512 + (((tile[n] & 7) ^ z) << 5) + (tile[n] >> 3) * 256 + p * 8;
  }
  f32x4 acc[4][NT], accx[NX];
#pragma unroll
  for (int m = 0; m < 4; m++)
#pragma unroll
    for (int n = 0; n < NT; n++) acc[m][n] = f32x4{0, 0, 0, 0};
#pragma unroll
  for (int n = 0; n < NX; n++) accx[n] = f32x4{0, 0, 0, 0};
  v8bf af[2][4], as[2], bf[2][NB];
  const bool ones = (r == 0) || c.isffn;
  auto ldq = [&](int buf, LAS char* base, int ks, int i) {
    af[buf][i] = *(LAS v8bf*)(base + aad[ks] + amain + i * 2048);
#pragma unroll
    for (int n = 0; n < NB; n++) if ((n & 3) == i) bf[buf][n] = trfrag(base + bad[n] + ks * 16384, 2048);
    if (i == 3) as[buf] = *(LAS v8bf*)(base + aad[ks]);
  };
  auto row = [&](int buf, int m) {
#pragma unroll
    for (int n = 0; n < NT; n++) acc[m][n] = mfma(bf[buf][n], af[buf][m], acc[m][n]);
    if (m == 3) {
      v4u s = __builtin_bit_cast(v4u, as[buf]);
#pragma unroll
      for (int e = 0; e < 4; e++) s[e] = ones ? 0x3F803F80u : s[e];
      v8bf sv = __builtin_bit_cast(v8bf, s);
#pragma unroll
      for (int n = 0; n < NX; n++) accx[n] = mfma(bf[buf][n < NOWN ? n : NT + n - NOWN], sv, accx[n]);
    }
#pragma unroll
    for (int i = 0; i < NT; i++) {
      __builtin_amdgcn_sched_group_barrier(0x8, 1, 0); __builtin_amdgcn_sched_group_barrier(0x100, 2, 0); __builtin_amdgcn_sched_group_barrier(0x2, 1, 0);
    }
    SB;
  };
  auto halfstage = [&](int buf, bool rd, LAS char* rbase, int rks, auto&& rq0, auto&& rq1, auto&& rq2) {
    if (rd) { ldq(buf ^ 1, rbase, rks, 0); ldq(buf ^ 1, rbase, rks, 1); }
    row(buf, 0); rq0(); SB;
    if (rd) ldq(buf ^ 1, rbase, rks, 2);
    row(buf, 1); rq1(); SB;
    if (rd) ldq(buf ^ 1, rbase, rks, 3);
    row(buf, 2); rq2(); SB;
    row(buf, 3);
  };
  auto none = [&]() {};
  auto lst = [&](int i) {};
#pragma unroll
  for (int m = 0; m < 4; m++)
#pragma unroll
    for (int n = 0; n < NT; n++) asm volatile("" : "+v"(acc[m][n]));
#pragma unroll
  for (int n = 0; n < NX; n++) asm volatile("" : "+v"(accx[n]));
#pragma unroll
  for (int n = 0; n < NB; n++) asm volatile("" : "+v"(bad[n]));
  asm volatile("" : "+v"(aad[0]), "+v"(aad[1]));
  if (c.side) ldsbar(); else waitbar<9>();
  asm volatile("" ::"v"(c.cpf));
  lst(0);
  STAMP(1);
  auto special = [&](int st) {
    if (st == 1) {
      const v2u ep = *(LAS v2u*)(smem + EPL_OFF);
      const u64 e = ((u64)(unsigned)__builtin_amdgcn_readfirstlane(ep.y) << 32) | (unsigned)__builtin_amdgcn_readfirstlane(ep.x);
      const u64 t = e * 0x5851F42D4C957F2Dull + 0x14057B7EF767814Full;
      c.tag = (unsigned)t; c.taghi = (unsigned)(t >> 32);
      if (c.w == 0) st32_sc1(c.rsC, lane == 0 ? RDY_OFF + c.bid * 4 : OOB, c.tag);
    } else if (st == 6) {
      if (c.w == 0) dma16_sc1(c.rsC, lds0 + RDYL_OFF, lane * 16, RDY_OFF);
    } else if (st == 7) {
      v4u rdy = *(LAS v4u*)(smem + RDYL_OFF + lane * 16);
      for (unsigned spins = 0;; spins++) {
        if (__all(rdy.x == c.tag && rdy.y == c.tag && rdy.z == c.tag && rdy.w == c.tag) || spins > SPIN_CAP) break;
        __builtin_amdgcn_s_sleep(POLL_SLEEP);
        asm volatile("buffer_load_dwordx4 %0, %1, %2, 0 offen offset:%3 sc1\n\ts_waitcnt vmcnt(0)" : "=&v"(rdy) : "v"(lane * 16), "s"(c.rsC), "i"(RDY_OFF) : "memory");
      }
      if (c.w < 2) dma16_sc1(c.rsC, lds0 + RS_OFF + c.w * 1024, lane * 16, STAT_OFF + c.grp * 2048 + c.w * 1024);
    }
  };
#pragma unroll
  for (int i = 0; i < 4; i++) ldq(0, smem, 0, i);
  int so = 0, po = 2 * ST_BYTES;
#pragma nounroll
  for (int st = 0; st < 7; st++) {
    const int no = so == 2 * ST_BYTES ? 0 : so + ST_BYTES;
    halfstage(0, true, smem + so, 1, [&]() { p1_req<3>(c, lds0 + po, st + 2); }, [&]() { p1_req<4>(c, lds0 + po, st + 2); }, [&]() { p1_req<5>(c, lds0 + po, st + 2); });
    waitbar<6>();
    SB;
    lst(st + 1);
    if ((PFMASK >> st) & 1) special(st);
    SB;
    halfstage(1, true, smem + no, 0, [&]() { p1_req<0>(c, lds0 + so, st + 3); }, [&]() { p1_req<1>(c, lds0 + so, st + 3); }, [&]() { p1_req<2>(c, lds0 + so, st + 3); });
    po = so; so = no;
  }
  halfstage(0, true, smem + ST_BYTES, 1, [&]() { p1_req<3>(c, lds0, 9); }, [&]() { p1_req<4>(c, lds0, 9); }, [&]() { p1_req<5>(c, lds0, 9); });
  waitbar<6>(); SB; lst(8);
  special(7); SB;
  halfstage(1, true, smem + 2 * ST_BYTES, 0, [&]() { tail_req<0>(c, lds0); }, [&]() { tail_req<1>(c, lds0); }, [&]() { tail_req<2>(c, lds0); });
  halfstage(0, true, smem + 2 * ST_BYTES, 1, none, none, none);
  waitbar<3>(); SB; lst(9);
  halfstage(1, true, smem, 0, [&]() { tail_req<3>(c, lds0); }, [&]() { tail_req<4>(c, lds0); }, [&]() { tail_req<5>(c, lds0); });
  halfstage(0, true, smem, 1, [&]() { tail_req<6>(c, lds0); }, [&]() { tail_req<7>(c, lds0); }, [&]() { tail_req<8>(c, lds0); });
  halfstage(1, false, smem, 0, [&]() { tail_req<9>(c, lds0); }, none, none);
  lst(10);
  STAMP(2);
#pragma unroll
  for (int n = 0; n < NX; n++)
    *(LAS f32x4*)(smem + (r == 0 ? CS_OFF + (tile[n < NOWN ? n : NT + n - NOWN] * 16 + 4 * g) * 4 : DUMP_OFF + lane * 16)) = accx[n];
  ldsbar();
  STAMP(8);
  f32x2 rs[4];
#pragma unroll
  for (int m = 0; m < 4; m++) rs[m] = *(LAS f32x2*)(smem + RS_OFF + (16 + c.wm * 64 + m * 16 + r) * 8);
  auto ldB = [&](int s) { SB; ldB5(B[s], c.rsT, lane * 16, (c.k0 + s) * 5120); SB; };
  if (c.isffn) {
    float c1[4];
#pragma unroll
    for (int m = 0; m < 4; m++) c1[m] = __builtin_amdgcn_rcpf(rs[m].y) * -1.4426950408889634f;
#pragma unroll
    for (int n = 0; n < NT; n++) {
      const f32x4 cs = *(LAS f32x4*)(smem + CS_OFF + (tile[n] * 16 + 4 * g) * 4);
#pragma unroll
      for (int mp = 0; mp < 2; mp++) {
        v4u o;
#pragma unroll
        for (int h = 0; h < 2; h++) {
          const int m = 2 * mp + h;
          const f32x4 t = acc[m][n] - rs[m].x * cs;
          const f32x4 zz = t * c1[m];
          f32x4 e;
#pragma unroll
          for (int j = 0; j < 4; j++) e[j] = __builtin_amdgcn_exp2f(zz[j]);
          const f32x4 d = e * rs[m].y + rs[m].y;
          f32x4 rr;
#pragma unroll
          for (int j = 0; j < 4; j++) rr[j] = __builtin_amdgcn_rcpf(d[j]);
          const f32x4 y = t * rr;
          o[2 * h] = pk2(y[0], y[1]); o[2 * h + 1] = pk2(y[2], y[3]);
        }
        st128_sc1(c.rsH, lane * 16, ((c.grp * 4 + c.wm * 2 + mp) * NCT + c.htile0 + tile[n]) * 1024, o);
      }
    }
  } else {
    float ri[4];
#pragma unroll
    for (int m = 0; m < 4; m++) ri[m] = __builtin_amdgcn_rcpf(rs[m].y);
    f32x4 cs[NB];
#pragma unroll
    for (int n = 0; n < NB; n++) cs[n] = *(LAS f32x4*)(smem + CS_OFF + (tile[n] * 16 + 4 * g) * 4);
    const f32x2 rh = *(LAS f32x2*)(smem + RS_OFF + r * 8);
    const float rih = __builtin_amdgcn_rcpf(rh.y);
    SB;
    static_for<4 * NT + NX>([&](auto U) {
      constexpr int u = decltype(U)::value;
      if constexpr (u < 15) B[u / 5][u % 5] = ldB1<u % 5>(c.rsT, lane * 16, (c.k0 + u / 5) * 5120);
      if constexpr (u < 4 * NT) {
        constexpr int n = u / 4, m = u % 4;
        const f32x4 y = (acc[m][n] - rs[m].x * cs[n]) * ri[m];
        v2u o; o.x = pk2(y[0], y[1]); o.y = pk2(y[2], y[3]);
        *(LAS v2u*)(smem + (16 + c.wm * 64 + m * 16 + r) * IMG_LD + tile[n] * 32 + g * 8) = o;
      } else {
        constexpr int x = u - 4 * NT, n = x < NOWN ? x : NT + x - NOWN;
        const f32x4 y = (accx[x] - rh.x * cs[n]) * rih;
        v2u o; o.x = pk2(y[0], y[1]); o.y = pk2(y[2], y[3]);
        *(LAS v2u*)(smem + r * IMG_LD + tile[n] * 32 + g * 8) = o;
      }
      SB;
    });
    ldsbar();
  }
}

__device__ __forceinline__ void wait_flags(const P1& c, int off) {
  for (unsigned spins = 0;; spins++) {
    unsigned f;
    asm volatile("buffer_load_dword %0, %1, %2, 0 offen sc1\n\ts_waitcnt vmcnt(0)" : "=&v"(f) : "v"(off), "s"(c.rsC) : "memory");
    if (__all(off == OOB || f == c.tag) || spins > SPIN_CAP) break;
    __builtin_amdgcn_s_sleep(POLL_SLEEP);
  }
}
__device__ __forceinline__ void mfma_ip(f32x4& acc, const v4u& b, const v4u& a) {
  asm volatile("v_mfma_f32_16x16x32_bf16 %0, %1, %2, %0" : "+v"(acc) : "v"(b), "v"(a));
}
__device__ __forceinline__ void swap_pair(v4u& l0, v4u& l1) {
  unsigned a = l0.z, b = l1.x, c = l0.w, d = l1.y;
  asm volatile("v_swap_b32 %0, %1" : "+v"(a), "+v"(b));
  l0.z = a; l1.x = b;
  asm volatile("v_swap_b32 %0, %1" : "+v"(c), "+v"(d));
  l0.w = c; l1.y = d;
}
template <int I> __device__ __forceinline__ v4u ldA1(v4i rsH, int voff, int so) {
  return ld128_sc1<(I & 1) * 1024>(rsH, voff, I < 2 ? so : so + NCT * 1024);
}
__device__ __forceinline__ void ldA4(v4u (&A)[4], v4i rsH, int voff, int so) {
  const int so2 = so == OOB ? OOB : so + NCT * 1024;
  A[0] = ld128_sc1<0>(rsH, voff, so); A[1] = ld128_sc1<1024>(rsH, voff, so);
  A[2] = ld128_sc1<0>(rsH, voff, so2); A[3] = ld128_sc1<1024>(rsH, voff, so2);
}
#define WAITA(A, N) asm volatile("s_waitcnt vmcnt(%4) lgkmcnt(0)" : "+v"((A)[0]), "+v"((A)[1]), "+v"((A)[2]), "+v"((A)[3]) : "i"(N) : "memory")
constexpr int h_na(int t) { return t == 1 ? 2 : (t + 3 < 14 ? 1 : 0); }
constexpr int h_a0(int t) { int n = 2; for (int i = 0; i < t; i++) n += h_na(i); return n; }
constexpr int hcnt(int t) { return 4 * h_na(t) + ((t >= 6 && t <= 10) ? 5 : 0) + (t == 8 ? 3 : 0); }
constexpr int hwait(int s) {
  int n = 0;
  if (s >= 9) { n = (s - 3 == 8 ? 3 : 0); for (int t = s - 2; t < s; t++) n += hcnt(t); return n; }
  if (s < 2) { n = 4 * (1 - s); for (int t = 0; t < s; t++) n += hcnt(t); return n; }
  int t0 = 0; while (h_a0(t0) + h_na(t0) <= s) t0++;
  n = hcnt(t0) - 4 * (s - h_a0(t0) + 1);
  for (int t = t0 + 1; t < s; t++) n += hcnt(t);
  return n;
}

typedef unsigned s8u __attribute__((ext_vector_type(8)));
__global__ __launch_bounds__(512, 2) void fused_kernel(const u16* x_, const u16* wqkv_, const u16* wo_, const u16* w1_, const u16* w2_, u16* out_, char* ws_) {
  extern __shared__ __attribute__((aligned(16))) char smem_[];
  LAS char* smem = (LAS char*)smem_;
  const unsigned lds0 = (unsigned)(size_t)smem;
  s8u ka; v4u kb; v2u kc;
  asm volatile("s_load_dwordx8 %0, %3, 0x0\n\ts_load_dwordx4 %1, %3, 0x20\n\ts_load_dwordx2 %2, %3, 0x30"
               : "=&s"(ka), "=&s"(kb), "=&s"(kc) : "s"(__builtin_amdgcn_kernarg_segment_ptr()));
  SB;
  const int tid = threadIdx.x, lane = tid & 63, w = __builtin_amdgcn_readfirstlane(tid >> 6);
  const int bid = blockIdx.x, xcd = bid & 7, idx = bid >> 3, half = xcd & 1, j8 = idx & 7;
  const int grp = (xcd >> 1) * 4 + (idx >> 3), mem = half * 8 + j8;
  const int isffn = j8 >> 2, sub = half * 4 + (j8 & 3);
  unsigned cpf;
  {
    u64 pc0; unsigned codelen;
    asm volatile("s_getpc_b64 %0\n.Lcode_begin_mark:\n\ts_mov_b32 %1, .Lcode_end_mark-.Lcode_begin_mark" : "=s"(pc0), "=s"(codelen));
    const v4i rp = mkrs((const void*)pc0, (int)codelen);
    asm volatile("buffer_load_dword %0, %1, %2, %3 offen" : "=v"(cpf) : "v"(lane < 2 ? lane * 128 : OOB), "s"(rp), "s"((idx * 8 + w) * 256));
  }
  P1 c;
  c.ldb2 = (3 * DM - isffn * (3 * DM - FF)) * 2;
  c.w = w; c.lane = lane; c.grp = grp; c.isffn = isffn; c.mem = mem;
  const int ftile0 = sub * 14 - half * (j8 & 3);
  const int ntile = 15 - isffn * (1 + half);
  {
    const int ar = lane >> 3, sw = ((lane & 7) ^ ((4 * w + (lane >> 4)) & 7)) << 4;
    c.avoff = (grp * 128 + 8 * w + ar) * (DM * 2) + sw;
    int hr = grp * 128 - 16 + 8 * w + ar; hr = hr < 0 ? 0 : hr;
    c.asvoff = hr * (DM * 2) + sw;
    const int kk = 2 * w + (lane >> 5), zz = (kk & 3) | (((kk >> 3) & 1) << 2), pc = lane & 31, seg = (pc >> 1) ^ zz;
    const int part = (seg * 13) >> 6;
    const int col = seg * 16 + (isffn ? ftile0 * 16 : sub * 80) + (isffn ? 0 : DM - 80) * part;
    const int bo = kk * c.ldb2 + col * 2 + (pc & 1) * 16;
    c.bvoff = seg < ntile ? bo : OOB;
  }
  c.halo = !isffn && w < 2;
  { const int cl = (j8 + grp + half) % NCLS; c.rot = cl * 10 / NCLS; }
  asm volatile("" : "+v"(c.avoff), "+v"(c.asvoff), "+v"(c.bvoff), "+s"(c.rot));
  asm volatile("s_waitcnt lgkmcnt(0)" : "+s"(ka), "+s"(kb), "+s"(kc));
  auto ptr = [](unsigned lo, unsigned hi) { return (char*)(((u64)hi << 32) | lo); };
  const u16* x = (const u16*)ptr(ka[0], ka[1]); const u16* wqkv = (const u16*)ptr(ka[2], ka[3]); const u16* wo = (const u16*)ptr(ka[4], ka[5]);
  const u16* w1 = (const u16*)ptr(ka[6], ka[7]); const u16* w2 = (const u16*)ptr(kb[0], kb[1]); u16* out = (u16*)ptr(kb[2], kb[3]);
  char* ws = ptr(kc[0], kc[1]);
  c.rsA = mkrs(x, SEQ * DM * 2);
  c.rsB = mkrs(isffn ? w1 : wqkv, DM * c.ldb2);
  static_for<6>([&](auto I) { p1_req<decltype(I)::value>(c, lds0, 0); });
  STAMP(0);
  c.rsH = mkrs(ws, H_BYTES);
  c.rsC = mkrs(ws + CTRL_OFF, 1 << 20);
  const int cg = (xcd >> 1) * 4 + half * 2 + (idx >> 4), rh = (idx >> 3) & 1, c3 = idx & 7;
  c.bt = ws + BT_OFF; c.bid = bid;
  c.rsT = mkrs(ws + BT_OFF + (size_t)c3 * BT_SLICE, BT_SLICE);
  c.wm = w >> 2;
  c.k0 = w < 4 ? 5 * w : hk0(w & 3);
  c.k0t = hk0(w & 3) + 5 + 2 * (w >> 2);
  c.tag = 0; c.taghi = 0;
  const int wn = w < 4 ? w : 7 - w;
  c.htile0 = 40 + ftile0;
  const int tsc = w == 3 ? CS_OFF : 2 * ST_BYTES + A_BYTES + 8192 + (w - 4) * 5120;
  v4u xs[10];
  const int sslot = w * 8 + (lane >> 3);
  const int kap = (7 - w) * 16 + grp;
  const bool dostat = isffn && w < 3, dotile = isffn && w >= 3 && kap < 74;
  if (dostat) {
    int rho = sub * 18 + (sslot < 18 ? sslot : 17);
    int row = grp * 128 - 16 + rho; row = row < 0 ? 0 : row;
    const int vo = row * (DM * 2) + (lane & 7) * 16;
    static_for<10>([&](auto I) { constexpr int i = decltype(I)::value; xs[i] = ld128<128 * i>(c.rsA, vo, 0); });
  } else if (dotile) {
    const v4i rw = mkrs(kap < 20 ? wo : w2, kap < 20 ? DM * DM * 2 : DM * FF * 2);
    int kq = kap;
    if (kap >= 20) asm volatile("s_sub_u32 %0, %0, 20" : "+s"(kq)::"scc");
    const int so = kq * 32 * (DM * 2);
#pragma unroll
    for (int i = 0; i < 5; i++) {
      const int pc = 64 * i + lane, rr = pc / 10;
      dma16(rw, lds0 + tsc + i * 1024, rr * (DM * 2) + sub * 160 + (pc - 10 * rr) * 16, so);
    }
  }
  static_for<6>([&](auto I) { p1_req<decltype(I)::value>(c, lds0 + ST_BYTES, 1); });
  static_for<3>([&](auto I) { p1_req<decltype(I)::value>(c, lds0 + 2 * ST_BYTES, 2); });
  c.side = dostat || dotile; c.cpf = cpf;
  if (c.side) asm volatile("s_waitcnt vmcnt(9)" ::: "memory");
  if (w == 7) dma16_sc1(c.rsC, lds0 + EPL_OFF, lane == 0 ? EPOCH_OFF : OOB, 0);
  if (dostat) {
    asm volatile("" : "+v"(xs[0]), "+v"(xs[1]), "+v"(xs[2]), "+v"(xs[3]), "+v"(xs[4]), "+v"(xs[5]), "+v"(xs[6]), "+v"(xs[7]), "+v"(xs[8]), "+v"(xs[9])::"memory");
    float s0 = 0, s1 = 0, q0 = 0, q1 = 0;
#pragma unroll
    for (int i = 0; i < 40; i += 2) {
      const unsigned a = xs[i >> 2][i & 3], b = xs[i >> 2][(i & 3) + 1];
      s0 = dot2(a, 0x3F803F80u, s0); s1 = dot2(b, 0x3F803F80u, s1); q0 = dot2(a, a, q0); q1 = dot2(b, b, q1);
    }
    const float mean = row8_sum(s0 + s1) * (1.0f / DM);
    const float var = fmaxf(row8_sum(q0 + q1) * (1.0f / DM) - mean * mean, 0.f);
    const float sd = __builtin_sqrtf(rbf(rbf(var) + 1e-5f));
    v2u gr; gr.x = __builtin_bit_cast(unsigned, rbf(mean)); gr.y = __builtin_bit_cast(unsigned, rbf(sd));
    st64_sc1(c.rsC, ((lane & 7) == 0 && sslot < 18) ? STAT_OFF + grp * 2048 + (sub * 18 + sslot) * 8 : OOB, gr);
  } else if (dotile) {
    LAS char* tb = smem + tsc + ((lane >> 4) * 4 + ((lane & 15) >> 2)) * 160 + (lane & 3) * 8;
#pragma unroll
    for (int n = 0; n < 5; n++)
      st128_sc1(mkrs(ws + BT_OFF, 8 * BT_SLICE), lane * 16, sub * BT_SLICE + (kap * 5 + n) * 1024, __builtin_bit_cast(v4u, trfrag(tb + n * 32, 16 * 160)));
  }
  v4u B[5][5];
  {
    const int n4 = ntile - 12;
    c.tstart = wn <= n4 ? 4 * wn : 4 * n4 + 3 * (wn - n4);
    if (!isffn) c.ftile0 = 1 + 6 * c.wm;
    else { const int fi = (wn - n4) * 2 + c.wm; c.ftile0 = 1 + 2 * (n4 == 2 ? fi : (fi & 1)); }
    if (wn < n4) phase1<4, 1, 0>(c, smem, lds0, ws, B);
    else if (!isffn) phase1<3, 2, 3>(c, smem, lds0, ws, B);
    else phase1<3, 2, 1>(c, smem, lds0, ws, B);
  }
  STAMP(9);
  const int r = lane & 15, g = lane >> 4;
  const int k0 = c.k0;
  auto ldB = [&](int s) { SB; ldB5(B[s], c.rsT, lane * 16, (k0 + s) * 5120); SB; };
  if (!isffn) {
    const int row = (w & 1) * 64 + lane;
    const int pos = grp * 128 + row;
    LAS char* qp = smem + (row + 16) * IMG_LD + (w >> 1) * 40;
    unsigned qv[10];
#pragma unroll
    for (int i = 0; i < 5; i++) { const v2u t = *(LAS v2u*)(qp + i * 8); qv[2 * i] = t.x; qv[2 * i + 1] = t.y; }
    float lg[8];
    static_for<8>([&](auto K) {
      constexpr int k = decltype(K)::value;
      if constexpr (k < 5) B[3][k] = ldB1<k>(c.rsT, lane * 16, (k0 + 3) * 5120);
      LAS char* kp = qp + 160 - 2 * k * IMG_LD;
      float a0 = 0, a1 = 0;
#pragma unroll
      for (int i = 0; i < 5; i++) { const v2u t = *(LAS v2u*)(kp + i * 8); a0 = dot2(qv[2 * i], t.x, a0); a1 = dot2(qv[2 * i + 1], t.y, a1); }
      lg[k] = pos - 2 * k >= 0 ? a0 + a1 : -4e9f;
    });
    float mx = lg[0];
#pragma unroll
    for (int k = 1; k < 8; k++) mx = fmaxf(mx, lg[k]);
    float sum = 0;
    constexpr float C = 0.22360679774997896f * 1.4426950408889634f;
#pragma unroll
    for (int k = 0; k < 8; k++) { lg[k] = __builtin_amdgcn_exp2f((lg[k] - mx) * C); sum += lg[k]; }
    const float inv = __builtin_amdgcn_rcpf(sum);
    float o[20];
#pragma unroll
    for (int d = 0; d < 20; d++) o[d] = 0;
    static_for<8>([&](auto K) {
      constexpr int k = decltype(K)::value;
      if constexpr (k < 5) B[4][k] = ldB1<k>(c.rsT, lane * 16, (k0 + 4) * 5120);
      const unsigned plo = pk2(lg[k] * inv, 0.f), phi = plo << 16;
      LAS char* vp = qp + 320 - 2 * k * IMG_LD;
#pragma unroll
      for (int i = 0; i < 5; i++) {
        const v2u t = *(LAS v2u*)(vp + i * 8);
        o[4 * i] = dot2(t.x, plo, o[4 * i]); o[4 * i + 1] = dot2(t.x, phi, o[4 * i + 1]);
        o[4 * i + 2] = dot2(t.y, plo, o[4 * i + 2]); o[4 * i + 3] = dot2(t.y, phi, o[4 * i + 3]);
      }
    });
#pragma unroll
    for (int i = 0; i < 5; i++) { v2u t; t.x = pk2(o[4 * i], o[4 * i + 1]); t.y = pk2(o[4 * i + 2], o[4 * i + 3]); *(LAS v2u*)(qp + i * 8) = t; }
    STAMP(11);
    ldsbar();
#pragma unroll
    for (int i = 0; i < 3; i++) {
      const int b0 = w + 8 * i, b = b0 < 20 ? b0 : 19, rp = b / 5, t = b - 5 * rp;
      LAS char* sp = smem + (16 + rp * 32 + r) * IMG_LD + t * 32 + g * 8;
      const v2u lo = *(LAS v2u*)sp, hi = *(LAS v2u*)(sp + 16 * IMG_LD);
      v4u v; v.x = lo.x; v.y = lo.y; v.z = hi.x; v.w = hi.y;
      st128_sc1(c.rsH, lane * 16, b0 < 20 ? ((grp * 4 + rp) * NCT + sub * 5 + t) * 1024 : OOB, v);
    }
  }
  waitbar<0>();
  st32_sc1(c.rsC, tid == 0 ? FLAG_OFF + grp * 256 + mem * 4 : OOB, c.tag);
  STAMP(4);
  if (isffn) {
    if (w < 4) __builtin_amdgcn_s_sleep(40);
    ldB(0); ldB(1); ldB(2); ldB(3); ldB(4);
  }
  {
    int m;
    if (w < 4) m = lane < 2 ? (w >> 1) * 8 + (w & 1) * 2 + lane : -1;
    else {
      const int t0 = 2 * (k0 - 20), t1 = t0 + 27 - (w & 2);
      const int f0 = t0 < 56 ? t0 / 14 : 4 + (t0 - 56) / 13, f1 = t1 < 56 ? t1 / 14 : 4 + (t1 - 56) / 13, f = f0 + lane;
      m = f <= f1 ? (f >> 2) * 8 + 4 + (f & 3) : -1;
    }
    wait_flags(c, m >= 0 ? FLAG_OFF + cg * 256 + m * 4 : OOB);
  }
  STAMP(5);
  const int hso = ((cg * 4 + rh * 2) * NCT + 2 * k0) * 1024;
  v2u xr[3];
  auto ldx = [&]() {
#pragma unroll
    for (int i = 0; i < 3; i++) {
      const int tl = w + 8 * i, tt = tl < 20 ? tl : 19, m = tt / 5, n = tt - 5 * m;
      xr[i] = ld64(c.rsA, (cg * 128 + rh * 64 + m * 16 + r) * (DM * 2) + (c3 * 80 + n * 16 + g * 4) * 2, 0);
    }
  };
  auto put = [&](const f32x4 (&acc)[4][5], int p0, int p1, int stride) {
    STAMP(6);
    asm volatile("s_nop 7\n\ts_nop 3\n\ts_waitcnt vmcnt(0) lgkmcnt(0)" : "+v"(xr[0]), "+v"(xr[1]), "+v"(xr[2])::"memory");
#pragma unroll
    for (int m = 0; m < 4; m++)
#pragma unroll
      for (int n = 0; n < 5; n++) *(LAS f32x4*)(smem + (m < 2 ? p0 : p1) + ((m & 1) * 5 + n) * stride + lane * 16) = acc[m][n];
  };
  const int vo = lane * 16;
  if (w < 4) {
    v4u A[5][4];
    auto ldA = [&](int s) { ldA4(A[s], c.rsH, vo, hso + s * 2048); };
    ldA(0); ldA(1); ldA(2);
    f32x4 acc[4][5];
#pragma unroll
    for (int m = 0; m < 4; m++)
#pragma unroll
      for (int n = 0; n < 5; n++) acc[m][n] = f32x4{0, 0, 0, 0};
#pragma unroll
    for (int m = 0; m < 4; m++) TIE5(acc[m]);
#pragma unroll
    for (int s = 0; s < 5; s++) TIE5(B[s]);
    WAITA(A[0], 8); swap_pair(A[0][0], A[0][1]); swap_pair(A[0][2], A[0][3]);
    asm volatile("s_nop 1");
    static_for<5>([&](auto S) {
      constexpr int s = decltype(S)::value;
      static_for<20>([&](auto I) {
        constexpr int i = decltype(I)::value;
        mfma_ip(acc[i / 5][i % 5], B[s][i % 5], A[s][i / 5]);
        if constexpr ((s == 1 || s == 2) && i < 8 && (i & 1)) A[s + 2][i >> 1] = ldA1<(i >> 1)>(c.rsH, vo, hso + (s + 2) * 2048);
        if constexpr (s == 2 && i == 9) ldx();
        if constexpr (s < 4 && i == 13) WAITA(A[s + 1], (s == 0 ? 4 : s == 1 ? 4 : s == 2 ? 7 : 3));
        if constexpr (s < 4 && i == 14) swap_pair(A[s + 1][0], A[s + 1][1]);
        if constexpr (s < 4 && i == 15) swap_pair(A[s + 1][2], A[s + 1][3]);
      });
    });
    put(acc, w * 20480, w < 3 ? w * 20480 + 10240 : CS_OFF, 1024);
#pragma unroll
    for (int s = 0; s < 5; s++)
#pragma unroll
      for (int n = 0; n < 5; n++) asm volatile("" : "=v"(B[s][n]));
  }
  int w_h = w;
  asm volatile("" : "+s"(w_h));
  if (w_h >= 4) {
    const int hw = w & 3;
    v4u A[14][4], Bs[14][5];
    const int last = hw < 2 ? 0 : OOB;
    ldA4(A[0], c.rsH, vo, hso); ldA4(A[1], c.rsH, vo, hso + 2048);
    f32x4 acc[4][5];
#pragma unroll
    for (int m = 0; m < 4; m++)
#pragma unroll
      for (int n = 0; n < 5; n++) acc[m][n] = f32x4{0, 0, 0, 0};
#pragma unroll
    for (int m = 0; m < 4; m++) TIE5(acc[m]);
    LAS char* eb = smem + RING_OFF + hw * 1024 + lane * 16;
#pragma unroll
    for (int s = 0; s < 5; s++) {
      TIE5(B[s]);
#pragma unroll
      for (int n = 0; n < 5; n++) Bs[s][n] = B[s][n];
    }
    WAITA(A[0], 4); swap_pair(A[0][0], A[0][1]); swap_pair(A[0][2], A[0][3]);
    asm volatile("s_nop 1");
    static_for<14>([&](auto S) {
      constexpr int s = decltype(S)::value;
      static_for<20>([&](auto I) {
        constexpr int i = decltype(I)::value;
        mfma_ip(acc[i / 5][i % 5], Bs[s][i % 5], A[s][i / 5]);
        if constexpr (i < 4 * h_na(s)) {
          constexpr int k = h_a0(s) + (i >> 2);
          A[k][i & 3] = ldA1<(i & 3)>(c.rsH, k == 13 ? (vo | last) : vo, hso + k * 2048);
        }
        if constexpr (s >= 6 && s <= 10 && i >= 4 && i < 9) Bs[s + 3][i - 4] = ldB1<i - 4>(c.rsT, s == 10 ? (vo | last) : vo, (k0 + s + 3) * 5120);
        if constexpr (s == 8 && i == 9) ldx();
        if constexpr (s >= 4 && s < 8 && i >= 9 && i < 14) {
          SB; Bs[s + 1][i - 9] = *(LAS v4u*)(eb + ((s & 1) * 5 + i - 9) * 8192 + ((s - 4) >> 1) * 4096); SB;
        }
        if constexpr (s < 13 && i == 14) {
          asm volatile("s_waitcnt vmcnt(%4)" : "+v"(A[s + 1][0]), "+v"(A[s + 1][1]), "+v"(A[s + 1][2]), "+v"(A[s + 1][3]) : "i"(hwait(s + 1)) : "memory");
          if constexpr (s + 1 >= 9) TIE5(Bs[s + 1]);
        }
        if constexpr (s < 13 && i == 15) swap_pair(A[s + 1][0], A[s + 1][1]);
        if constexpr (s < 13 && i == 16) swap_pair(A[s + 1][2], A[s + 1][3]);
      });
    });
    put(acc, RING_OFF + hw * 1024, RING_OFF + hw * 1024 + 4096, 8192);
  }
  STAMP(12);
  ldsbar();
  STAMP(13);
  if (w == 0 && bid == 0) { v2u t; t.x = c.tag; t.y = c.taghi; st64_sc1(c.rsC, lane == 0 ? EPOCH_OFF : OOB, t); }
#pragma unroll
  for (int i = 0; i < 3; i++) {
    const int tl = w + 8 * i;
    if (tl < 20) {
      const int m = tl / 5, n = tl - 5 * m;
      LAS char* ps = smem + tl * 1024 + lane * 16;
      LAS char* pf = smem + RING_OFF + (tl < 10 ? tl * 8192 : (tl - 10) * 8192 + 4096) + lane * 16;
      const f32x4 s = (*(LAS f32x4*)ps + *(LAS f32x4*)(ps + 20480)) + (*(LAS f32x4*)(ps + 40960) + *(LAS f32x4*)(ps + (tl < 10 ? 61440 : CS_OFF - 10240)));
      const f32x4 f = (*(LAS f32x4*)pf + *(LAS f32x4*)(pf + 1024)) + (*(LAS f32x4*)(pf + 2048) + *(LAS f32x4*)(pf + 3072));
      float y[4];
#pragma unroll
      for (int j = 0; j < 4; j++) {
        const float xv = (j & 1) ? bfhi(xr[i][j >> 1]) : bflo(xr[i][j >> 1]);
        y[j] = xv + s[j] * silu_f(f[j]);
      }
      v2u o; o.x = pk2(y[0], y[1]); o.y = pk2(y[2], y[3]);
      *(GAS v2u*)((GAS char*)out + (size_t)(cg * 128 + rh * 64 + m * 16 + r) * (DM * 2) + (c3 * 80 + n * 16 + g * 4) * 2) = o;
    }
  }
  STAMP(7);
  asm volatile(".Lcode_end_mark:");
}

__global__ __launch_bounds__(256) void pre_kernel(const unsigned* a, const unsigned* b, const unsigned* c, const unsigned* d, const unsigned* e, unsigned* sink, int mode) {
  if (mode == 0) return;
  const int t = blockIdx.x * 256 + threadIdx.x;
  unsigned v = 0;
  if (t < 20480) v ^= a[t * 32];
  if (t < 19200) v ^= b[t * 32];
  if (t < 6400) v ^= c[t * 32];
  if (t < 17280) { v ^= d[t * 32]; v ^= e[t * 32]; }
  if (v == 0x12345678u) sink[0] = v;
}
}

extern "C" void kernel_launch(void* const* d_in, const int* in_sizes, int n_in, void* d_out, int out_size, void* d_ws, size_t ws_size,
                              hipStream_t stream) {
  (void)hipFuncSetAttribute((const void*)fused_kernel, hipFuncAttributeMaxDynamicSharedMemorySize, LDS_BYTES);
  pre_kernel<<<80, 256, 0, stream>>>((const unsigned*)d_in[0], (const unsigned*)d_in[1], (const unsigned*)d_in[2], (const unsigned*)d_in[4], (const unsigned*)d_in[5], (unsigned*)((char*)d_ws + (64u << 20)), 0);
  fused_kernel<<<256, 512, LDS_BYTES, stream>>>((const u16*)d_in[0], (const u16*)d_in[1], (const u16*)d_in[2], (const u16*)d_in[4],
                                                (const u16*)d_in[5], (u16*)d_out, (char*)d_ws);
}
```

```cpp
#include <hip/hip_runtime.h>
#include <stdint.h>
#include <type_traits>
#include <utility>

#define GAS __attribute__((address_space(1)))
#define LAS __attribute__((address_space(3)))
typedef unsigned short u16;
typedef unsigned long long u64;
typedef __bf16 v2bf __attribute__((ext_vector_type(2)));
typedef __bf16 v4bf __attribute__((ext_vector_type(4)));
typedef __bf16 v8bf __attribute__((ext_vector_type(8)));
typedef float f32x2 __attribute__((ext_vector_type(2)));
typedef float f32x4 __attribute__((ext_vector_type(4)));
typedef unsigned v4u __attribute__((ext_vector_type(4)));
typedef unsigned v2u __attribute__((ext_vector_type(2)));
typedef int v4i __attribute__((ext_vector_type(4)));

namespace {
constexpr int SEQ = 2048, DM = 640, FF = 1728;
constexpr int NCT = 148;
constexpr int H_BYTES = 64 * NCT * 1024;
constexpr size_t CTRL_OFF = 16u << 20;
constexpr int EPOCH_OFF = 0, RDY_OFF = 2048, FLAG_OFF = 8192, STAT_OFF = 65536;
constexpr size_t BT_OFF = 12u << 20;
constexpr int BT_SLICE = 74 * 5120;
constexpr int A_BYTES = 144 * 128, B_BYTES = 64 * 512, ST_BYTES = A_BYTES + B_BYTES;
constexpr int CS_OFF = 3 * ST_BYTES, RS_OFF = CS_OFF + 1024;
constexpr int RDYL_OFF = CS_OFF + 6144, EPL_OFF = CS_OFF + 7168;
constexpr int DUMP_OFF = CS_OFF + 4096;
constexpr int IMG_LD = 488;
constexpr int RING_OFF = 71680;
constexpr int LDS_BYTES = 163840;
constexpr int OOB = 0x7FF00000;
constexpr unsigned SPIN_CAP = 1u << 16;
#define SGB 1
#define NCLS 3
#define PFMASK 0x42
#define MAP3 1
#define POLL_SLEEP 8
#define STAMP(i)
#define SB __builtin_amdgcn_sched_barrier(0)
#define LDPOL2 " sc1"

__device__ __forceinline__ v4i mkrs(const void* p, int bytes) {
  u64 a = (u64)p; v4i r; r.x = (int)(unsigned)a; r.y = (int)((unsigned)(a >> 32) & 0xFFFFu); r.z = bytes; r.w = 0x00020000; return r;
}
__device__ __forceinline__ void dma16(v4i rs, unsigned lds, int voff, int soff) {
  asm volatile("s_mov_b32 m0, %0\n\ts_nop 0\n\tbuffer_load_dwordx4 %1, %2, %3 offen lds" ::"s"(lds), "v"(voff), "s"(rs), "s"(soff));
}
__device__ __forceinline__ void dma16_sc1(v4i rs, unsigned lds, int voff, int soff) {
  asm volatile("s_mov_b32 m0, %0\n\ts_nop 3\n\tbuffer_load_dwordx4 %1, %2, %3 offen" LDPOL2 " lds" ::"s"(lds), "v"(voff), "s"(rs), "s"(soff));
}
__device__ __forceinline__ void dma16_50(v4i rs, unsigned lds, int voff, int soff) {
  asm volatile("s_mov_b32 m0, %0\n\ts_mov_b32 exec_hi, 0x3ffff\n\tbuffer_load_dwordx4 %1, %2, %3 offen lds\n\ts_mov_b32 exec_hi, -1" ::"s"(lds), "v"(voff), "s"(rs), "s"(soff));
}
#define LDPOL " sc1"
#define STPOL " sc1"
template <int IMM> __device__ __forceinline__ v4u ld128_sc1(v4i rs, int voff, int soff) {
  v4u r; asm volatile("buffer_load_dwordx4 %0, %1, %2, %3 offen offset:%4" LDPOL : "=v"(r) : "v"(voff), "s"(rs), "s"(soff), "i"(IMM)); return r;
}
template <int IMM> __device__ __forceinline__ v4u ld128(v4i rs, int voff, int soff) {
  v4u r; asm volatile("buffer_load_dwordx4 %0, %1, %2, %3 offen offset:%4" : "=v"(r) : "v"(voff), "s"(rs), "s"(soff), "i"(IMM)); return r;
}
template <int IMM> __device__ __forceinline__ unsigned ld16(v4i rs, int voff, int soff) {
  unsigned r; asm volatile("buffer_load_ushort %0, %1, %2, %3 offen offset:%4" : "=v"(r) : "v"(voff), "s"(rs), "s"(soff), "i"(IMM)); return r;
}
__device__ __forceinline__ v2u ld64(v4i rs, int voff, int soff) {
  v2u r; asm volatile("buffer_load_dwordx2 %0, %1, %2, %3 offen" : "=v"(r) : "v"(voff), "s"(rs), "s"(soff)); return r;
}
__device__ __forceinline__ v2u ld64_sc1(v4i rs, int voff, int soff) {
  v2u r; asm volatile("buffer_load_dwordx2 %0, %1, %2, %3 offen sc1" : "=v"(r) : "v"(voff), "s"(rs), "s"(soff)); return r;
}
__device__ __forceinline__ v2u ld64_sc1_wait(v4i rs, int voff) {
  v2u r; asm volatile("buffer_load_dwordx2 %0, %1, %2, 0 offen sc1\n\ts_waitcnt vmcnt(0)" : "=&v"(r) : "v"(voff), "s"(rs) : "memory"); return r;
}
__device__ __forceinline__ void st128_sc1(v4i rs, int voff, int soff, v4u v) {
  asm volatile("buffer_store_dwordx4 %0, %1, %2, %3 offen" STPOL "\n\ts_nop 1" ::"v"(v), "v"(voff), "s"(rs), "s"(soff) : "memory");
}
__device__ __forceinline__ void st64_sc1(v4i rs, int voff, v2u v) {
  asm volatile("buffer_store_dwordx2 %0, %1, %2, 0 offen sc1" ::"v"(v), "v"(voff), "s"(rs) : "memory");
}
__device__ __forceinline__ void st32_sc1(v4i rs, int voff, unsigned v) {
  asm volatile("buffer_store_dword %0, %1, %2, 0 offen sc1" ::"v"(v), "v"(voff), "s"(rs) : "memory");
}
template <int N> __device__ __forceinline__ void waitbar() { asm volatile("s_waitcnt vmcnt(%0) lgkmcnt(0)\n\ts_barrier" ::"i"(N) : "memory"); }
__device__ __forceinline__ void ldsbar() { asm volatile("s_waitcnt lgkmcnt(0)\n\ts_barrier" ::: "memory"); }
__device__ __forceinline__ void drain() { asm volatile("s_waitcnt vmcnt(0)" ::: "memory"); }
__device__ __forceinline__ float bflo(unsigned u) { return __builtin_bit_cast(float, u << 16); }
__device__ __forceinline__ float bfhi(unsigned u) { return __builtin_bit_cast(float, u & 0xFFFF0000u); }
__device__ __forceinline__ unsigned pk2(float a, float b) {
  v2bf v; v.x = (__bf16)a; v.y = (__bf16)b; return __builtin_bit_cast(unsigned, v);
}
__device__ __forceinline__ float rbf(float a) { return (float)(__bf16)a; }
__device__ __forceinline__ float dot2(unsigned a, unsigned b, float c) {
  return __builtin_amdgcn_fdot2_f32_bf16(__builtin_bit_cast(v2bf, a), __builtin_bit_cast(v2bf, b), c, false);
}
__device__ __forceinline__ f32x4 mfma(v8bf a, v8bf b, f32x4 c) { return __builtin_amdgcn_mfma_f32_16x16x32_bf16(a, b, c, 0, 0, 0); }
__device__ __forceinline__ v8bf trfrag(LAS char* p, int second) {
  v4bf lo = __builtin_amdgcn_ds_read_tr16_b64_v4bf16((LAS v4bf*)p);
  v4bf hi = __builtin_amdgcn_ds_read_tr16_b64_v4bf16((LAS v4bf*)(p + second));
  return __builtin_shufflevector(lo, hi, 0, 1, 2, 3, 4, 5, 6, 7);
}
__device__ __forceinline__ float row8_sum(float v) {
  v += __builtin_bit_cast(float, __builtin_amdgcn_update_dpp(0, __builtin_bit_cast(int, v), 0xB1, 0xF, 0xF, false));
  v += __builtin_bit_cast(float, __builtin_amdgcn_update_dpp(0, __builtin_bit_cast(int, v), 0x4E, 0xF, 0xF, false));
  v += __builtin_bit_cast(float, __builtin_amdgcn_update_dpp(0, __builtin_bit_cast(int, v), 0x141, 0xF, 0xF, false));
  return v;
}
__device__ __forceinline__ float silu_f(float y) { return y * __builtin_amdgcn_rcpf(1.0f + __builtin_amdgcn_exp2f(y * -1.4426950408889634f)); }
template <class F, int... I> __device__ __forceinline__ void static_for_impl(F&& f, std::integer_sequence<int, I...>) { (f(std::integral_constant<int, I>{}), ...); }
template <int N, class F> __device__ __forceinline__ void static_for(F&& f) { static_for_impl(f, std::make_integer_sequence<int, N>{}); }

struct P1 {
  v4i rsA, rsB, rsH, rsC, rsT;
  int avoff, asvoff, bvoff, ldb2;
  int pfso, bid;
  int w, wm, lane, grp, mem, rot;
  const char* bt;
  int tstart, ftile0;
  int isffn, htile0;
  unsigned tag, taghi;
  int k0, k0t, halo, side;
  unsigned cpf;
};
__device__ __forceinline__ int hk0(int hw) { return 20 + 14 * hw - ((hw + 1) >> 2); }

template <int I> __device__ __forceinline__ void p1_req(const P1& c, unsigned base, int st) {
  st += c.rot;
  { int t; asm("s_cmp_ge_i32 %0, 10\n\ts_cselect_b32 %1, 10, 0\n\ts_sub_i32 %0, %0, %1" : "+s"(st), "=&s"(t)::"scc"); }
  if (I == 0 && c.halo) dma16(c.rsA, base + c.w * 1024, c.asvoff, st * 128);
  if (I < 2) dma16(c.rsA, base + (2 + c.w + 8 * I) * 1024, c.avoff, st * 128 + I * 64 * (DM * 2));
  else dma16(c.rsB, base + A_BYTES + (c.w + 8 * (I - 2)) * 1024, c.bvoff, (st * 64 + (I - 2) * 16) * c.ldb2);
}
template <int E> __device__ __forceinline__ void tail_req(const P1& c, unsigned lds0) {
  dma16_sc1(c.rsT, lds0 + RING_OFF + E * 8192 + c.w * 1024, c.lane * 16, c.k0t * 5120 + E * 1024);
}

__device__ __forceinline__ void ldB5(v4u (&B)[5], v4i rsT, int voff, int so) {
  const int so2 = so == OOB ? OOB : so + 4096;
  B[0] = ld128_sc1<0>(rsT, voff, so); B[1] = ld128_sc1<1024>(rsT, voff, so); B[2] = ld128_sc1<2048>(rsT, voff, so);
  B[3] = ld128_sc1<3072>(rsT, voff, so); B[4] = ld128_sc1<0>(rsT, voff, so2);
}
#define TIE5(B) asm volatile("" : "+v"((B)[0]), "+v"((B)[1]), "+v"((B)[2]), "+v"((B)[3]), "+v"((B)[4]))

template <int I> __device__ __forceinline__ v4u ldB1(v4i rsT, int voff, int so) {
  return ld128_sc1<(I & 3) * 1024>(rsT, voff, I < 4 ? so : so + 4096);
}
template <int NT, int NOWN, int NFOR>
__device__ __forceinline__ void phase1(P1& c, LAS char* smem, unsigned lds0, char* ws, v4u (&B)[5][5]) {
  constexpr int NB = NT + NFOR, NX = NOWN + NFOR;
  const int lane = c.lane, r = lane & 15, g = lane >> 4, q = r >> 2, p = lane & 3;
  int aad[2];
#pragma unroll
  for (int ks = 0; ks < 2; ks++) aad[ks] = r * 128 + (((4 * ks + g) ^ (r >> 1)) << 4);
  const int amain = (1 + c.wm * 4) * 2048;
  const int z = q | ((g & 1) << 2);
  int tile[NB], bad[NB];
#pragma unroll
  for (int n = 0; n < NB; n++) {
    if (n < NT) { int t = n + 2 * c.wm; t = t >= NT ? t - NT : t; tile[n] = c.tstart + t; }
    else tile[n] = c.ftile0 + 2 * (n - NT);
    bad[n] = A_BYTES + (8 * g + q) * 512 + (((tile[n] & 7) ^ z) << 5) + (tile[n] >> 3) * 256 + p * 8;
  }
  f32x4 acc[4][NT], accx[NX];
#pragma unroll
  for (int m = 0; m < 4; m++)
#pragma unroll
    for (int n = 0; n < NT; n++) acc[m][n] = f32x4{0, 0, 0, 0};
#pragma unroll
  for (int n = 0; n < NX; n++) accx[n] = f32x4{0, 0, 0, 0};
  v8bf af[2][4], as[2], bf[2][NB];
  const bool ones = (r == 0) || c.isffn;
  auto ldq = [&](int buf, LAS char* base, int ks, int i) {
    af[buf][i] = *(LAS v8bf*)(base + aad[ks] + amain + i * 2048);
#pragma unroll
    for (int n = 0; n < NB; n++) if ((n & 3) == i) bf[buf][n] = trfrag(base + bad[n] + ks * 16384, 2048);
    if (i == 3) as[buf] = *(LAS v8bf*)(base + aad[ks]);
  };
  auto row = [&](int buf, int m) {
#pragma unroll
    for (int n = 0; n < NT; n++) acc[m][n] = mfma(bf[buf][n], af[buf][m], acc[m][n]);
    if (m == 3) {
      v4u s = __builtin_bit_cast(v4u, as[buf]);
#pragma unroll
      for (int e = 0; e < 4; e++) s[e] = ones ? 0x3F803F80u : s[e];
      v8bf sv = __builtin_bit_cast(v8bf, s);
#pragma unroll
      for (int n = 0; n < NX; n++) accx[n] = mfma(bf[buf][n < NOWN ? n : NT + n - NOWN], sv, accx[n]);
    }
#pragma unroll
    for (int i = 0; i < NT; i++) {
      __builtin_amdgcn_sched_group_barrier(0x8, 1, 0); __builtin_amdgcn_sched_group_barrier(0x100, 2, 0); __builtin_amdgcn_sched_group_barrier(0x2, 1, 0);
    }
    SB;
  };
  auto halfstage = [&](int buf, bool rd, LAS char* rbase, int rks, auto&& rq0, auto&& rq1, auto&& rq2) {
    if (rd) { ldq(buf ^ 1, rbase, rks, 0); ldq(buf ^ 1, rbase, rks, 1); }
    row(buf, 0); rq0(); SB;
    if (rd) ldq(buf ^ 1, rbase, rks, 2);
    row(buf, 1); rq1(); SB;
    if (rd) ldq(buf ^ 1, rbase, rks, 3);
    row(buf, 2); rq2(); SB;
    row(buf, 3);
  };
  auto none = [&]() {};
  auto lst = [&](int i) {};
#pragma unroll
  for (int m = 0; m < 4; m++)
#pragma unroll
    for (int n = 0; n < NT; n++) asm volatile("" : "+v"(acc[m][n]));
#pragma unroll
  for (int n = 0; n < NX; n++) asm volatile("" : "+v"(accx[n]));
#pragma unroll
  for (int n = 0; n < NB; n++) asm volatile("" : "+v"(bad[n]));
  asm volatile("" : "+v"(aad[0]), "+v"(aad[1]));
  if (c.side) ldsbar(); else waitbar<9>();
  asm volatile("" ::"v"(c.cpf));
  lst(0);
  STAMP(1);
  auto special = [&](int st) {
    if (st == 1) {
      const v2u ep = *(LAS v2u*)(smem + EPL_OFF);
      const u64 e = ((u64)(unsigned)__builtin_amdgcn_readfirstlane(ep.y) << 32) | (unsigned)__builtin_amdgcn_readfirstlane(ep.x);
      const u64 t = e * 0x5851F42D4C957F2Dull + 0x14057B7EF767814Full;
      c.tag = (unsigned)t; c.taghi = (unsigned)(t >> 32);
      if (c.w == 0) st32_sc1(c.rsC, lane == 0 ? RDY_OFF + c.bid * 4 : OOB, c.tag);
    } else if (st == 6) {
      if (c.w == 0) dma16_sc1(c.rsC, lds0 + RDYL_OFF, lane * 16, RDY_OFF);
    } else if (st == 7) {
      v4u rdy = *(LAS v4u*)(smem + RDYL_OFF + lane * 16);
      for (unsigned spins = 0;; spins++) {
        if (__all(rdy.x == c.tag && rdy.y == c.tag && rdy.z == c.tag && rdy.w == c.tag) || spins > SPIN_CAP) break;
        __builtin_amdgcn_s_sleep(POLL_SLEEP);
        asm volatile("buffer_load_dwordx4 %0, %1, %2, 0 offen offset:%3 sc1\n\ts_waitcnt vmcnt(0)" : "=&v"(rdy) : "v"(lane * 16), "s"(c.rsC), "i"(RDY_OFF) : "memory");
      }
      if (c.w < 2) dma16_sc1(c.rsC, lds0 + RS_OFF + c.w * 1024, lane * 16, STAT_OFF + c.grp * 2048 + c.w * 1024);
    }
  };
#pragma unroll
  for (int i = 0; i < 4; i++) ldq(0, smem, 0, i);
  int so = 0, po = 2 * ST_BYTES;
#pragma nounroll
  for (int st = 0; st < 7; st++) {
    const int no = so == 2 * ST_BYTES ? 0 : so + ST_BYTES;
    halfstage(0, true, smem + so, 1, [&]() { p1_req<3>(c, lds0 + po, st + 2); }, [&]() { p1_req<4>(c, lds0 + po, st + 2); }, [&]() { p1_req<5>(c, lds0 + po, st + 2); });
    waitbar<6>();
    SB;
    lst(st + 1);
    if ((PFMASK >> st) & 1) special(st);
    SB;
    halfstage(1, true, smem + no, 0, [&]() { p1_req<0>(c, lds0 + so, st + 3); }, [&]() { p1_req<1>(c, lds0 + so, st + 3); }, [&]() { p1_req<2>(c, lds0 + so, st + 3); });
    po = so; so = no;
  }
  halfstage(0, true, smem + ST_BYTES, 1, [&]() { p1_req<3>(c, lds0, 9); }, [&]() { p1_req<4>(c, lds0, 9); }, [&]() { p1_req<5>(c, lds0, 9); });
  waitbar<6>(); SB; lst(8);
  special(7); SB;
  halfstage(1, true, smem + 2 * ST_BYTES, 0, [&]() { tail_req<0>(c, lds0); }, [&]() { tail_req<1>(c, lds0); }, [&]() { tail_req<2>(c, lds0); });
  halfstage(0, true, smem + 2 * ST_BYTES, 1, none, none, none);
  waitbar<3>(); SB; lst(9);
  halfstage(1, true, smem, 0, [&]() { tail_req<3>(c, lds0); }, [&]() { tail_req<4>(c, lds0); }, [&]() { tail_req<5>(c, lds0); });
  halfstage(0, true, smem, 1, [&]() { tail_req<6>(c, lds0); }, [&]() { tail_req<7>(c, lds0); }, [&]() { tail_req<8>(c, lds0); });
  halfstage(1, false, smem, 0, [&]() { tail_req<9>(c, lds0); }, none, none);
  lst(10);
  STAMP(2);
#pragma unroll
  for (int n = 0; n < NX; n++)
    *(LAS f32x4*)(smem + (r == 0 ? CS_OFF + (tile[n < NOWN ? n : NT + n - NOWN] * 16 + 4 * g) * 4 : DUMP_OFF + lane * 16)) = accx[n];
  ldsbar();
  STAMP(8);
  f32x2 rs[4];
#pragma unroll
  for (int m = 0; m < 4; m++) rs[m] = *(LAS f32x2*)(smem + RS_OFF + (16 + c.wm * 64 + m * 16 + r) * 8);
  auto ldB = [&](int s) { SB; ldB5(B[s], c.rsT, lane * 16, (c.k0 + s) * 5120); SB; };
  if (c.isffn) {
    float c1[4];
#pragma unroll
    for (int m = 0; m < 4; m++) c1[m] = __builtin_amdgcn_rcpf(rs[m].y) * -1.4426950408889634f;
#pragma unroll
    for (int n = 0; n < NT; n++) {
      const f32x4 cs = *(LAS f32x4*)(smem + CS_OFF + (tile[n] * 16 + 4 * g) * 4);
#pragma unroll
      for (int mp = 0; mp < 2; mp++) {
        v4u o;
#pragma unroll
        for (int h = 0; h < 2; h++) {
          const int m = 2 * mp + h;
          const f32x4 t = acc[m][n] - rs[m].x * cs;
          const f32x4 zz = t * c1[m];
          f32x4 e;
#pragma unroll
          for (int j = 0; j < 4; j++) e[j] = __builtin_amdgcn_exp2f(zz[j]);
          const f32x4 d = e * rs[m].y + rs[m].y;
          f32x4 rr;
#pragma unroll
          for (int j = 0; j < 4; j++) rr[j] = __builtin_amdgcn_rcpf(d[j]);
          const f32x4 y = t * rr;
          o[2 * h] = pk2(y[0], y[1]); o[2 * h + 1] = pk2(y[2], y[3]);
        }
        st128_sc1(c.rsH, lane * 16, ((c.grp * 4 + c.wm * 2 + mp) * NCT + c.htile0 + tile[n]) * 1024, o);
      }
    }
  } else {
    float ri[4];
#pragma unroll
    for (int m = 0; m < 4; m++) ri[m] = __builtin_amdgcn_rcpf(rs[m].y);
    f32x4 cs[NB];
#pragma unroll
    for (int n = 0; n < NB; n++) cs[n] = *(LAS f32x4*)(smem + CS_OFF + (tile[n] * 16 + 4 * g) * 4);
    const f32x2 rh = *(LAS f32x2*)(smem + RS_OFF + r * 8);
    const float rih = __builtin_amdgcn_rcpf(rh.y);
    SB;
    static_for<4 * NT + NX>([&](auto U) {
      constexpr int u = decltype(U)::value;
      if constexpr (u < 15) B[u / 5][u % 5] = ldB1<u % 5>(c.rsT, lane * 16, (c.k0 + u / 5) * 5120);
      if constexpr (u < 4 * NT) {
        constexpr int n = u / 4, m = u % 4;
        const f32x4 y = (acc[m][n] - rs[m].x * cs[n]) * ri[m];
        v2u o; o.x = pk2(y[0], y[1]); o.y = pk2(y[2], y[3]);
        *(LAS v2u*)(smem + (16 + c.wm * 64 + m * 16 + r) * IMG_LD + tile[n] * 32 + g * 8) = o;
      } else {
        constexpr int x = u - 4 * NT, n = x < NOWN ? x : NT + x - NOWN;
        const f32x4 y = (accx[x] - rh.x * cs[n]) * rih;
        v2u o; o.x = pk2(y[0], y[1]); o.y = pk2(y[2], y[3]);
        *(LAS v2u*)(smem + r * IMG_LD + tile[n] * 32 + g * 8) = o;
      }
      SB;
    });
    ldsbar();
  }
}

__device__ __forceinline__ void wait_flags(const P1& c, int off) {
  for (unsigned spins = 0;; spins++) {
    unsigned f;
    asm volatile("buffer_load_dword %0, %1, %2, 0 offen sc1\n\ts_waitcnt vmcnt(0)" : "=&v"(f) : "v"(off), "s"(c.rsC) : "memory");
    if (__all(off == OOB || f == c.tag) || spins > SPIN_CAP) break;
    __builtin_amdgcn_s_sleep(POLL_SLEEP);
  }
}
__device__ __forceinline__ void mfma_ip(f32x4& acc, const v4u& b, const v4u& a) {
  asm volatile("v_mfma_f32_16x16x32_bf16 %0, %1, %2, %0" : "+v"(acc) : "v"(b), "v"(a));
}
__device__ __forceinline__ void swap_pair(v4u& l0, v4u& l1) {
  unsigned a = l0.z, b = l1.x, c = l0.w, d = l1.y;
  asm volatile("v_swap_b32 %0, %1" : "+v"(a), "+v"(b));
  l0.z = a; l1.x = b;
  asm volatile("v_swap_b32 %0, %1" : "+v"(c), "+v"(d));
  l0.w = c; l1.y = d;
}
template <int I> __device__ __forceinline__ v4u ldA1(v4i rsH, int voff, int so) {
  return ld128_sc1<(I & 1) * 1024>(rsH, voff, I < 2 ? so : so + NCT * 1024);
}
__device__ __forceinline__ void ldA4(v4u (&A)[4], v4i rsH, int voff, int so) {
  const int so2 = so == OOB ? OOB : so + NCT * 1024;
  A[0] = ld128_sc1<0>(rsH, voff, so); A[1] = ld128_sc1<1024>(rsH, voff, so);
  A[2] = ld128_sc1<0>(rsH, voff, so2); A[3] = ld128_sc1<1024>(rsH, voff, so2);
}
#define WAITA(A, N) asm volatile("s_waitcnt vmcnt(%4) lgkmcnt(0)" : "+v"((A)[0]), "+v"((A)[1]), "+v"((A)[2]), "+v"((A)[3]) : "i"(N) : "memory")
constexpr int h_na(int t) { return t == 1 ? 2 : (t + 3 < 14 ? 1 : 0); }
constexpr int h_a0(int t) { int n = 2; for (int i = 0; i < t; i++) n += h_na(i); return n; }
constexpr int hcnt(int t) { return 4 * h_na(t) + ((t >= 6 && t <= 10) ? 5 : 0) + (t == 8 ? 3 : 0); }
constexpr int hwait(int s) {
  int n = 0;
  if (s >= 9) { n = (s - 3 == 8 ? 3 : 0); for (int t = s - 2; t < s; t++) n += hcnt(t); return n; }
  if (s < 2) { n = 4 * (1 - s); for (int t = 0; t < s; t++) n += hcnt(t); return n; }
  int t0 = 0; while (h_a0(t0) + h_na(t0) <= s) t0++;
  n = hcnt(t0) - 4 * (s - h_a0(t0) + 1);
  for (int t = t0 + 1; t < s; t++) n += hcnt(t);
  return n;
}

typedef unsigned s8u __attribute__((ext_vector_type(8)));
__global__ __launch_bounds__(512, 2) void fused_kernel(const u16* x_, const u16* wqkv_, const u16* wo_, const u16* w1_, const u16* w2_, u16* out_, char* ws_) {
  extern __shared__ __attribute__((aligned(16))) char smem_[];
  LAS char* smem = (LAS char*)smem_;
  const unsigned lds0 = (unsigned)(size_t)smem;
  s8u ka; v4u kb; v2u kc;
  asm volatile("s_load_dwordx8 %0, %3, 0x0\n\ts_load_dwordx4 %1, %3, 0x20\n\ts_load_dwordx2 %2, %3, 0x30"
               : "=&s"(ka), "=&s"(kb), "=&s"(kc) : "s"(__builtin_amdgcn_kernarg_segment_ptr()));
  SB;
  const int tid = threadIdx.x, lane = tid & 63, w = __builtin_amdgcn_readfirstlane(tid >> 6);
  const int bid = blockIdx.x, xcd = bid & 7, idx = bid >> 3, half = xcd & 1, j8 = idx & 7;
  const int grp = (xcd >> 1) * 4 + (idx >> 3), mem = half * 8 + j8;
  const int isffn = j8 >> 2, sub = half * 4 + (j8 & 3);
  unsigned cpf;
  {
    u64 pc0; unsigned codelen;
    asm volatile("s_getpc_b64 %0\n.Lcode_begin_mark:\n\ts_mov_b32 %1, .Lcode_end_mark-.Lcode_begin_mark" : "=s"(pc0), "=s"(codelen));
    const v4i rp = mkrs((const void*)pc0, (int)codelen);
    asm volatile("buffer_load_dword %0, %1, %2, %3 offen" : "=v"(cpf) : "v"(lane < 2 ? lane * 128 : OOB), "s"(rp), "s"((idx * 8 + w) * 256));
  }
  P1 c;
  c.ldb2 = (3 * DM - isffn * (3 * DM - FF)) * 2;
  c.w = w; c.lane = lane; c.grp = grp; c.isffn = isffn; c.mem = mem;
  const int ftile0 = sub * 14 - half * (j8 & 3);
  const int ntile = 15 - isffn * (1 + half);
  {
    const int ar = lane >> 3, sw = ((lane & 7) ^ ((4 * w + (lane >> 4)) & 7)) << 4;
    c.avoff = (grp * 128 + 8 * w + ar) * (DM * 2) + sw;
    int hr = grp * 128 - 16 + 8 * w + ar; hr = hr < 0 ? 0 : hr;
    c.asvoff = hr * (DM * 2) + sw;
    const int kk = 2 * w + (lane >> 5), zz = (kk & 3) | (((kk >> 3) & 1) << 2), pc = lane & 31, seg = (pc >> 1) ^ zz;
    const int part = (seg * 13) >> 6;
    const int col = seg * 16 + (isffn ? ftile0 * 16 : sub * 80) + (isffn ? 0 : DM - 80) * part;
    const int bo = kk * c.ldb2 + col * 2 + (pc & 1) * 16;
    c.bvoff = seg < ntile ? bo : OOB;
  }
  c.halo = !isffn && w < 2;
  { const int cl = (j8 + grp + half) % NCLS; c.rot = cl * 10 / NCLS; }
  asm volatile("" : "+v"(c.avoff), "+v"(c.asvoff), "+v"(c.bvoff), "+s"(c.rot));
  asm volatile("s_waitcnt lgkmcnt(0)" : "+s"(ka), "+s"(kb), "+s"(kc));
  auto ptr = [](unsigned lo, unsigned hi) { return (char*)(((u64)hi << 32) | lo); };
  const u16* x = (const u16*)ptr(ka[0], ka[1]); const u16* wqkv = (const u16*)ptr(ka[2], ka[3]); const u16* wo = (const u16*)ptr(ka[4], ka[5]);
  const u16* w1 = (const u16*)ptr(ka[6], ka[7]); const u16* w2 = (const u16*)ptr(kb[0], kb[1]); u16* out = (u16*)ptr(kb[2], kb[3]);
  char* ws = ptr(kc[0], kc[1]);
  c.rsA = mkrs(x, SEQ * DM * 2);
  c.rsB = mkrs(isffn ? w1 : wqkv, DM * c.ldb2);
  static_for<6>([&](auto I) { p1_req<decltype(I)::value>(c, lds0, 0); });
  STAMP(0);
  c.rsH = mkrs(ws, H_BYTES);
  c.rsC = mkrs(ws + CTRL_OFF, 1 << 20);
  const int cg = (xcd >> 1) * 4 + half * 2 + (idx >> 4), rh = (idx >> 3) & 1, c3 = idx & 7;
  c.bt = ws + BT_OFF; c.bid = bid;
  c.rsT = mkrs(ws + BT_OFF + (size_t)c3 * BT_SLICE, BT_SLICE);
  c.wm = w >> 2;
  c.k0 = w < 4 ? 5 * w : hk0(w & 3);
  c.k0t = hk0(w & 3) + 5 + 2 * (w >> 2);
  c.tag = 0; c.taghi = 0;
  const int wn = w < 4 ? w : 7 - w;
  c.htile0 = 40 + ftile0;
  const int tsc = w == 3 ? CS_OFF : 2 * ST_BYTES + A_BYTES + 8192 + (w - 4) * 5120;
  v4u xs[10];
  const int sslot = w * 8 + (lane >> 3);
  const int kap = (7 - w) * 16 + grp;
  const bool dostat = isffn && w < 3, dotile = isffn && w >= 3 && kap < 74;
  if (dostat) {
    int rho = sub * 18 + (sslot < 18 ? sslot : 17);
    int row = grp * 128 - 16 + rho; row = row < 0 ? 0 : row;
    const int vo = row * (DM * 2) + (lane & 7) * 16;
    static_for<10>([&](auto I) { constexpr int i = decltype(I)::value; xs[i] = ld128<128 * i>(c.rsA, vo, 0); });
  } else if (dotile) {
    const v4i rw = mkrs(kap < 20 ? wo : w2, kap < 20 ? DM * DM * 2 : DM * FF * 2);
    int kq = kap;
    if (kap >= 20) asm volatile("s_sub_u32 %0, %0, 20" : "+s"(kq)::"scc");
    const int so = kq * 32 * (DM * 2);
#pragma unroll
    for (int i = 0; i < 5; i++) {
      const int pc = 64 * i + lane, rr = pc / 10;
      dma16(rw, lds0 + tsc + i * 1024, rr * (DM * 2) + sub * 160 + (pc - 10 * rr) * 16, so);
    }
  }
  static_for<6>([&](auto I) { p1_req<decltype(I)::value>(c, lds0 + ST_BYTES, 1); });
  static_for<3>([&](auto I) { p1_req<decltype(I)::value>(c, lds0 + 2 * ST_BYTES, 2); });
  c.side = dostat || dotile; c.cpf = cpf;
  if (c.side) asm volatile("s_waitcnt vmcnt(9)" ::: "memory");
  if (w == 7) dma16_sc1(c.rsC, lds0 + EPL_OFF, lane == 0 ? EPOCH_OFF : OOB, 0);
  if (dostat) {
    asm volatile("" : "+v"(xs[0]), "+v"(xs[1]), "+v"(xs[2]), "+v"(xs[3]), "+v"(xs[4]), "+v"(xs[5]), "+v"(xs[6]), "+v"(xs[7]), "+v"(xs[8]), "+v"(xs[9])::"memory");
    float s0 = 0, s1 = 0, q0 = 0, q1 = 0;
#pragma unroll
    for (int i = 0; i < 40; i += 2) {
      const unsigned a = xs[i >> 2][i & 3], b = xs[i >> 2][(i & 3) + 1];
      s0 = dot2(a, 0x3F803F80u, s0); s1 = dot2(b, 0x3F803F80u, s1); q0 = dot2(a, a, q0); q1 = dot2(b, b, q1);
    }
    const float mean = row8_sum(s0 + s1) * (1.0f / DM);
    const float var = fmaxf(row8_sum(q0 + q1) * (1.0f / DM) - mean * mean, 0.f);
    const float sd = __builtin_sqrtf(rbf(rbf(var) + 1e-5f));
    v2u gr; gr.x = __builtin_bit_cast(unsigned, rbf(mean)); gr.y = __builtin_bit_cast(unsigned, rbf(sd));
    st64_sc1(c.rsC, ((lane & 7) == 0 && sslot < 18) ? STAT_OFF + grp * 2048 + (sub * 18 + sslot) * 8 : OOB, gr);
  } else if (dotile) {
    LAS char* tb = smem + tsc + ((lane >> 4) * 4 + ((lane & 15) >> 2)) * 160 + (lane & 3) * 8;
#pragma unroll
    for (int n = 0; n < 5; n++)
      st128_sc1(mkrs(ws + BT_OFF, 8 * BT_SLICE), lane * 16, sub * BT_SLICE + (kap * 5 + n) * 1024, __builtin_bit_cast(v4u, trfrag(tb + n * 32, 16 * 160)));
  }
  v4u B[5][5];
  {
    const int n4 = ntile - 12;
    c.tstart = wn <= n4 ? 4 * wn : 4 * n4 + 3 * (wn - n4);
    if (!isffn) c.ftile0 = 1 + 6 * c.wm;
    else { const int fi = (wn - n4) * 2 + c.wm; c.ftile0 = 1 + 2 * (n4 == 2 ? fi : (fi & 1)); }
    if (wn < n4) phase1<4, 1, 0>(c, smem, lds0, ws, B);
    else if (!isffn) phase1<3, 2, 3>(c, smem, lds0, ws, B);
    else phase1<3, 2, 1>(c, smem, lds0, ws, B);
  }
  STAMP(9);
  const int r = lane & 15, g = lane >> 4;
  const int k0 = c.k0;
  auto ldB = [&](int s) { SB; ldB5(B[s], c.rsT, lane * 16, (k0 + s) * 5120); SB; };
  if (!isffn) {
    const int row = (w & 1) * 64 + lane;
    const int pos = grp * 128 + row;
    LAS char* qp = smem + (row + 16) * IMG_LD + (w >> 1) * 40;
    unsigned qv[10];
#pragma unroll
    for (int i = 0; i < 5; i++) { const v2u t = *(LAS v2u*)(qp + i * 8); qv[2 * i] = t.x; qv[2 * i + 1] = t.y; }
    float lg[8];
    static_for<8>([&](auto K) {
      constexpr int k = decltype(K)::value;
      if constexpr (k < 5) B[3][k] = ldB1<k>(c.rsT, lane * 16, (k0 + 3) * 5120);
      LAS char* kp = qp + 160 - 2 * k * IMG_LD;
      float a0 = 0, a1 = 0;
#pragma unroll
      for (int i = 0; i < 5; i++) { const v2u t = *(LAS v2u*)(kp + i * 8); a0 = dot2(qv[2 * i], t.x, a0); a1 = dot2(qv[2 * i + 1], t.y, a1); }
      lg[k] = pos - 2 * k >= 0 ? a0 + a1 : -4e9f;
    });
    float mx = lg[0];
#pragma unroll
    for (int k = 1; k < 8; k++) mx = fmaxf(mx, lg[k]);
    float sum = 0;
    constexpr float C = 0.22360679774997896f * 1.4426950408889634f;
#pragma unroll
    for (int k = 0; k < 8; k++) { lg[k] = __builtin_amdgcn_exp2f((lg[k] - mx) * C); sum += lg[k]; }
    const float inv = __builtin_amdgcn_rcpf(sum);
    float o[20];
#pragma unroll
    for (int d = 0; d < 20; d++) o[d] = 0;
    static_for<8>([&](auto K) {
      constexpr int k = decltype(K)::value;
      if constexpr (k < 5) B[4][k] = ldB1<k>(c.rsT, lane * 16, (k0 + 4) * 5120);
      const unsigned plo = pk2(lg[k] * inv, 0.f), phi = plo << 16;
      LAS char* vp = qp + 320 - 2 * k * IMG_LD;
#pragma unroll
      for (int i = 0; i < 5; i++) {
        const v2u t = *(LAS v2u*)(vp + i * 8);
        o[4 * i] = dot2(t.x, plo, o[4 * i]); o[4 * i + 1] = dot2(t.x, phi, o[4 * i + 1]);
        o[4 * i + 2] = dot2(t.y, plo, o[4 * i + 2]); o[4 * i + 3] = dot2(t.y, phi, o[4 * i + 3]);
      }
    });
#pragma unroll
    for (int i = 0; i < 5; i++) { v2u t; t.x = pk2(o[4 * i], o[4 * i + 1]); t.y = pk2(o[4 * i + 2], o[4 * i + 3]); *(LAS v2u*)(qp + i * 8) = t; }
    STAMP(11);
    ldsbar();
#pragma unroll
    for (int i = 0; i < 3; i++) {
      const int b0 = w + 8 * i, b = b0 < 20 ? b0 : 19, rp = b / 5, t = b - 5 * rp;
      LAS char* sp = smem + (16 + rp * 32 + r) * IMG_LD + t * 32 + g * 8;
      const v2u lo = *(LAS v2u*)sp, hi = *(LAS v2u*)(sp + 16 * IMG_LD);
      v4u v; v.x = lo.x; v.y = lo.y; v.z = hi.x; v.w = hi.y;
      st128_sc1(c.rsH, lane * 16, b0 < 20 ? ((grp * 4 + rp) * NCT + sub * 5 + t) * 1024 : OOB, v);
    }
  }
  waitbar<0>();
  st32_sc1(c.rsC, tid == 0 ? FLAG_OFF + grp * 256 + mem * 4 : OOB, c.tag);
  STAMP(4);
  if (isffn) {
    if (w < 4) __builtin_amdgcn_s_sleep(40);
    ldB(0); ldB(1); ldB(2); ldB(3); ldB(4);
  }
  {
    int m;
    if (w < 4) m = lane < 2 ? (w >> 1) * 8 + (w & 1) * 2 + lane : -1;
    else {
      const int t0 = 2 * (k0 - 20), t1 = t0 + 27 - (w & 2);
      const int f0 = t0 < 56 ? t0 / 14 : 4 + (t0 - 56) / 13, f1 = t1 < 56 ? t1 / 14 : 4 + (t1 - 56) / 13, f = f0 + lane;
      m = f <= f1 ? (f >> 2) * 8 + 4 + (f & 3) : -1;
    }
    wait_flags(c, m >= 0 ? FLAG_OFF + cg * 256 + m * 4 : OOB);
  }
  STAMP(5);
  const int hso = ((cg * 4 + rh * 2) * NCT + 2 * k0) * 1024;
  v2u xr[3];
  auto ldx = [&]() {
#pragma unroll
    for (int i = 0; i < 3; i++) {
      const int tl = w + 8 * i, tt = tl < 20 ? tl : 19, m = tt / 5, n = tt - 5 * m;
      xr[i] = ld64(c.rsA, (cg * 128 + rh * 64 + m * 16 + r) * (DM * 2) + (c3 * 80 + n * 16 + g * 4) * 2, 0);
    }
  };
  auto put = [&](const f32x4 (&acc)[4][5], int p0, int p1, int stride) {
    STAMP(6);
    asm volatile("s_nop 7\n\ts_nop 3\n\ts_waitcnt vmcnt(0) lgkmcnt(0)" : "+v"(xr[0]), "+v"(xr[1]), "+v"(xr[2])::"memory");
#pragma unroll
    for (int m = 0; m < 4; m++)
#pragma unroll
      for (int n = 0; n < 5; n++) *(LAS f32x4*)(smem + (m < 2 ? p0 : p1) + ((m & 1) * 5 + n) * stride + lane * 16) = acc[m][n];
  };
  const int vo = lane * 16;
  if (w < 4) {
    v4u A[5][4];
    auto ldA = [&](int s) { ldA4(A[s], c.rsH, vo, hso + s * 2048); };
    ldA(0); ldA(1); ldA(2);
    f32x4 acc[4][5];
#pragma unroll
    for (int m = 0; m < 4; m++)
#pragma unroll
      for (int n = 0; n < 5; n++) acc[m][n] = f32x4{0, 0, 0, 0};
#pragma unroll
    for (int m = 0; m < 4; m++) TIE5(acc[m]);
#pragma unroll
    for (int s = 0; s < 5; s++) TIE5(B[s]);
    WAITA(A[0], 8); swap_pair(A[0][0], A[0][1]); swap_pair(A[0][2], A[0][3]);
    asm volatile("s_nop 1");
    static_for<5>([&](auto S) {
      constexpr int s = decltype(S)::value;
      static_for<20>([&](auto I) {
        constexpr int i = decltype(I)::value;
        mfma_ip(acc[i / 5][i % 5], B[s][i % 5], A[s][i / 5]);
        if constexpr ((s == 1 || s == 2) && i < 8 && (i & 1)) A[s + 2][i >> 1] = ldA1<(i >> 1)>(c.rsH, vo, hso + (s + 2) * 2048);
        if constexpr (s == 2 && i == 9) ldx();
        if constexpr (s < 4 && i == 13) WAITA(A[s + 1], (s == 0 ? 4 : s == 1 ? 4 : s == 2 ? 7 : 3));
        if constexpr (s < 4 && i == 14) swap_pair(A[s + 1][0], A[s + 1][1]);
        if constexpr (s < 4 && i == 15) swap_pair(A[s + 1][2], A[s + 1][3]);
      });
    });
    put(acc, w * 20480, w < 3 ? w * 20480 + 10240 : CS_OFF, 1024);
#pragma unroll
    for (int s = 0; s < 5; s++)
#pragma unroll
      for (int n = 0; n < 5; n++) asm volatile("" : "=v"(B[s][n]));
  }
  int w_h = w;
  asm volatile("" : "+s"(w_h));
  if (w_h >= 4) {
    const int hw = w & 3;
    v4u A[14][4], Bs[14][5];
    const int last = hw < 2 ? 0 : OOB;
    ldA4(A[0], c.rsH, vo, hso); ldA4(A[1], c.rsH, vo, hso + 2048);
    f32x4 acc[4][5];
#pragma unroll
    for (int m = 0; m < 4; m++)
#pragma unroll
      for (int n = 0; n < 5; n++) acc[m][n] = f32x4{0, 0, 0, 0};
#pragma unroll
    for (int m = 0; m < 4; m++) TIE5(acc[m]);
    LAS char* eb = smem + RING_OFF + hw * 1024 + lane * 16;
#pragma unroll
    for (int s = 0; s < 5; s++) {
      TIE5(B[s]);
#pragma unroll
      for (int n = 0; n < 5; n++) Bs[s][n] = B[s][n];
    }
    WAITA(A[0], 4); swap_pair(A[0][0], A[0][1]); swap_pair(A[0][2], A[0][3]);
    asm volatile("s_nop 1");
    static_for<14>([&](auto S) {
      constexpr int s = decltype(S)::value;
      static_for<20>([&](auto I) {
        constexpr int i = decltype(I)::value;
        mfma_ip(acc[i / 5][i % 5], Bs[s][i % 5], A[s][i / 5]);
        if constexpr (i < 4 * h_na(s)) {
          constexpr int k = h_a0(s) + (i >> 2);
          A[k][i & 3] = ldA1<(i & 3)>(c.rsH, k == 13 ? (vo | last) : vo, hso + k * 2048);
        }
        if constexpr (s >= 6 && s <= 10 && i >= 4 && i < 9) Bs[s + 3][i - 4] = ldB1<i - 4>(c.rsT, s == 10 ? (vo | last) : vo, (k0 + s + 3) * 5120);
        if constexpr (s == 8 && i == 9) ldx();
        if constexpr (s >= 4 && s < 8 && i >= 9 && i < 14) {
          SB; Bs[s + 1][i - 9] = *(LAS v4u*)(eb + ((s & 1) * 5 + i - 9) * 8192 + ((s - 4) >> 1) * 4096); SB;
        }
        if constexpr (s < 13 && i == 14) {
          asm volatile("s_waitcnt vmcnt(%4)" : "+v"(A[s + 1][0]), "+v"(A[s + 1][1]), "+v"(A[s + 1][2]), "+v"(A[s + 1][3]) : "i"(hwait(s + 1)) : "memory");
          if constexpr (s + 1 >= 9) TIE5(Bs[s + 1]);
        }
        if constexpr (s < 13 && i == 15) swap_pair(A[s + 1][0], A[s + 1][1]);
        if constexpr (s < 13 && i == 16) swap_pair(A[s + 1][2], A[s + 1][3]);
      });
    });
    put(acc, RING_OFF + hw * 1024, RING_OFF + hw * 1024 + 4096, 8192);
  }
  STAMP(12);
  ldsbar();
  STAMP(13);
  if (w == 0 && bid == 0) { v2u t; t.x = c.tag; t.y = c.taghi; st64_sc1(c.rsC, lane == 0 ? EPOCH_OFF : OOB, t); }
#pragma unroll
  for (int i = 0; i < 3; i++) {
    const int tl = w + 8 * i;
    if (tl < 20) {
      const int m = tl / 5, n = tl - 5 * m;
      LAS char* ps = smem + tl * 1024 + lane * 16;
      LAS char* pf = smem + RING_OFF + (tl < 10 ? tl * 8192 : (tl - 10) * 8192 + 4096) + lane * 16;
      const f32x4 s = (*(LAS f32x4*)ps + *(LAS f32x4*)(ps + 20480)) + (*(LAS f32x4*)(ps + 40960) + *(LAS f32x4*)(ps + (tl < 10 ? 61440 : CS_OFF - 10240)));
      const f32x4 f = (*(LAS f32x4*)pf + *(LAS f32x4*)(pf + 1024)) + (*(LAS f32x4*)(pf + 2048) + *(LAS f32x4*)(pf + 3072));
      float y[4];
#pragma unroll
      for (int j = 0; j < 4; j++) {
        const float xv = (j & 1) ? bfhi(xr[i][j >> 1]) : bflo(xr[i][j >> 1]);
        y[j] = xv + s[j] * silu_f(f[j]);
      }
      v2u o; o.x = pk2(y[0], y[1]); o.y = pk2(y[2], y[3]);
      *(GAS v2u*)((GAS char*)out + (size_t)(cg * 128 + rh * 64 + m * 16 + r) * (DM * 2) + (c3 * 80 + n * 16 + g * 4) * 2) = o;
    }
  }
  STAMP(7);
  asm volatile(".Lcode_end_mark:");
}
}

extern "C" void kernel_launch(void* const* d_in, const int* in_sizes, int n_in, void* d_out, int out_size, void* d_ws, size_t ws_size,
                              hipStream_t stream) {
  (void)hipFuncSetAttribute((const void*)fused_kernel, hipFuncAttributeMaxDynamicSharedMemorySize, LDS_BYTES);
  fused_kernel<<<256, 512, LDS_BYTES, stream>>>((const u16*)d_in[0], (const u16*)d_in[1], (const u16*)d_in[2], (const u16*)d_in[4],
                                                (const u16*)d_in[5], (u16*)d_out, (char*)d_ws);
}
```

```cpp
#include <hip/hip_runtime.h>
#include <stdint.h>
#include <type_traits>
#include <utility>

#define GAS __attribute__((address_space(1)))
#define LAS __attribute__((address_space(3)))
typedef unsigned short u16;
typedef unsigned long long u64;
typedef __bf16 v2bf __attribute__((ext_vector_type(2)));
typedef __bf16 v4bf __attribute__((ext_vector_type(4)));
typedef __bf16 v8bf __attribute__((ext_vector_type(8)));
typedef float f32x2 __attribute__((ext_vector_type(2)));
typedef float f32x4 __attribute__((ext_vector_type(4)));
typedef unsigned v4u __attribute__((ext_vector_type(4)));
typedef unsigned v2u __attribute__((ext_vector_type(2)));
typedef int v4i __attribute__((ext_vector_type(4)));

namespace {
constexpr int SEQ = 2048, DM = 640, FF = 1728;
constexpr int NCT = 148;
constexpr int H_BYTES = 64 * NCT * 1024;
constexpr size_t CTRL_OFF = 16u << 20;
constexpr int EPOCH_OFF = 0, RDY_OFF = 2048, FLAG_OFF = 8192, STAT_OFF = 65536;
constexpr size_t BT_OFF = 12u << 20;
constexpr int BT_SLICE = 74 * 5120;
constexpr int A_BYTES = 144 * 128, B_BYTES = 64 * 512, ST_BYTES = A_BYTES + B_BYTES;
constexpr int CS_OFF = 3 * ST_BYTES, RS_OFF = CS_OFF + 1024;
constexpr int RDYL_OFF = CS_OFF + 6144, EPL_OFF = CS_OFF + 7168;
constexpr int DUMP_OFF = CS_OFF + 4096;
constexpr int IMG_LD = 488;
constexpr int RING_OFF = 71680;
constexpr int LDS_BYTES = 163840;
constexpr int OOB = 0x7FF00000;
constexpr unsigned SPIN_CAP = 1u << 16;
#define SGB 1
#define NCLS 3
#define PFMASK 0x42
#define OSLEEP 30
#define FSLEEP 60
#define FSLEEP2 10
#define MAP3 1
#define POLL_SLEEP 8
#define STAMP(i)
#define SB __builtin_amdgcn_sched_barrier(0)
#define LDPOL2 " sc1"

__device__ __forceinline__ v4i mkrs(const void* p, int bytes) {
  u64 a = (u64)p; v4i r; r.x = (int)(unsigned)a; r.y = (int)((unsigned)(a >> 32) & 0xFFFFu); r.z = bytes; r.w = 0x00020000; return r;
}
__device__ __forceinline__ void dma16(v4i rs, unsigned lds, int voff, int soff) {
  asm volatile("s_mov_b32 m0, %0\n\ts_nop 0\n\tbuffer_load_dwordx4 %1, %2, %3 offen lds" ::"s"(lds), "v"(voff), "s"(rs), "s"(soff));
}
__device__ __forceinline__ void dma16_sc1(v4i rs, unsigned lds, int voff, int soff) {
  asm volatile("s_mov_b32 m0, %0\n\ts_nop 3\n\tbuffer_load_dwordx4 %1, %2, %3 offen" LDPOL2 " lds" ::"s"(lds), "v"(voff), "s"(rs), "s"(soff));
}
__device__ __forceinline__ void dma16_50(v4i rs, unsigned lds, int voff, int soff) {
  asm volatile("s_mov_b32 m0, %0\n\ts_mov_b32 exec_hi, 0x3ffff\n\tbuffer_load_dwordx4 %1, %2, %3 offen lds\n\ts_mov_b32 exec_hi, -1" ::"s"(lds), "v"(voff), "s"(rs), "s"(soff));
}
#define LDPOL " sc1"
#define STPOL " sc1"
template <int IMM> __device__ __forceinline__ v4u ld128_sc1(v4i rs, int voff, int soff) {
  v4u r; asm volatile("buffer_load_dwordx4 %0, %1, %2, %3 offen offset:%4" LDPOL : "=v"(r) : "v"(voff), "s"(rs), "s"(soff), "i"(IMM)); return r;
}
template <int IMM> __device__ __forceinline__ v4u ld128(v4i rs, int voff, int soff) {
  v4u r; asm volatile("buffer_load_dwordx4 %0, %1, %2, %3 offen offset:%4" : "=v"(r) : "v"(voff), "s"(rs), "s"(soff), "i"(IMM)); return r;
}
template <int IMM> __device__ __forceinline__ unsigned ld16(v4i rs, int voff, int soff) {
  unsigned r; asm volatile("buffer_load_ushort %0, %1, %2, %3 offen offset:%4" : "=v"(r) : "v"(voff), "s"(rs), "s"(soff), "i"(IMM)); return r;
}
__device__ __forceinline__ v2u ld64(v4i rs, int voff, int soff) {
  v2u r; asm volatile("buffer_load_dwordx2 %0, %1, %2, %3 offen" : "=v"(r) : "v"(voff), "s"(rs), "s"(soff)); return r;
}
__device__ __forceinline__ unsigned ld32_sc1(v4i rs, int voff) {
  unsigned r; asm volatile("buffer_load_dword %0, %1, %2, 0 offen sc1" : "=v"(r) : "v"(voff), "s"(rs)); return r;
}
__device__ __forceinline__ v2u ld64_sc1(v4i rs, int voff, int soff) {
  v2u r; asm volatile("buffer_load_dwordx2 %0, %1, %2, %3 offen sc1" : "=v"(r) : "v"(voff), "s"(rs), "s"(soff)); return r;
}
__device__ __forceinline__ v2u ld64_sc1_wait(v4i rs, int voff) {
  v2u r; asm volatile("buffer_load_dwordx2 %0, %1, %2, 0 offen sc1\n\ts_waitcnt vmcnt(0)" : "=&v"(r) : "v"(voff), "s"(rs) : "memory"); return r;
}
__device__ __forceinline__ void st128_sc1(v4i rs, int voff, int soff, v4u v) {
  asm volatile("buffer_store_dwordx4 %0, %1, %2, %3 offen" STPOL "\n\ts_nop 1" ::"v"(v), "v"(voff), "s"(rs), "s"(soff) : "memory");
}
__device__ __forceinline__ void st64_sc1(v4i rs, int voff, v2u v) {
  asm volatile("buffer_store_dwordx2 %0, %1, %2, 0 offen sc1" ::"v"(v), "v"(voff), "s"(rs) : "memory");
}
__device__ __forceinline__ void st32_sc1(v4i rs, int voff, unsigned v) {
  asm volatile("buffer_store_dword %0, %1, %2, 0 offen sc1" ::"v"(v), "v"(voff), "s"(rs) : "memory");
}
template <int N> __device__ __forceinline__ void waitbar() { asm volatile("s_waitcnt vmcnt(%0) lgkmcnt(0)\n\ts_barrier" ::"i"(N) : "memory"); }
__device__ __forceinline__ void ldsbar() { asm volatile("s_waitcnt lgkmcnt(0)\n\ts_barrier" ::: "memory"); }
__device__ __forceinline__ void drain() { asm volatile("s_waitcnt vmcnt(0)" ::: "memory"); }
__device__ __forceinline__ float bflo(unsigned u) { return __builtin_bit_cast(float, u << 16); }
__device__ __forceinline__ float bfhi(unsigned u) { return __builtin_bit_cast(float, u & 0xFFFF0000u); }
__device__ __forceinline__ unsigned pk2(float a, float b) {
  v2bf v; v.x = (__bf16)a; v.y = (__bf16)b; return __builtin_bit_cast(unsigned, v);
}
__device__ __forceinline__ float rbf(float a) { return (float)(__bf16)a; }
__device__ __forceinline__ float dot2(unsigned a, unsigned b, float c) {
  return __builtin_amdgcn_fdot2_f32_bf16(__builtin_bit_cast(v2bf, a), __builtin_bit_cast(v2bf, b), c, false);
}
__device__ __forceinline__ f32x4 mfma(v8bf a, v8bf b, f32x4 c) { return __builtin_amdgcn_mfma_f32_16x16x32_bf16(a, b, c, 0, 0, 0); }
__device__ __forceinline__ v8bf trfrag(LAS char* p, int second) {
  v4bf lo = __builtin_amdgcn_ds_read_tr16_b64_v4bf16((LAS v4bf*)p);
  v4bf hi = __builtin_amdgcn_ds_read_tr16_b64_v4bf16((LAS v4bf*)(p + second));
  return __builtin_shufflevector(lo, hi, 0, 1, 2, 3, 4, 5, 6, 7);
}
__device__ __forceinline__ float row8_sum(float v) {
  v += __builtin_bit_cast(float, __builtin_amdgcn_update_dpp(0, __builtin_bit_cast(int, v), 0xB1, 0xF, 0xF, false));
  v += __builtin_bit_cast(float, __builtin_amdgcn_update_dpp(0, __builtin_bit_cast(int, v), 0x4E, 0xF, 0xF, false));
  v += __builtin_bit_cast(float, __builtin_amdgcn_update_dpp(0, __builtin_bit_cast(int, v), 0x141, 0xF, 0xF, false));
  return v;
}
__device__ __forceinline__ float silu_f(float y) { return y * __builtin_amdgcn_rcpf(1.0f + __builtin_amdgcn_exp2f(y * -1.4426950408889634f)); }
template <class F, int... I> __device__ __forceinline__ void static_for_impl(F&& f, std::integer_sequence<int, I...>) { (f(std::integral_constant<int, I>{}), ...); }
template <int N, class F> __device__ __forceinline__ void static_for(F&& f) { static_for_impl(f, std::make_integer_sequence<int, N>{}); }

struct P1 {
  v4i rsA, rsB, rsH, rsC, rsT;
  int avoff, asvoff, bvoff, ldb2;
  int pfso, bid;
  int w, wm, lane, grp, mem, rot;
  const char* bt;
  int tstart, ftile0;
  int isffn, htile0;
  unsigned tag, taghi;
  int k0, k0t, halo, side;
  unsigned cpf;
};
__device__ __forceinline__ int hk0(int hw) { return 20 + 14 * hw - ((hw + 1) >> 2); }

template <int I> __device__ __forceinline__ void p1_req(const P1& c, unsigned base, int st) {
  st += c.rot;
  { int t; asm("s_cmp_ge_i32 %0, 10\n\ts_cselect_b32 %1, 10, 0\n\ts_sub_i32 %0, %0, %1" : "+s"(st), "=&s"(t)::"scc"); }
  if (I == 0 && c.halo) dma16(c.rsA, base + c.w * 1024, c.asvoff, st * 128);
  if (I < 2) dma16(c.rsA, base + (2 + c.w + 8 * I) * 1024, c.avoff, st * 128 + I * 64 * (DM * 2));
  else dma16(c.rsB, base + A_BYTES + (c.w + 8 * (I - 2)) * 1024, c.bvoff, (st * 64 + (I - 2) * 16) * c.ldb2);
}
template <int E> __device__ __forceinline__ void tail_req(const P1& c, unsigned lds0) {
  dma16_sc1(c.rsT, lds0 + RING_OFF + E * 8192 + c.w * 1024, c.lane * 16, c.k0t * 5120 + E * 1024);
}

__device__ __forceinline__ void ldB5(v4u (&B)[5], v4i rsT, int voff, int so) {
  const int so2 = so == OOB ? OOB : so + 4096;
  B[0] = ld128_sc1<0>(rsT, voff, so); B[1] = ld128_sc1<1024>(rsT, voff, so); B[2] = ld128_sc1<2048>(rsT, voff, so);
  B[3] = ld128_sc1<3072>(rsT, voff, so); B[4] = ld128_sc1<0>(rsT, voff, so2);
}
#define TIE5(B) asm volatile("" : "+v"((B)[0]), "+v"((B)[1]), "+v"((B)[2]), "+v"((B)[3]), "+v"((B)[4]))

template <int I> __device__ __forceinline__ v4u ldB1(v4i rsT, int voff, int so) {
  return ld128_sc1<(I & 3) * 1024>(rsT, voff, I < 4 ? so : so + 4096);
}
template <int NT, int NOWN, int NFOR>
__device__ __forceinline__ void phase1(P1& c, LAS char* smem, unsigned lds0, char* ws, v4u (&B)[5][5]) {
  constexpr int NB = NT + NFOR, NX = NOWN + NFOR;
  const int lane = c.lane, r = lane & 15, g = lane >> 4, q = r >> 2, p = lane & 3;
  int aad[2];
#pragma unroll
  for (int ks = 0; ks < 2; ks++) aad[ks] = r * 128 + (((4 * ks + g) ^ (r >> 1)) << 4);
  const int amain = (1 + c.wm * 4) * 2048;
  const int z = q | ((g & 1) << 2);
  int tile[NB], bad[NB];
#pragma unroll
  for (int n = 0; n < NB; n++) {
    if (n < NT) { int t = n + 2 * c.wm; t = t >= NT ? t - NT : t; tile[n] = c.tstart + t; }
    else tile[n] = c.ftile0 + 2 * (n - NT);
    bad[n] = A_BYTES + (8 * g + q) * 512 + (((tile[n] & 7) ^ z) << 5) + (tile[n] >> 3) * 256 + p * 8;
  }
  f32x4 acc[4][NT], accx[NX];
#pragma unroll
  for (int m = 0; m < 4; m++)
#pragma unroll
    for (int n = 0; n < NT; n++) acc[m][n] = f32x4{0, 0, 0, 0};
#pragma unroll
  for (int n = 0; n < NX; n++) accx[n] = f32x4{0, 0, 0, 0};
  v8bf af[2][4], as[2], bf[2][NB];
  const bool ones = (r == 0) || c.isffn;
  auto ldq = [&](int buf, LAS char* base, int ks, int i) {
    af[buf][i] = *(LAS v8bf*)(base + aad[ks] + amain + i * 2048);
#pragma unroll
    for (int n = 0; n < NB; n++) if ((n & 3) == i) bf[buf][n] = trfrag(base + bad[n] + ks * 16384, 2048);
    if (i == 3) as[buf] = *(LAS v8bf*)(base + aad[ks]);
  };
  auto row = [&](int buf, int m) {
#pragma unroll
    for (int n = 0; n < NT; n++) acc[m][n] = mfma(bf[buf][n], af[buf][m], acc[m][n]);
    if (m == 3) {
      v4u s = __builtin_bit_cast(v4u, as[buf]);
#pragma unroll
      for (int e = 0; e < 4; e++) s[e] = ones ? 0x3F803F80u : s[e];
      v8bf sv = __builtin_bit_cast(v8bf, s);
#pragma unroll
      for (int n = 0; n < NX; n++) accx[n] = mfma(bf[buf][n < NOWN ? n : NT + n - NOWN], sv, accx[n]);
    }
#pragma unroll
    for (int i = 0; i < NT; i++) {
      __builtin_amdgcn_sched_group_barrier(0x8, 1, 0); __builtin_amdgcn_sched_group_barrier(0x100, 2, 0); __builtin_amdgcn_sched_group_barrier(0x2, 1, 0);
    }
    SB;
  };
  auto halfstage = [&](int buf, bool rd, LAS char* rbase, int rks, auto&& rq0, auto&& rq1, auto&& rq2) {
    if (rd) { ldq(buf ^ 1, rbase, rks, 0); ldq(buf ^ 1, rbase, rks, 1); }
    row(buf, 0); rq0(); SB;
    if (rd) ldq(buf ^ 1, rbase, rks, 2);
    row(buf, 1); rq1(); SB;
    if (rd) ldq(buf ^ 1, rbase, rks, 3);
    row(buf, 2); rq2(); SB;
    row(buf, 3);
  };
  auto none = [&]() {};
  auto lst = [&](int i) {};
#pragma unroll
  for (int m = 0; m < 4; m++)
#pragma unroll
    for (int n = 0; n < NT; n++) asm volatile("" : "+v"(acc[m][n]));
#pragma unroll
  for (int n = 0; n < NX; n++) asm volatile("" : "+v"(accx[n]));
#pragma unroll
  for (int n = 0; n < NB; n++) asm volatile("" : "+v"(bad[n]));
  asm volatile("" : "+v"(aad[0]), "+v"(aad[1]));
  if (c.side) ldsbar(); else waitbar<9>();
  asm volatile("" ::"v"(c.cpf));
  lst(0);
  STAMP(1);
  auto special = [&](int st) {
    if (st == 1) {
      const v2u ep = *(LAS v2u*)(smem + EPL_OFF);
      const u64 e = ((u64)(unsigned)__builtin_amdgcn_readfirstlane(ep.y) << 32) | (unsigned)__builtin_amdgcn_readfirstlane(ep.x);
      const u64 t = e * 0x5851F42D4C957F2Dull + 0x14057B7EF767814Full;
      c.tag = (unsigned)t; c.taghi = (unsigned)(t >> 32);
      if (c.w == 0) st32_sc1(c.rsC, lane == 0 ? RDY_OFF + c.bid * 4 : OOB, c.tag);
    } else if (st == 6) {
      if (c.w == 0) dma16_sc1(c.rsC, lds0 + RDYL_OFF, lane * 16, RDY_OFF);
    } else if (st == 7) {
      v4u rdy = *(LAS v4u*)(smem + RDYL_OFF + lane * 16);
      for (unsigned spins = 0;; spins++) {
        if (__all(rdy.x == c.tag && rdy.y == c.tag && rdy.z == c.tag && rdy.w == c.tag) || spins > SPIN_CAP) break;
        __builtin_amdgcn_s_sleep(POLL_SLEEP);
        asm volatile("buffer_load_dwordx4 %0, %1, %2, 0 offen offset:%3 sc1\n\ts_waitcnt vmcnt(0)" : "=&v"(rdy) : "v"(lane * 16), "s"(c.rsC), "i"(RDY_OFF) : "memory");
      }
      if (c.w < 2) dma16_sc1(c.rsC, lds0 + RS_OFF + c.w * 1024, lane * 16, STAT_OFF + c.grp * 2048 + c.w * 1024);
    }
  };
#pragma unroll
  for (int i = 0; i < 4; i++) ldq(0, smem, 0, i);
  int so = 0, po = 2 * ST_BYTES;
#pragma nounroll
  for (int st = 0; st < 7; st++) {
    const int no = so == 2 * ST_BYTES ? 0 : so + ST_BYTES;
    halfstage(0, true, smem + so, 1, [&]() { p1_req<3>(c, lds0 + po, st + 2); }, [&]() { p1_req<4>(c, lds0 + po, st + 2); }, [&]() { p1_req<5>(c, lds0 + po, st + 2); });
    waitbar<6>();
    SB;
    lst(st + 1);
    if ((PFMASK >> st) & 1) special(st);
    SB;
    halfstage(1, true, smem + no, 0, [&]() { p1_req<0>(c, lds0 + so, st + 3); }, [&]() { p1_req<1>(c, lds0 + so, st + 3); }, [&]() { p1_req<2>(c, lds0 + so, st + 3); });
    po = so; so = no;
  }
  halfstage(0, true, smem + ST_BYTES, 1, [&]() { p1_req<3>(c, lds0, 9); }, [&]() { p1_req<4>(c, lds0, 9); }, [&]() { p1_req<5>(c, lds0, 9); });
  waitbar<6>(); SB; lst(8);
  special(7); SB;
  halfstage(1, true, smem + 2 * ST_BYTES, 0, [&]() { tail_req<0>(c, lds0); }, [&]() { tail_req<1>(c, lds0); }, [&]() { tail_req<2>(c, lds0); });
  halfstage(0, true, smem + 2 * ST_BYTES, 1, none, none, none);
  waitbar<3>(); SB; lst(9);
  halfstage(1, true, smem, 0, [&]() { tail_req<3>(c, lds0); }, [&]() { tail_req<4>(c, lds0); }, [&]() { tail_req<5>(c, lds0); });
  halfstage(0, true, smem, 1, [&]() { tail_req<6>(c, lds0); }, [&]() { tail_req<7>(c, lds0); }, [&]() { tail_req<8>(c, lds0); });
  halfstage(1, false, smem, 0, [&]() { tail_req<9>(c, lds0); }, none, none);
  lst(10);
  STAMP(2);
#pragma unroll
  for (int n = 0; n < NX; n++)
    *(LAS f32x4*)(smem + (r == 0 ? CS_OFF + (tile[n < NOWN ? n : NT + n - NOWN] * 16 + 4 * g) * 4 : DUMP_OFF + lane * 16)) = accx[n];
  ldsbar();
  STAMP(8);
  f32x2 rs[4];
#pragma unroll
  for (int m = 0; m < 4; m++) rs[m] = *(LAS f32x2*)(smem + RS_OFF + (16 + c.wm * 64 + m * 16 + r) * 8);
  auto ldB = [&](int s) { SB; ldB5(B[s], c.rsT, lane * 16, (c.k0 + s) * 5120); SB; };
  if (c.isffn) {
    if (c.w >= 4) { ldB(0); ldB(1); ldB(2); ldB(3); ldB(4); }
    float c1[4];
#pragma unroll
    for (int m = 0; m < 4; m++) c1[m] = __builtin_amdgcn_rcpf(rs[m].y) * -1.4426950408889634f;
#pragma unroll
    for (int n = 0; n < NT; n++) {
      const f32x4 cs = *(LAS f32x4*)(smem + CS_OFF + (tile[n] * 16 + 4 * g) * 4);
#pragma unroll
      for (int mp = 0; mp < 2; mp++) {
        v4u o;
#pragma unroll
        for (int h = 0; h < 2; h++) {
          const int m = 2 * mp + h;
          const f32x4 t = acc[m][n] - rs[m].x * cs;
          const f32x4 zz = t * c1[m];
          f32x4 e;
#pragma unroll
          for (int j = 0; j < 4; j++) e[j] = __builtin_amdgcn_exp2f(zz[j]);
          const f32x4 d = e * rs[m].y + rs[m].y;
          f32x4 rr;
#pragma unroll
          for (int j = 0; j < 4; j++) rr[j] = __builtin_amdgcn_rcpf(d[j]);
          const f32x4 y = t * rr;
          o[2 * h] = pk2(y[0], y[1]); o[2 * h + 1] = pk2(y[2], y[3]);
        }
        st128_sc1(c.rsH, lane * 16, ((c.grp * 4 + c.wm * 2 + mp) * NCT + c.htile0 + tile[n]) * 1024, o);
      }
    }
  } else {
    float ri[4];
#pragma unroll
    for (int m = 0; m < 4; m++) ri[m] = __builtin_amdgcn_rcpf(rs[m].y);
    f32x4 cs[NB];
#pragma unroll
    for (int n = 0; n < NB; n++) cs[n] = *(LAS f32x4*)(smem + CS_OFF + (tile[n] * 16 + 4 * g) * 4);
    const f32x2 rh = *(LAS f32x2*)(smem + RS_OFF + r * 8);
    const float rih = __builtin_amdgcn_rcpf(rh.y);
    SB;
    static_for<4 * NT + NX>([&](auto U) {
      constexpr int u = decltype(U)::value;
      if constexpr (u < 15) B[u / 5][u % 5] = ldB1<u % 5>(c.rsT, lane * 16, (c.k0 + u / 5) * 5120);
      if constexpr (u < 4 * NT) {
        constexpr int n = u / 4, m = u % 4;
        const f32x4 y = (acc[m][n] - rs[m].x * cs[n]) * ri[m];
        v2u o; o.x = pk2(y[0], y[1]); o.y = pk2(y[2], y[3]);
        *(LAS v2u*)(smem + (16 + c.wm * 64 + m * 16 + r) * IMG_LD + tile[n] * 32 + g * 8) = o;
      } else {
        constexpr int x = u - 4 * NT, n = x < NOWN ? x : NT + x - NOWN;
        const f32x4 y = (accx[x] - rh.x * cs[n]) * rih;
        v2u o; o.x = pk2(y[0], y[1]); o.y = pk2(y[2], y[3]);
        *(LAS v2u*)(smem + r * IMG_LD + tile[n] * 32 + g * 8) = o;
      }
      SB;
    });
    ldsbar();
  }
}

__device__ __forceinline__ void wait_flags(const P1& c, int off, unsigned f) {
  for (unsigned spins = 0;; spins++) {
    if (__all(off == OOB || f == c.tag) || spins > SPIN_CAP) break;
    if (spins) __builtin_amdgcn_s_sleep(POLL_SLEEP);
    asm volatile("buffer_load_dword %0, %1, %2, 0 offen sc1\n\ts_waitcnt vmcnt(0)" : "=&v"(f) : "v"(off), "s"(c.rsC) : "memory");
  }
}
__device__ __forceinline__ void mfma_ip(f32x4& acc, const v4u& b, const v4u& a) {
  asm volatile("v_mfma_f32_16x16x32_bf16 %0, %1, %2, %0" : "+v"(acc) : "v"(b), "v"(a));
}
__device__ __forceinline__ void swap_pair(v4u& l0, v4u& l1) {
  unsigned a = l0.z, b = l1.x, c = l0.w, d = l1.y;
  asm volatile("v_swap_b32 %0, %1" : "+v"(a), "+v"(b));
  l0.z = a; l1.x = b;
  asm volatile("v_swap_b32 %0, %1" : "+v"(c), "+v"(d));
  l0.w = c; l1.y = d;
}
template <int I> __device__ __forceinline__ v4u ldA1(v4i rsH, int voff, int so) {
  return ld128_sc1<(I & 1) * 1024>(rsH, voff, I < 2 ? so : so + NCT * 1024);
}
__device__ __forceinline__ void ldA4(v4u (&A)[4], v4i rsH, int voff, int so) {
  const int so2 = so == OOB ? OOB : so + NCT * 1024;
  A[0] = ld128_sc1<0>(rsH, voff, so); A[1] = ld128_sc1<1024>(rsH, voff, so);
  A[2] = ld128_sc1<0>(rsH, voff, so2); A[3] = ld128_sc1<1024>(rsH, voff, so2);
}
#define WAITA(A, N) asm volatile("s_waitcnt vmcnt(%4) lgkmcnt(0)" : "+v"((A)[0]), "+v"((A)[1]), "+v"((A)[2]), "+v"((A)[3]) : "i"(N) : "memory")
#define ADEEP 6
constexpr int h_k(int s) { return s < 9 ? s : (s == 9 ? 13 : s - 1); }
constexpr int h_na(int t) { return t == ADEEP ? 2 : (t < ADEEP ? 1 : (t + 3 < 14 ? 1 : 0)); }
constexpr int h_a0(int t) { int n = 2; for (int i = 0; i < t; i++) n += h_na(i); return n; }
constexpr int hcnt(int t) { return 4 * h_na(t) + ((t >= 1 && t <= 5) ? 5 : 0) + (t == 8 ? 3 : 0); }
constexpr int hwait(int s) {
  int n = 0;
  if (s < 2) { n = 4 * (1 - s); for (int t = 0; t < s; t++) n += hcnt(t); return n; }
  int t0 = 0; while (h_a0(t0) + h_na(t0) <= s) t0++;
  n = hcnt(t0) - 4 * (s - h_a0(t0) + 1);
  for (int t = t0 + 1; t < s; t++) n += hcnt(t);
  return n;
}

typedef unsigned s8u __attribute__((ext_vector_type(8)));
__global__ __launch_bounds__(512, 2) void fused_kernel(const u16* x_, const u16* wqkv_, const u16* wo_, const u16* w1_, const u16* w2_, u16* out_, char* ws_) {
  extern __shared__ __attribute__((aligned(16))) char smem_[];
  LAS char* smem = (LAS char*)smem_;
  const unsigned lds0 = (unsigned)(size_t)smem;
  s8u ka; v4u kb; v2u kc;
  asm volatile("s_load_dwordx8 %0, %3, 0x0\n\ts_load_dwordx4 %1, %3, 0x20\n\ts_load_dwordx2 %2, %3, 0x30"
               : "=&s"(ka), "=&s"(kb), "=&s"(kc) : "s"(__builtin_amdgcn_kernarg_segment_ptr()));
  SB;
  const int tid = threadIdx.x, lane = tid & 63, w = __builtin_amdgcn_readfirstlane(tid >> 6);
  const int bid = blockIdx.x, xcd = bid & 7, idx = bid >> 3, half = xcd & 1, j8 = idx & 7;
  const int grp = (xcd >> 1) * 4 + (idx >> 3), mem = half * 8 + j8;
  const int isffn = j8 >> 2, sub = half * 4 + (j8 & 3);
  unsigned cpf;
  {
    u64 pc0; unsigned codelen;
    asm volatile("s_getpc_b64 %0\n.Lcode_begin_mark:\n\ts_mov_b32 %1, .Lcode_end_mark-.Lcode_begin_mark" : "=s"(pc0), "=s"(codelen));
    const v4i rp = mkrs((const void*)pc0, (int)codelen);
    asm volatile("buffer_load_dword %0, %1, %2, %3 offen" : "=v"(cpf) : "v"(lane < 2 ? lane * 128 : OOB), "s"(rp), "s"((idx * 8 + w) * 256));
  }
  P1 c;
  c.ldb2 = (3 * DM - isffn * (3 * DM - FF)) * 2;
  c.w = w; c.lane = lane; c.grp = grp; c.isffn = isffn; c.mem = mem;
  const int ftile0 = sub * 14 - half * (j8 & 3);
  const int ntile = 15 - isffn * (1 + half);
  {
    const int ar = lane >> 3, sw = ((lane & 7) ^ ((4 * w + (lane >> 4)) & 7)) << 4;
    c.avoff = (grp * 128 + 8 * w + ar) * (DM * 2) + sw;
    int hr = grp * 128 - 16 + 8 * w + ar; hr = hr < 0 ? 0 : hr;
    c.asvoff = hr * (DM * 2) + sw;
    const int kk = 2 * w + (lane >> 5), zz = (kk & 3) | (((kk >> 3) & 1) << 2), pc = lane & 31, seg = (pc >> 1) ^ zz;
    const int part = (seg * 13) >> 6;
    const int col = seg * 16 + (isffn ? ftile0 * 16 : sub * 80) + (isffn ? 0 : DM - 80) * part;
    const int bo = kk * c.ldb2 + col * 2 + (pc & 1) * 16;
    c.bvoff = seg < ntile ? bo : OOB;
  }
  c.halo = !isffn && w < 2;
  { const int cl = (j8 + grp + half) % NCLS; c.rot = cl * 10 / NCLS; }
  asm volatile("" : "+v"(c.avoff), "+v"(c.asvoff), "+v"(c.bvoff), "+s"(c.rot));
  asm volatile("s_waitcnt lgkmcnt(0)" : "+s"(ka), "+s"(kb), "+s"(kc));
  auto ptr = [](unsigned lo, unsigned hi) { return (char*)(((u64)hi << 32) | lo); };
  const u16* x = (const u16*)ptr(ka[0], ka[1]); const u16* wqkv = (const u16*)ptr(ka[2], ka[3]); const u16* wo = (const u16*)ptr(ka[4], ka[5]);
  const u16* w1 = (const u16*)ptr(ka[6], ka[7]); const u16* w2 = (const u16*)ptr(kb[0], kb[1]); u16* out = (u16*)ptr(kb[2], kb[3]);
  char* ws = ptr(kc[0], kc[1]);
  c.rsA = mkrs(x, SEQ * DM * 2);
  c.rsB = mkrs(isffn ? w1 : wqkv, DM * c.ldb2);
  static_for<6>([&](auto I) { p1_req<decltype(I)::value>(c, lds0, 0); });
  STAMP(0);
  c.rsH = mkrs(ws, H_BYTES);
  c.rsC = mkrs(ws + CTRL_OFF, 1 << 20);
  const int cg = (xcd >> 1) * 4 + half * 2 + (idx >> 4), rh = (idx >> 3) & 1, c3 = idx & 7;
  c.bt = ws + BT_OFF; c.bid = bid;
  c.rsT = mkrs(ws + BT_OFF + (size_t)c3 * BT_SLICE, BT_SLICE);
  c.wm = w >> 2;
  c.k0 = w < 4 ? 5 * w : hk0(w & 3);
  c.k0t = hk0(w & 3) + 9 + 2 * (w >> 2);
  c.tag = 0; c.taghi = 0;
  const int wn = w < 4 ? w : 7 - w;
  c.htile0 = 40 + ftile0;
  const int tsc = w == 3 ? CS_OFF : 2 * ST_BYTES + A_BYTES + 8192 + (w - 4) * 5120;
  v4u xs[10];
  const int sslot = w * 8 + (lane >> 3);
  const int kap = (7 - w) * 16 + grp;
  const bool dostat = isffn && w < 3, dotile = isffn && w >= 3 && kap < 74;
  if (dostat) {
    int rho = sub * 18 + (sslot < 18 ? sslot : 17);
    int row = grp * 128 - 16 + rho; row = row < 0 ? 0 : row;
    const int vo = row * (DM * 2) + (lane & 7) * 16;
    static_for<10>([&](auto I) { constexpr int i = decltype(I)::value; xs[i] = ld128<128 * i>(c.rsA, vo, 0); });
  } else if (dotile) {
    const v4i rw = mkrs(kap < 20 ? wo : w2, kap < 20 ? DM * DM * 2 : DM * FF * 2);
    int kq = kap;
    if (kap >= 20) asm volatile("s_sub_u32 %0, %0, 20" : "+s"(kq)::"scc");
    const int so = kq * 32 * (DM * 2);
#pragma unroll
    for (int i = 0; i < 5; i++) {
      const int pc = 64 * i + lane, rr = pc / 10;
      dma16(rw, lds0 + tsc + i * 1024, rr * (DM * 2) + sub * 160 + (pc - 10 * rr) * 16, so);
    }
  }
  static_for<6>([&](auto I) { p1_req<decltype(I)::value>(c, lds0 + ST_BYTES, 1); });
  static_for<3>([&](auto I) { p1_req<decltype(I)::value>(c, lds0 + 2 * ST_BYTES, 2); });
  c.side = dostat || dotile; c.cpf = cpf;
  if (c.side) asm volatile("s_waitcnt vmcnt(9)" ::: "memory");
  if (w == 7) dma16_sc1(c.rsC, lds0 + EPL_OFF, lane == 0 ? EPOCH_OFF : OOB, 0);
  if (dostat) {
    asm volatile("" : "+v"(xs[0]), "+v"(xs[1]), "+v"(xs[2]), "+v"(xs[3]), "+v"(xs[4]), "+v"(xs[5]), "+v"(xs[6]), "+v"(xs[7]), "+v"(xs[8]), "+v"(xs[9])::"memory");
    float s0 = 0, s1 = 0, q0 = 0, q1 = 0;
#pragma unroll
    for (int i = 0; i < 40; i += 2) {
      const unsigned a = xs[i >> 2][i & 3], b = xs[i >> 2][(i & 3) + 1];
      s0 = dot2(a, 0x3F803F80u, s0); s1 = dot2(b, 0x3F803F80u, s1); q0 = dot2(a, a, q0); q1 = dot2(b, b, q1);
    }
    const float mean = row8_sum(s0 + s1) * (1.0f / DM);
    const float var = fmaxf(row8_sum(q0 + q1) * (1.0f / DM) - mean * mean, 0.f);
    const float sd = __builtin_sqrtf(rbf(rbf(var) + 1e-5f));
    v2u gr; gr.x = __builtin_bit_cast(unsigned, rbf(mean)); gr.y = __builtin_bit_cast(unsigned, rbf(sd));
    st64_sc1(c.rsC, ((lane & 7) == 0 && sslot < 18) ? STAT_OFF + grp * 2048 + (sub * 18 + sslot) * 8 : OOB, gr);
  } else if (dotile) {
    LAS char* tb = smem + tsc + ((lane >> 4) * 4 + ((lane & 15) >> 2)) * 160 + (lane & 3) * 8;
#pragma unroll
    for (int n = 0; n < 5; n++)
      st128_sc1(mkrs(ws + BT_OFF, 8 * BT_SLICE), lane * 16, sub * BT_SLICE + (kap * 5 + n) * 1024, __builtin_bit_cast(v4u, trfrag(tb + n * 32, 16 * 160)));
  }
  v4u B[5][5];
  {
    const int n4 = ntile - 12;
    c.tstart = wn <= n4 ? 4 * wn : 4 * n4 + 3 * (wn - n4);
    if (!isffn) c.ftile0 = 1 + 6 * c.wm;
    else { const int fi = (wn - n4) * 2 + c.wm; c.ftile0 = 1 + 2 * (n4 == 2 ? fi : (fi & 1)); }
    if (wn < n4) phase1<4, 1, 0>(c, smem, lds0, ws, B);
    else if (!isffn) phase1<3, 2, 3>(c, smem, lds0, ws, B);
    else phase1<3, 2, 1>(c, smem, lds0, ws, B);
  }
  STAMP(9);
  const int r = lane & 15, g = lane >> 4;
  const int k0 = c.k0;
  auto ldB = [&](int s) { SB; ldB5(B[s], c.rsT, lane * 16, (k0 + s) * 5120); SB; };
  int foff;
  {
    int m;
    if (w < 4) m = lane < 2 ? (w >> 1) * 8 + (w & 1) * 2 + lane : -1;
    else {
      const int t0 = 2 * (k0 - 20), t1 = t0 + 27 - (w & 2);
      const int f0 = t0 < 56 ? t0 / 14 : 4 + (t0 - 56) / 13, f1 = t1 < 56 ? t1 / 14 : 4 + (t1 - 56) / 13, f = f0 + lane;
      m = f <= f1 ? (f >> 2) * 8 + 4 + (f & 3) : -1;
    }
    foff = m >= 0 ? FLAG_OFF + cg * 256 + m * 4 : OOB;
  }
  const int hso = ((cg * 4 + rh * 2) * NCT + 2 * k0) * 1024;
  v4u E[2][4];
  if (!isffn) {
    unsigned fl;
    const int row = (w & 1) * 64 + lane;
    const int pos = grp * 128 + row;
    LAS char* qp = smem + (row + 16) * IMG_LD + (w >> 1) * 40;
    unsigned qv[10];
#pragma unroll
    for (int i = 0; i < 5; i++) { const v2u t = *(LAS v2u*)(qp + i * 8); qv[2 * i] = t.x; qv[2 * i + 1] = t.y; }
    float lg[8];
    static_for<8>([&](auto K) {
      constexpr int k = decltype(K)::value;
      if constexpr (k < 5) B[3][k] = ldB1<k>(c.rsT, lane * 16, (k0 + 3) * 5120);
      LAS char* kp = qp + 160 - 2 * k * IMG_LD;
      float a0 = 0, a1 = 0;
#pragma unroll
      for (int i = 0; i < 5; i++) { const v2u t = *(LAS v2u*)(kp + i * 8); a0 = dot2(qv[2 * i], t.x, a0); a1 = dot2(qv[2 * i + 1], t.y, a1); }
      lg[k] = pos - 2 * k >= 0 ? a0 + a1 : -4e9f;
    });
    float mx = lg[0];
#pragma unroll
    for (int k = 1; k < 8; k++) mx = fmaxf(mx, lg[k]);
    float sum = 0;
    constexpr float C = 0.22360679774997896f * 1.4426950408889634f;
#pragma unroll
    for (int k = 0; k < 8; k++) { lg[k] = __builtin_amdgcn_exp2f((lg[k] - mx) * C); sum += lg[k]; }
    const float inv = __builtin_amdgcn_rcpf(sum);
    float o[20];
#pragma unroll
    for (int d = 0; d < 20; d++) o[d] = 0;
    static_for<8>([&](auto K) {
      constexpr int k = decltype(K)::value;
      if constexpr (k < 5) B[4][k] = ldB1<k>(c.rsT, lane * 16, (k0 + 4) * 5120);
      if constexpr (k == 5) fl = ld32_sc1(c.rsC, w >= 4 ? foff : OOB);
      const unsigned plo = pk2(lg[k] * inv, 0.f), phi = plo << 16;
      LAS char* vp = qp + 320 - 2 * k * IMG_LD;
#pragma unroll
      for (int i = 0; i < 5; i++) {
        const v2u t = *(LAS v2u*)(vp + i * 8);
        o[4 * i] = dot2(t.x, plo, o[4 * i]); o[4 * i + 1] = dot2(t.x, phi, o[4 * i + 1]);
        o[4 * i + 2] = dot2(t.y, plo, o[4 * i + 2]); o[4 * i + 3] = dot2(t.y, phi, o[4 * i + 3]);
      }
    });
#pragma unroll
    for (int i = 0; i < 5; i++) { v2u t; t.x = pk2(o[4 * i], o[4 * i + 1]); t.y = pk2(o[4 * i + 2], o[4 * i + 3]); *(LAS v2u*)(qp + i * 8) = t; }
    STAMP(11);
    ldsbar();
#pragma unroll
    for (int i = 0; i < 3; i++) {
      const int b0 = w + 8 * i, b = b0 < 20 ? b0 : 19, rp = b / 5, t = b - 5 * rp;
      LAS char* sp = smem + (16 + rp * 32 + r) * IMG_LD + t * 32 + g * 8;
      const v2u lo = *(LAS v2u*)sp, hi = *(LAS v2u*)(sp + 16 * IMG_LD);
      v4u v; v.x = lo.x; v.y = lo.y; v.z = hi.x; v.w = hi.y;
      st128_sc1(c.rsH, lane * 16, b0 < 20 ? ((grp * 4 + rp) * NCT + sub * 5 + t) * 1024 : OOB, v);
    }
    if (w >= 4) {
      asm volatile("s_waitcnt vmcnt(3)" : "+v"(fl)::"memory");
      wait_flags(c, foff, fl);
      ldA4(E[0], c.rsH, lane * 16, hso); ldA4(E[1], c.rsH, lane * 16, hso + 2048);
    }
  }
  if (!isffn && w >= 4) waitbar<8>(); else waitbar<0>();
  st32_sc1(c.rsC, tid == 0 ? FLAG_OFF + grp * 256 + mem * 4 : OOB, c.tag);
  STAMP(4);
  if (isffn && w < 4) {
    __builtin_amdgcn_s_sleep(FSLEEP);
    ldB(0); ldB(1); ldB(2); ldB(3); ldB(4);
    __builtin_amdgcn_s_sleep(FSLEEP2);
  }
  if (w < 4 && !isffn) __builtin_amdgcn_s_sleep(OSLEEP);
  if (isffn || w < 4) wait_flags(c, foff, ~c.tag);
  if (isffn && w >= 4) { ldA4(E[0], c.rsH, lane * 16, hso); ldA4(E[1], c.rsH, lane * 16, hso + 2048); }
  STAMP(5);
  v2u xr[3];
  auto ldx = [&]() {
#pragma unroll
    for (int i = 0; i < 3; i++) {
      const int tl = w + 8 * i, tt = tl < 20 ? tl : 19, m = tt / 5, n = tt - 5 * m;
      xr[i] = ld64(c.rsA, (cg * 128 + rh * 64 + m * 16 + r) * (DM * 2) + (c3 * 80 + n * 16 + g * 4) * 2, 0);
    }
  };
  auto put = [&](const f32x4 (&acc)[4][5], int p0, int p1, int stride) {
    STAMP(6);
    asm volatile("s_nop 7\n\ts_nop 3\n\ts_waitcnt vmcnt(0) lgkmcnt(0)" : "+v"(xr[0]), "+v"(xr[1]), "+v"(xr[2])::"memory");
#pragma unroll
    for (int m = 0; m < 4; m++)
#pragma unroll
      for (int n = 0; n < 5; n++) *(LAS f32x4*)(smem + (m < 2 ? p0 : p1) + ((m & 1) * 5 + n) * stride + lane * 16) = acc[m][n];
  };
  const int vo = lane * 16;
  if (w < 4) {
    v4u A[5][4];
    auto ldA = [&](int s) { ldA4(A[s], c.rsH, vo, hso + s * 2048); };
    ldA(0); ldA(1); ldA(2);
    f32x4 acc[4][5];
#pragma unroll
    for (int m = 0; m < 4; m++)
#pragma unroll
      for (int n = 0; n < 5; n++) acc[m][n] = f32x4{0, 0, 0, 0};
#pragma unroll
    for (int m = 0; m < 4; m++) TIE5(acc[m]);
#pragma unroll
    for (int s = 0; s < 5; s++) TIE5(B[s]);
    WAITA(A[0], 8); swap_pair(A[0][0], A[0][1]); swap_pair(A[0][2], A[0][3]);
    asm volatile("s_nop 1");
    static_for<5>([&](auto S) {
      constexpr int s = decltype(S)::value;
      static_for<20>([&](auto I) {
        constexpr int i = decltype(I)::value;
        mfma_ip(acc[i / 5][i % 5], B[s][i % 5], A[s][i / 5]);
        if constexpr ((s == 1 || s == 2) && i < 8 && (i & 1)) A[s + 2][i >> 1] = ldA1<(i >> 1)>(c.rsH, vo, hso + (s + 2) * 2048);
        if constexpr (s == 2 && i == 9) ldx();
        if constexpr (s < 4 && i == 13) WAITA(A[s + 1], (s == 0 ? 4 : s == 1 ? 4 : s == 2 ? 7 : 3));
        if constexpr (s < 4 && i == 14) swap_pair(A[s + 1][0], A[s + 1][1]);
        if constexpr (s < 4 && i == 15) swap_pair(A[s + 1][2], A[s + 1][3]);
      });
    });
    put(acc, w * 20480, w < 3 ? w * 20480 + 10240 : CS_OFF, 1024);
#pragma unroll
    for (int s = 0; s < 5; s++)
#pragma unroll
      for (int n = 0; n < 5; n++) asm volatile("" : "=v"(B[s][n]));
#pragma unroll
    for (int i = 0; i < 8; i++) asm volatile("" : "=v"(E[i >> 2][i & 3]));
  }
  int w_h = w;
  asm volatile("" : "+s"(w_h));
  if (w_h >= 4) {
    const int hw = w & 3;
    v4u A[14][4], Bs[14][5];
    const int last = hw < 2 ? 0 : OOB;
#pragma unroll
    for (int i = 0; i < 8; i++) A[i >> 2][i & 3] = E[i >> 2][i & 3];
    f32x4 acc[4][5];
#pragma unroll
    for (int m = 0; m < 4; m++)
#pragma unroll
      for (int n = 0; n < 5; n++) acc[m][n] = f32x4{0, 0, 0, 0};
#pragma unroll
    for (int m = 0; m < 4; m++) TIE5(acc[m]);
    LAS char* eb = smem + RING_OFF + hw * 1024 + lane * 16;
#pragma unroll
    for (int s = 0; s < 5; s++) {
      TIE5(B[s]);
#pragma unroll
      for (int n = 0; n < 5; n++) Bs[s][n] = B[s][n];
    }
    WAITA(A[0], 4); swap_pair(A[0][0], A[0][1]); swap_pair(A[0][2], A[0][3]);
    asm volatile("s_nop 1");
    static_for<14>([&](auto S) {
      constexpr int s = decltype(S)::value;
      static_for<20>([&](auto I) {
        constexpr int i = decltype(I)::value;
        mfma_ip(acc[i / 5][i % 5], Bs[s][i % 5], A[s][i / 5]);
        if constexpr (i < 4 * h_na(s)) {
          constexpr int k = h_a0(s) + (i >> 2);
          A[k][i & 3] = ldA1<(i & 3)>(c.rsH, k == 9 ? (vo | last) : vo, hso + h_k(k) * 2048);
        }
        if constexpr (s >= 1 && s <= 5 && i >= 4 && i < 9) Bs[s + 4][i - 4] = ldB1<i - 4>(c.rsT, s == 5 ? (vo | last) : vo, (k0 + h_k(s + 4)) * 5120);
        if constexpr (s == 8 && i == 9) ldx();
        if constexpr (s >= 9 && s < 13 && i >= 9 && i < 14) {
          SB; Bs[s + 1][i - 9] = *(LAS v4u*)(eb + (((s - 9) & 1) * 5 + i - 9) * 8192 + ((s - 9) >> 1) * 4096); SB;
        }
        if constexpr (s < 13 && i == 14) {
          asm volatile("s_waitcnt vmcnt(%4)" : "+v"(A[s + 1][0]), "+v"(A[s + 1][1]), "+v"(A[s + 1][2]), "+v"(A[s + 1][3]) : "i"(hwait(s + 1)) : "memory");
          if constexpr (s + 1 >= 5 && s + 1 <= 9) TIE5(Bs[s + 1]);
        }
        if constexpr (s < 13 && i == 15) swap_pair(A[s + 1][0], A[s + 1][1]);
        if constexpr (s < 13 && i == 16) swap_pair(A[s + 1][2], A[s + 1][3]);
      });
    });
    put(acc, RING_OFF + hw * 1024, RING_OFF + hw * 1024 + 4096, 8192);
  }
  STAMP(12);
  ldsbar();
  STAMP(13);
  if (w == 0 && bid == 0) { v2u t; t.x = c.tag; t.y = c.taghi; st64_sc1(c.rsC, lane == 0 ? EPOCH_OFF : OOB, t); }
#pragma unroll
  for (int i = 0; i < 3; i++) {
    const int tl = w + 8 * i;
    if (tl < 20) {
      const int m = tl / 5, n = tl - 5 * m;
      LAS char* ps = smem + tl * 1024 + lane * 16;
      LAS char* pf = smem + RING_OFF + (tl < 10 ? tl * 8192 : (tl - 10) * 8192 + 4096) + lane * 16;
      const f32x4 s = (*(LAS f32x4*)ps + *(LAS f32x4*)(ps + 20480)) + (*(LAS f32x4*)(ps + 40960) + *(LAS f32x4*)(ps + (tl < 10 ? 61440 : CS_OFF - 10240)));
      const f32x4 f = (*(LAS f32x4*)pf + *(LAS f32x4*)(pf + 1024)) + (*(LAS f32x4*)(pf + 2048) + *(LAS f32x4*)(pf + 3072));
      float y[4];
#pragma unroll
      for (int j = 0; j < 4; j++) {
        const float xv = (j & 1) ? bfhi(xr[i][j >> 1]) : bflo(xr[i][j >> 1]);
        y[j] = xv + s[j] * silu_f(f[j]);
      }
      v2u o; o.x = pk2(y[0], y[1]); o.y = pk2(y[2], y[3]);
      *(GAS v2u*)((GAS char*)out + (size_t)(cg * 128 + rh * 64 + m * 16 + r) * (DM * 2) + (c3 * 80 + n * 16 + g * 4) * 2) = o;
    }
  }
  STAMP(7);
  asm volatile(".Lcode_end_mark:");
}
}

extern "C" void kernel_launch(void* const* d_in, const int* in_sizes, int n_in, void* d_out, int out_size, void* d_ws, size_t ws_size,
                              hipStream_t stream) {
  (void)hipFuncSetAttribute((const void*)fused_kernel, hipFuncAttributeMaxDynamicSharedMemorySize, LDS_BYTES);
  fused_kernel<<<256, 512, LDS_BYTES, stream>>>((const u16*)d_in[0], (const u16*)d_in[1], (const u16*)d_in[2], (const u16*)d_in[4],
                                                (const u16*)d_in[5], (u16*)d_out, (char*)d_ws);
}
```

```cpp
#include <hip/hip_runtime.h>
#include <stdint.h>
#include <type_traits>
#include <utility>

#define GAS __attribute__((address_space(1)))
#define LAS __attribute__((address_space(3)))
typedef unsigned short u16;
typedef unsigned long long u64;
typedef __bf16 v2bf __attribute__((ext_vector_type(2)));
typedef __bf16 v4bf __attribute__((ext_vector_type(4)));
typedef __bf16 v8bf __attribute__((ext_vector_type(8)));
typedef float f32x2 __attribute__((ext_vector_type(2)));
typedef float f32x4 __attribute__((ext_vector_type(4)));
typedef unsigned v4u __attribute__((ext_vector_type(4)));
typedef unsigned v2u __attribute__((ext_vector_type(2)));
typedef int v4i __attribute__((ext_vector_type(4)));

namespace {
constexpr int SEQ = 2048, DM = 640, FF = 1728;
constexpr int NCT = 148;
constexpr int H_BYTES = 64 * NCT * 1024;
constexpr size_t CTRL_OFF = 16u << 20;
constexpr int EPOCH_OFF = 0, RDY_OFF = 2048, FLAG_OFF = 8192, STAT_OFF = 65536;
constexpr size_t BT_OFF = 12u << 20;
constexpr int BT_SLICE = 74 * 5120;
constexpr int A_BYTES = 144 * 128, B_BYTES = 64 * 512, ST_BYTES = A_BYTES + B_BYTES;
constexpr int CS_OFF = 3 * ST_BYTES, RS_OFF = CS_OFF + 1024;
constexpr int RDYL_OFF = CS_OFF + 6144, EPL_OFF = CS_OFF + 7168;
constexpr int DUMP_OFF = CS_OFF + 4096;
constexpr int IMG_LD = 488;
constexpr int RING_OFF = 71680;
constexpr int LDS_BYTES = 163840;
constexpr int OOB = 0x7FF00000;
constexpr unsigned SPIN_CAP = 1u << 16;
#define SGB 1
#define NCLS 3
#define PFMASK 0x42
#define OSLEEP 30
#define FSLEEP 0
#define FSLEEP2 70
#define SSLEEP 16
#define MAP3 1
#define POLL_SLEEP 8
#define SB __builtin_amdgcn_sched_barrier(0)
#define LDPOL2 " sc1"

__device__ __forceinline__ v4i mkrs(const void* p, int bytes) {
  u64 a = (u64)p; v4i r; r.x = (int)(unsigned)a; r.y = (int)((unsigned)(a >> 32) & 0xFFFFu); r.z = bytes; r.w = 0x00020000; return r;
}
__device__ __forceinline__ void dma16(v4i rs, unsigned lds, int voff, int soff) {
  asm volatile("s_mov_b32 m0, %0\n\ts_nop 0\n\tbuffer_load_dwordx4 %1, %2, %3 offen lds" ::"s"(lds), "v"(voff), "s"(rs), "s"(soff));
}
__device__ __forceinline__ void dma16_sc1(v4i rs, unsigned lds, int voff, int soff) {
  asm volatile("s_mov_b32 m0, %0\n\ts_nop 3\n\tbuffer_load_dwordx4 %1, %2, %3 offen" LDPOL2 " lds" ::"s"(lds), "v"(voff), "s"(rs), "s"(soff));
}
__device__ __forceinline__ void dma16_50(v4i rs, unsigned lds, int voff, int soff) {
  asm volatile("s_mov_b32 m0, %0\n\ts_mov_b32 exec_hi, 0x3ffff\n\tbuffer_load_dwordx4 %1, %2, %3 offen lds\n\ts_mov_b32 exec_hi, -1" ::"s"(lds), "v"(voff), "s"(rs), "s"(soff));
}
#define LDPOL " sc1"
#define STPOL " sc1"
template <int IMM> __device__ __forceinline__ v4u ld128_sc1(v4i rs, int voff, int soff) {
  v4u r; asm volatile("buffer_load_dwordx4 %0, %1, %2, %3 offen offset:%4" LDPOL : "=v"(r) : "v"(voff), "s"(rs), "s"(soff), "i"(IMM)); return r;
}
template <int IMM> __device__ __forceinline__ v4u ld128(v4i rs, int voff, int soff) {
  v4u r; asm volatile("buffer_load_dwordx4 %0, %1, %2, %3 offen offset:%4" : "=v"(r) : "v"(voff), "s"(rs), "s"(soff), "i"(IMM)); return r;
}
template <int IMM> __device__ __forceinline__ unsigned ld16(v4i rs, int voff, int soff) {
  unsigned r; asm volatile("buffer_load_ushort %0, %1, %2, %3 offen offset:%4" : "=v"(r) : "v"(voff), "s"(rs), "s"(soff), "i"(IMM)); return r;
}
__device__ __forceinline__ v2u ld64(v4i rs, int voff, int soff) {
  v2u r; asm volatile("buffer_load_dwordx2 %0, %1, %2, %3 offen" : "=v"(r) : "v"(voff), "s"(rs), "s"(soff)); return r;
}
__device__ __forceinline__ unsigned ld32_sc1(v4i rs, int voff) {
  unsigned r; asm volatile("buffer_load_dword %0, %1, %2, 0 offen sc1" : "=v"(r) : "v"(voff), "s"(rs)); return r;
}
__device__ __forceinline__ v2u ld64_sc1(v4i rs, int voff, int soff) {
  v2u r; asm volatile("buffer_load_dwordx2 %0, %1, %2, %3 offen sc1" : "=v"(r) : "v"(voff), "s"(rs), "s"(soff)); return r;
}
__device__ __forceinline__ v2u ld64_sc1_wait(v4i rs, int voff) {
  v2u r; asm volatile("buffer_load_dwordx2 %0, %1, %2, 0 offen sc1\n\ts_waitcnt vmcnt(0)" : "=&v"(r) : "v"(voff), "s"(rs) : "memory"); return r;
}
__device__ __forceinline__ void st128_sc1(v4i rs, int voff, int soff, v4u v) {
  asm volatile("buffer_store_dwordx4 %0, %1, %2, %3 offen" STPOL "\n\ts_nop 1" ::"v"(v), "v"(voff), "s"(rs), "s"(soff) : "memory");
}
__device__ __forceinline__ void st64_sc1(v4i rs, int voff, v2u v) {
  asm volatile("buffer_store_dwordx2 %0, %1, %2, 0 offen sc1" ::"v"(v), "v"(voff), "s"(rs) : "memory");
}
__device__ __forceinline__ void st32_sc1(v4i rs, int voff, unsigned v) {
  asm volatile("buffer_store_dword %0, %1, %2, 0 offen sc1" ::"v"(v), "v"(voff), "s"(rs) : "memory");
}
template <int N> __device__ __forceinline__ void waitbar() { asm volatile("s_waitcnt vmcnt(%0) lgkmcnt(0)\n\ts_barrier" ::"i"(N) : "memory"); }
__device__ __forceinline__ void ldsbar() { asm volatile("s_waitcnt lgkmcnt(0)\n\ts_barrier" ::: "memory"); }
__device__ __forceinline__ void drain() { asm volatile("s_waitcnt vmcnt(0)" ::: "memory"); }
__device__ __forceinline__ float bflo(unsigned u) { return __builtin_bit_cast(float, u << 16); }
__device__ __forceinline__ float bfhi(unsigned u) { return __builtin_bit_cast(float, u & 0xFFFF0000u); }
__device__ __forceinline__ unsigned pk2(float a, float b) {
  v2bf v; v.x = (__bf16)a; v.y = (__bf16)b; return __builtin_bit_cast(unsigned, v);
}
__device__ __forceinline__ float rbf(float a) { return (float)(__bf16)a; }
__device__ __forceinline__ float dot2(unsigned a, unsigned b, float c) {
  return __builtin_amdgcn_fdot2_f32_bf16(__builtin_bit_cast(v2bf, a), __builtin_bit_cast(v2bf, b), c, false);
}
__device__ __forceinline__ f32x4 mfma(v8bf a, v8bf b, f32x4 c) { return __builtin_amdgcn_mfma_f32_16x16x32_bf16(a, b, c, 0, 0, 0); }
__device__ __forceinline__ v8bf trfrag(LAS char* p, int second) {
  v4bf lo = __builtin_amdgcn_ds_read_tr16_b64_v4bf16((LAS v4bf*)p);
  v4bf hi = __builtin_amdgcn_ds_read_tr16_b64_v4bf16((LAS v4bf*)(p + second));
  return __builtin_shufflevector(lo, hi, 0, 1, 2, 3, 4, 5, 6, 7);
}
__device__ __forceinline__ float row8_sum(float v) {
  v += __builtin_bit_cast(float, __builtin_amdgcn_update_dpp(0, __builtin_bit_cast(int, v), 0xB1, 0xF, 0xF, false));
  v += __builtin_bit_cast(float, __builtin_amdgcn_update_dpp(0, __builtin_bit_cast(int, v), 0x4E, 0xF, 0xF, false));
  v += __builtin_bit_cast(float, __builtin_amdgcn_update_dpp(0, __builtin_bit_cast(int, v), 0x141, 0xF, 0xF, false));
  return v;
}
__device__ __forceinline__ float silu_f(float y) { return y * __builtin_amdgcn_rcpf(1.0f + __builtin_amdgcn_exp2f(y * -1.4426950408889634f)); }
template <class F, int... I> __device__ __forceinline__ void static_for_impl(F&& f, std::integer_sequence<int, I...>) { (f(std::integral_constant<int, I>{}), ...); }
template <int N, class F> __device__ __forceinline__ void static_for(F&& f) { static_for_impl(f, std::make_integer_sequence<int, N>{}); }

struct P1 {
  v4i rsA, rsB, rsH, rsC, rsT;
  int avoff, asvoff, bvoff, ldb2;
  int pfso, bid;
  int w, wm, lane, grp, mem, rot;
  const char* bt;
  int tstart, ftile0;
  int isffn, htile0;
  unsigned tag, taghi;
  int k0, k0t, halo, side;
  unsigned cpf;
};
__device__ __forceinline__ int hk0(int hw) { return 20 + 14 * hw - ((hw + 1) >> 2); }

template <int I, bool FIRST = false> __device__ __forceinline__ void p1_req(const P1& c, unsigned base, int st) {
  if (!FIRST) {
    st += c.rot;
    int t; asm("s_cmp_ge_i32 %0, 10\n\ts_cselect_b32 %1, 9, 0\n\ts_sub_i32 %0, %0, %1" : "+s"(st), "=&s"(t)::"scc");
  }
  if (I == 0 && c.halo) dma16(c.rsA, base + c.w * 1024, c.asvoff, st * 128);
  if (I < 2) dma16(c.rsA, base + (2 + c.w + 8 * I) * 1024, c.avoff, st * 128 + I * 64 * (DM * 2));
  else dma16(c.rsB, base + A_BYTES + (c.w + 8 * (I - 2)) * 1024, c.bvoff, (st * 64 + (I - 2) * 16) * c.ldb2);
}
template <int E> __device__ __forceinline__ void tail_req(const P1& c, unsigned lds0) {
  dma16_sc1(c.rsT, lds0 + RING_OFF + E * 8192 + c.w * 1024, c.lane * 16, c.k0t * 5120 + E * 1024);
}

__device__ __forceinline__ void ldB5(v4u (&B)[5], v4i rsT, int voff, int so) {
  const int so2 = so == OOB ? OOB : so + 4096;
  B[0] = ld128_sc1<0>(rsT, voff, so); B[1] = ld128_sc1<1024>(rsT, voff, so); B[2] = ld128_sc1<2048>(rsT, voff, so);
  B[3] = ld128_sc1<3072>(rsT, voff, so); B[4] = ld128_sc1<0>(rsT, voff, so2);
}
#define TIE5(B) asm volatile("" : "+v"((B)[0]), "+v"((B)[1]), "+v"((B)[2]), "+v"((B)[3]), "+v"((B)[4]))

template <int I> __device__ __forceinline__ v4u ldB1(v4i rsT, int voff, int so) {
  return ld128_sc1<(I & 3) * 1024>(rsT, voff, I < 4 ? so : so + 4096);
}
template <int NT, int NOWN, int NFOR>
__device__ __forceinline__ void phase1(P1& c, LAS char* smem, unsigned lds0, char* ws, v4u (&B)[5][5]) {
  constexpr int NB = NT + NFOR, NX = NOWN + NFOR;
  const int lane = c.lane, r = lane & 15, g = lane >> 4, q = r >> 2, p = lane & 3;
  int aad[2];
#pragma unroll
  for (int ks = 0; ks < 2; ks++) aad[ks] = r * 128 + (((4 * ks + g) ^ (r >> 1)) << 4);
  const int amain = (1 + c.wm * 4) * 2048;
  const int z = q | ((g & 1) << 2);
  int tile[NB], bad[NB];
#pragma unroll
  for (int n = 0; n < NB; n++) {
    if (n < NT) { int t = n + 2 * c.wm; t = t >= NT ? t - NT : t; tile[n] = c.tstart + t; }
    else tile[n] = c.ftile0 + 2 * (n - NT);
    bad[n] = A_BYTES + (8 * g + q) * 512 + (((tile[n] & 7) ^ z) << 5) + (tile[n] >> 3) * 256 + p * 8;
  }
  f32x4 acc[4][NT], accx[NX];
#pragma unroll
  for (int m = 0; m < 4; m++)
#pragma unroll
    for (int n = 0; n < NT; n++) acc[m][n] = f32x4{0, 0, 0, 0};
#pragma unroll
  for (int n = 0; n < NX; n++) accx[n] = f32x4{0, 0, 0, 0};
  v8bf af[2][4], as[2], bf[2][NB];
  const bool ones = (r == 0) || c.isffn;
  auto ldq = [&](int buf, LAS char* base, int ks, int i) {
    af[buf][i] = *(LAS v8bf*)(base + aad[ks] + amain + i * 2048);
#pragma unroll
    for (int n = 0; n < NB; n++) if ((n & 3) == i) bf[buf][n] = trfrag(base + bad[n] + ks * 16384, 2048);
    if (i == 3) as[buf] = *(LAS v8bf*)(base + aad[ks]);
  };
  auto row = [&](int buf, int m) {
#pragma unroll
    for (int n = 0; n < NT; n++) acc[m][n] = mfma(bf[buf][n], af[buf][m], acc[m][n]);
    if (m == 3) {
      v4u s = __builtin_bit_cast(v4u, as[buf]);
#pragma unroll
      for (int e = 0; e < 4; e++) s[e] = ones ? 0x3F803F80u : s[e];
      v8bf sv = __builtin_bit_cast(v8bf, s);
#pragma unroll
      for (int n = 0; n < NX; n++) accx[n] = mfma(bf[buf][n < NOWN ? n : NT + n - NOWN], sv, accx[n]);
    }
#pragma unroll
    for (int i = 0; i < NT; i++) {
      __builtin_amdgcn_sched_group_barrier(0x8, 1, 0); __builtin_amdgcn_sched_group_barrier(0x100, 2, 0); __builtin_amdgcn_sched_group_barrier(0x2, 1, 0);
    }
    SB;
  };
  auto halfstage = [&](int buf, bool rd, LAS char* rbase, int rks, auto&& rq0, auto&& rq1, auto&& rq2) {
    if (rd) { ldq(buf ^ 1, rbase, rks, 0); ldq(buf ^ 1, rbase, rks, 1); }
    row(buf, 0); rq0(); SB;
    if (rd) ldq(buf ^ 1, rbase, rks, 2);
    row(buf, 1); rq1(); SB;
    if (rd) ldq(buf ^ 1, rbase, rks, 3);
    row(buf, 2); rq2(); SB;
    row(buf, 3);
  };
  auto none = [&]() {};
#pragma unroll
  for (int m = 0; m < 4; m++)
#pragma unroll
    for (int n = 0; n < NT; n++) asm volatile("" : "+v"(acc[m][n]));
#pragma unroll
  for (int n = 0; n < NX; n++) asm volatile("" : "+v"(accx[n]));
#pragma unroll
  for (int n = 0; n < NB; n++) asm volatile("" : "+v"(bad[n]));
  asm volatile("" : "+v"(aad[0]), "+v"(aad[1]));
  if (c.side) ldsbar(); else waitbar<9>();
  asm volatile("" ::"v"(c.cpf));

  auto special = [&](int st) {
    if (st == 1) {
      const v2u ep = *(LAS v2u*)(smem + EPL_OFF);
      const u64 e = ((u64)(unsigned)__builtin_amdgcn_readfirstlane(ep.y) << 32) | (unsigned)__builtin_amdgcn_readfirstlane(ep.x);
      const u64 t = e * 0x5851F42D4C957F2Dull + 0x14057B7EF767814Full;
      c.tag = (unsigned)t; c.taghi = (unsigned)(t >> 32);
      if (c.w == 0) st32_sc1(c.rsC, lane == 0 ? RDY_OFF + c.bid * 4 : OOB, c.tag);
    } else if (st == 6) {
      if (c.w == 0) dma16_sc1(c.rsC, lds0 + RDYL_OFF, lane * 16, RDY_OFF);
    } else if (st == 7) {
      v4u rdy = *(LAS v4u*)(smem + RDYL_OFF + lane * 16);
      for (unsigned spins = 0;; spins++) {
        if (__all(rdy.x == c.tag && rdy.y == c.tag && rdy.z == c.tag && rdy.w == c.tag) || spins > SPIN_CAP) break;
        __builtin_amdgcn_s_sleep(POLL_SLEEP);
        asm volatile("buffer_load_dwordx4 %0, %1, %2, 0 offen offset:%3 sc1\n\ts_waitcnt vmcnt(0)" : "=&v"(rdy) : "v"(lane * 16), "s"(c.rsC), "i"(RDY_OFF) : "memory");
      }
      if (c.w < 2) dma16_sc1(c.rsC, lds0 + RS_OFF + c.w * 1024, lane * 16, STAT_OFF + c.grp * 2048 + c.w * 1024);
    }
  };
#pragma unroll
  for (int i = 0; i < 4; i++) ldq(0, smem, 0, i);
  int so = 0, po = 2 * ST_BYTES;
#pragma nounroll
  for (int st = 0; st < 7; st++) {
    const int no = so == 2 * ST_BYTES ? 0 : so + ST_BYTES;
    halfstage(0, true, smem + so, 1, [&]() { p1_req<3>(c, lds0 + po, st + 2); }, [&]() { p1_req<4>(c, lds0 + po, st + 2); }, [&]() { p1_req<5>(c, lds0 + po, st + 2); });
    waitbar<6>();
    SB;
    if ((PFMASK >> st) & 1) special(st);
    SB;
    halfstage(1, true, smem + no, 0, [&]() { p1_req<0>(c, lds0 + so, st + 3); }, [&]() { p1_req<1>(c, lds0 + so, st + 3); }, [&]() { p1_req<2>(c, lds0 + so, st + 3); });
    po = so; so = no;
  }
  halfstage(0, true, smem + ST_BYTES, 1, [&]() { p1_req<3>(c, lds0, 9); }, [&]() { p1_req<4>(c, lds0, 9); }, [&]() { p1_req<5>(c, lds0, 9); });
  waitbar<6>(); SB;
  special(7); SB;
  halfstage(1, true, smem + 2 * ST_BYTES, 0, [&]() { tail_req<0>(c, lds0); }, [&]() { tail_req<1>(c, lds0); }, [&]() { tail_req<2>(c, lds0); });
  halfstage(0, true, smem + 2 * ST_BYTES, 1, none, none, none);
  waitbar<3>(); SB;
  halfstage(1, true, smem, 0, [&]() { tail_req<3>(c, lds0); }, [&]() { tail_req<4>(c, lds0); }, [&]() { tail_req<5>(c, lds0); });
  halfstage(0, true, smem, 1, [&]() { tail_req<6>(c, lds0); }, [&]() { tail_req<7>(c, lds0); }, [&]() { tail_req<8>(c, lds0); });
  halfstage(1, false, smem, 0, [&]() { tail_req<9>(c, lds0); }, none, none);

#pragma unroll
  for (int n = 0; n < NX; n++)
    *(LAS f32x4*)(smem + (r == 0 ? CS_OFF + (tile[n < NOWN ? n : NT + n - NOWN] * 16 + 4 * g) * 4 : DUMP_OFF + lane * 16)) = accx[n];
  ldsbar();
  f32x2 rs[4];
#pragma unroll
  for (int m = 0; m < 4; m++) rs[m] = *(LAS f32x2*)(smem + RS_OFF + (16 + c.wm * 64 + m * 16 + r) * 8);
  auto ldB = [&](int s) { SB; ldB5(B[s], c.rsT, lane * 16, (c.k0 + s) * 5120); SB; };
  if (c.isffn) {
    if (c.w >= 4) { ldB(0); ldB(1); ldB(2); ldB(3); ldB(4); }
    float c1[4];
#pragma unroll
    for (int m = 0; m < 4; m++) c1[m] = __builtin_amdgcn_rcpf(rs[m].y) * -1.4426950408889634f;
#pragma unroll
    for (int n = 0; n < NT; n++) {
      const f32x4 cs = *(LAS f32x4*)(smem + CS_OFF + (tile[n] * 16 + 4 * g) * 4);
#pragma unroll
      for (int mp = 0; mp < 2; mp++) {
        v4u o;
#pragma unroll
        for (int h = 0; h < 2; h++) {
          const int m = 2 * mp + h;
          const f32x4 t = acc[m][n] - rs[m].x * cs;
          const f32x4 zz = t * c1[m];
          f32x4 e;
#pragma unroll
          for (int j = 0; j < 4; j++) e[j] = __builtin_amdgcn_exp2f(zz[j]);
          const f32x4 d = e * rs[m].y + rs[m].y;
          f32x4 rr;
#pragma unroll
          for (int j = 0; j < 4; j++) rr[j] = __builtin_amdgcn_rcpf(d[j]);
          const f32x4 y = t * rr;
          o[2 * h] = pk2(y[0], y[1]); o[2 * h + 1] = pk2(y[2], y[3]);
        }
        st128_sc1(c.rsH, lane * 16, ((c.grp * 4 + c.wm * 2 + mp) * NCT + c.htile0 + tile[n]) * 1024, o);
      }
    }
  } else {
    float ri[4];
#pragma unroll
    for (int m = 0; m < 4; m++) ri[m] = __builtin_amdgcn_rcpf(rs[m].y);
    f32x4 cs[NB];
#pragma unroll
    for (int n = 0; n < NB; n++) cs[n] = *(LAS f32x4*)(smem + CS_OFF + (tile[n] * 16 + 4 * g) * 4);
    const f32x2 rh = *(LAS f32x2*)(smem + RS_OFF + r * 8);
    const float rih = __builtin_amdgcn_rcpf(rh.y);
    SB;
    static_for<4 * NT + NX>([&](auto U) {
      constexpr int u = decltype(U)::value;
      if constexpr (u < 15) B[u / 5][u % 5] = ldB1<u % 5>(c.rsT, lane * 16, (c.k0 + u / 5) * 5120);
      if constexpr (u < 4 * NT) {
        constexpr int n = u / 4, m = u % 4;
        const f32x4 y = (acc[m][n] - rs[m].x * cs[n]) * ri[m];
        v2u o; o.x = pk2(y[0], y[1]); o.y = pk2(y[2], y[3]);
        *(LAS v2u*)(smem + (16 + c.wm * 64 + m * 16 + r) * IMG_LD + tile[n] * 32 + g * 8) = o;
      } else {
        constexpr int x = u - 4 * NT, n = x < NOWN ? x : NT + x - NOWN;
        const f32x4 y = (accx[x] - rh.x * cs[n]) * rih;
        v2u o; o.x = pk2(y[0], y[1]); o.y = pk2(y[2], y[3]);
        *(LAS v2u*)(smem + r * IMG_LD + tile[n] * 32 + g * 8) = o;
      }
      SB;
    });
    ldsbar();
  }
}

__device__ __forceinline__ void wait_flags(const P1& c, int off, unsigned f) {
  for (unsigned spins = 0;; spins++) {
    if (__all(off == OOB || f == c.tag) || spins > SPIN_CAP) break;
    if (spins) __builtin_amdgcn_s_sleep(POLL_SLEEP);
    asm volatile("buffer_load_dword %0, %1, %2, 0 offen sc1\n\ts_waitcnt vmcnt(0)" : "=&v"(f) : "v"(off), "s"(c.rsC) : "memory");
  }
}
__device__ __forceinline__ void mfma_ip(f32x4& acc, const v4u& b, const v4u& a) {
  asm volatile("v_mfma_f32_16x16x32_bf16 %0, %1, %2, %0" : "+v"(acc) : "v"(b), "v"(a));
}
__device__ __forceinline__ void swap_pair(v4u& l0, v4u& l1) {
  unsigned a = l0.z, b = l1.x, c = l0.w, d = l1.y;
  asm volatile("v_swap_b32 %0, %1" : "+v"(a), "+v"(b));
  l0.z = a; l1.x = b;
  asm volatile("v_swap_b32 %0, %1" : "+v"(c), "+v"(d));
  l0.w = c; l1.y = d;
}
template <int I> __device__ __forceinline__ v4u ldA1(v4i rsH, int voff, int so) {
  return ld128_sc1<(I & 1) * 1024>(rsH, voff, I < 2 ? so : so + NCT * 1024);
}
__device__ __forceinline__ void ldA4(v4u (&A)[4], v4i rsH, int voff, int so) {
  const int so2 = so == OOB ? OOB : so + NCT * 1024;
  A[0] = ld128_sc1<0>(rsH, voff, so); A[1] = ld128_sc1<1024>(rsH, voff, so);
  A[2] = ld128_sc1<0>(rsH, voff, so2); A[3] = ld128_sc1<1024>(rsH, voff, so2);
}
#define WAITA(A, N) asm volatile("s_waitcnt vmcnt(%4) lgkmcnt(0)" : "+v"((A)[0]), "+v"((A)[1]), "+v"((A)[2]), "+v"((A)[3]) : "i"(N) : "memory")
#define ADEEP 6
constexpr int h_k(int s) { return s < 9 ? s : (s == 9 ? 13 : s - 1); }
constexpr int h_na(int t) { return t == ADEEP ? 2 : (t < ADEEP ? 1 : (t + 3 < 14 ? 1 : 0)); }
constexpr int h_a0(int t) { int n = 2; for (int i = 0; i < t; i++) n += h_na(i); return n; }
constexpr int hcnt(int t) { return 4 * h_na(t) + ((t >= 1 && t <= 5) ? 5 : 0) + (t == 8 ? 3 : 0); }
constexpr int hwait(int s) {
  int n = 0;
  if (s < 2) { n = 4 * (1 - s); for (int t = 0; t < s; t++) n += hcnt(t); return n; }
  int t0 = 0; while (h_a0(t0) + h_na(t0) <= s) t0++;
  n = hcnt(t0) - 4 * (s - h_a0(t0) + 1);
  for (int t = t0 + 1; t < s; t++) n += hcnt(t);
  return n;
}

typedef unsigned s8u __attribute__((ext_vector_type(8)));
__global__ __launch_bounds__(512, 2) void fused_kernel(const u16* x_, const u16* wqkv_, const u16* wo_, const u16* w1_, const u16* w2_, u16* out_, char* ws_) {
  extern __shared__ __attribute__((aligned(16))) char smem_[];
  LAS char* smem = (LAS char*)smem_;
  const unsigned lds0 = (unsigned)(size_t)smem;
  s8u ka; v4u kb; v2u kc;
  asm volatile("s_load_dwordx8 %0, %3, 0x0\n\ts_load_dwordx4 %1, %3, 0x20\n\ts_load_dwordx2 %2, %3, 0x30"
               : "=&s"(ka), "=&s"(kb), "=&s"(kc) : "s"(__builtin_amdgcn_kernarg_segment_ptr()));
  SB;
  const int tid = threadIdx.x, lane = tid & 63, w = __builtin_amdgcn_readfirstlane(tid >> 6);
  const int bid = blockIdx.x, xcd = bid & 7, idx = bid >> 3, half = xcd & 1, j8 = idx & 7;
  const int grp = (xcd >> 1) * 4 + (idx >> 3), mem = half * 8 + j8;
  const int isffn = j8 >> 2, sub = half * 4 + (j8 & 3);
  unsigned cpf;
  {
    u64 pc0; unsigned codelen;
    asm volatile("s_getpc_b64 %0\n.Lcode_begin_mark:\n\ts_mov_b32 %1, .Lcode_end_mark-.Lcode_begin_mark" : "=s"(pc0), "=s"(codelen));
    const v4i rp = mkrs((const void*)pc0, (int)codelen);
    asm volatile("buffer_load_dword %0, %1, %2, %3 offen" : "=v"(cpf) : "v"(lane < 2 ? lane * 128 : OOB), "s"(rp), "s"((idx * 8 + w) * 256));
  }
  P1 c;
  c.ldb2 = (3 * DM - isffn * (3 * DM - FF)) * 2;
  c.w = w; c.lane = lane; c.grp = grp; c.isffn = isffn; c.mem = mem;
  const int ftile0 = sub * 14 - half * (j8 & 3);
  const int ntile = 15 - isffn * (1 + half);
  {
    const int ar = lane >> 3, sw = ((lane & 7) ^ ((4 * w + (lane >> 4)) & 7)) << 4;
    c.avoff = (grp * 128 + 8 * w + ar) * (DM * 2) + sw;
    int hr = grp * 128 - 16 + 8 * w + ar; hr = hr < 0 ? 0 : hr;
    c.asvoff = hr * (DM * 2) + sw;
    const int kk = 2 * w + (lane >> 5), zz = (kk & 3) | (((kk >> 3) & 1) << 2), pc = lane & 31, seg = (pc >> 1) ^ zz;
    const int part = (seg * 13) >> 6;
    const int col = seg * 16 + (isffn ? ftile0 * 16 : sub * 80) + (isffn ? 0 : DM - 80) * part;
    const int bo = kk * c.ldb2 + col * 2 + (pc & 1) * 16;
    c.bvoff = seg < ntile ? bo : OOB;
  }
  c.halo = !isffn && w < 2;
  { const int cl = (j8 + grp + half) % NCLS; c.rot = cl * 9 / NCLS; }
  asm volatile("" : "+v"(c.avoff), "+v"(c.asvoff), "+v"(c.bvoff));
  asm volatile("" : "+s"(c.rot));
  asm volatile("s_waitcnt lgkmcnt(0)" : "+s"(ka), "+s"(kb), "+s"(kc));
  auto ptr = [](unsigned lo, unsigned hi) { return (char*)(((u64)hi << 32) | lo); };
  const u16* x = (const u16*)ptr(ka[0], ka[1]); const u16* wqkv = (const u16*)ptr(ka[2], ka[3]); const u16* wo = (const u16*)ptr(ka[4], ka[5]);
  const u16* w1 = (const u16*)ptr(ka[6], ka[7]); const u16* w2 = (const u16*)ptr(kb[0], kb[1]); u16* out = (u16*)ptr(kb[2], kb[3]);
  char* ws = ptr(kc[0], kc[1]);
  c.rsA = mkrs(x, SEQ * DM * 2);
  c.rsB = mkrs(isffn ? w1 : wqkv, DM * c.ldb2);
  static_for<6>([&](auto I) { p1_req<decltype(I)::value, true>(c, lds0, 0); });
  c.rsH = mkrs(ws, H_BYTES);
  c.rsC = mkrs(ws + CTRL_OFF, 1 << 20);
  const int cg = (xcd >> 1) * 4 + half * 2 + (idx >> 4), rh = (idx >> 3) & 1, c3 = idx & 7;
  c.bt = ws + BT_OFF; c.bid = bid;
  c.rsT = mkrs(ws + BT_OFF + (size_t)c3 * BT_SLICE, BT_SLICE);
  c.wm = w >> 2;
  c.k0 = w < 4 ? 5 * w : hk0(w & 3);
  c.k0t = hk0(w & 3) + 9 + 2 * (w >> 2);
  c.tag = 0; c.taghi = 0;
  const int wn = w < 4 ? w : 7 - w;
  c.htile0 = 40 + ftile0;
  const int tsc = w == 3 ? CS_OFF : 2 * ST_BYTES + A_BYTES + 8192 + (w - 4) * 5120;
  v4u xs[10];
  const int sslot = w * 8 + (lane >> 3);
  const int kap = (7 - w) * 16 + grp;
  const bool dostat = isffn && w < 3, dotile = isffn && w >= 3 && kap < 74;
  if (dostat) {
    int rho = sub * 18 + (sslot < 18 ? sslot : 17);
    int row = grp * 128 - 16 + rho; row = row < 0 ? 0 : row;
    const int vo = row * (DM * 2) + (lane & 7) * 16;
    static_for<10>([&](auto I) { constexpr int i = decltype(I)::value; xs[i] = ld128<128 * i>(c.rsA, vo, 0); });
  } else if (dotile) {
    const v4i rw = mkrs(kap < 20 ? wo : w2, kap < 20 ? DM * DM * 2 : DM * FF * 2);
    int kq = kap;
    if (kap >= 20) asm volatile("s_sub_u32 %0, %0, 20" : "+s"(kq)::"scc");
    const int so = kq * 32 * (DM * 2);
#pragma unroll
    for (int i = 0; i < 5; i++) {
      const int pc = 64 * i + lane, rr = pc / 10;
      dma16(rw, lds0 + tsc + i * 1024, rr * (DM * 2) + sub * 160 + (pc - 10 * rr) * 16, so);
    }
  }
  __builtin_amdgcn_s_sleep(SSLEEP);
  static_for<6>([&](auto I) { p1_req<decltype(I)::value>(c, lds0 + ST_BYTES, 1); });
  static_for<3>([&](auto I) { p1_req<decltype(I)::value>(c, lds0 + 2 * ST_BYTES, 2); });
  c.side = dostat || dotile; c.cpf = cpf;
  if (c.side) asm volatile("s_waitcnt vmcnt(9)" ::: "memory");
  if (w == 7) dma16_sc1(c.rsC, lds0 + EPL_OFF, lane == 0 ? EPOCH_OFF : OOB, 0);
  if (dostat) {
    asm volatile("" : "+v"(xs[0]), "+v"(xs[1]), "+v"(xs[2]), "+v"(xs[3]), "+v"(xs[4]), "+v"(xs[5]), "+v"(xs[6]), "+v"(xs[7]), "+v"(xs[8]), "+v"(xs[9])::"memory");
    float s0 = 0, s1 = 0, q0 = 0, q1 = 0;
#pragma unroll
    for (int i = 0; i < 40; i += 2) {
      const unsigned a = xs[i >> 2][i & 3], b = xs[i >> 2][(i & 3) + 1];
      s0 = dot2(a, 0x3F803F80u, s0); s1 = dot2(b, 0x3F803F80u, s1); q0 = dot2(a, a, q0); q1 = dot2(b, b, q1);
    }
    const float mean = row8_sum(s0 + s1) * (1.0f / DM);
    const float var = fmaxf(row8_sum(q0 + q1) * (1.0f / DM) - mean * mean, 0.f);
    const float sd = __builtin_sqrtf(rbf(rbf(var) + 1e-5f));
    v2u gr; gr.x = __builtin_bit_cast(unsigned, rbf(mean)); gr.y = __builtin_bit_cast(unsigned, rbf(sd));
    st64_sc1(c.rsC, ((lane & 7) == 0 && sslot < 18) ? STAT_OFF + grp * 2048 + (sub * 18 + sslot) * 8 : OOB, gr);
  } else if (dotile) {
    LAS char* tb = smem + tsc + ((lane >> 4) * 4 + ((lane & 15) >> 2)) * 160 + (lane & 3) * 8;
#pragma unroll
    for (int n = 0; n < 5; n++)
      st128_sc1(mkrs(ws + BT_OFF, 8 * BT_SLICE), lane * 16, sub * BT_SLICE + (kap * 5 + n) * 1024, __builtin_bit_cast(v4u, trfrag(tb + n * 32, 16 * 160)));
  }
  v4u B[5][5];
  {
    const int n4 = ntile - 12;
    c.tstart = wn <= n4 ? 4 * wn : 4 * n4 + 3 * (wn - n4);
    if (!isffn) c.ftile0 = 1 + 6 * c.wm;
    else { const int fi = (wn - n4) * 2 + c.wm; c.ftile0 = 1 + 2 * (n4 == 2 ? fi : (fi & 1)); }
    if (wn < n4) phase1<4, 1, 0>(c, smem, lds0, ws, B);
    else if (!isffn) phase1<3, 2, 3>(c, smem, lds0, ws, B);
    else phase1<3, 2, 1>(c, smem, lds0, ws, B);
  }
  const int r = lane & 15, g = lane >> 4;
  const int k0 = c.k0;
  auto ldB = [&](int s) { SB; ldB5(B[s], c.rsT, lane * 16, (k0 + s) * 5120); SB; };
  int foff;
  {
    int m;
    if (w < 4) m = lane < 2 ? (w >> 1) * 8 + (w & 1) * 2 + lane : -1;
    else {
      const int t0 = 2 * (k0 - 20), t1 = t0 + 27 - (w & 2);
      const int f0 = t0 < 56 ? t0 / 14 : 4 + (t0 - 56) / 13, f1 = t1 < 56 ? t1 / 14 : 4 + (t1 - 56) / 13, f = f0 + lane;
      m = f <= f1 ? (f >> 2) * 8 + 4 + (f & 3) : -1;
    }
    foff = m >= 0 ? FLAG_OFF + cg * 256 + m * 4 : OOB;
  }
  const int hso = ((cg * 4 + rh * 2) * NCT + 2 * k0) * 1024;
  v4u E[2][4];
  if (!isffn) {
    unsigned fl;
    const int row = (w & 1) * 64 + lane;
    const int pos = grp * 128 + row;
    LAS char* qp = smem + (row + 16) * IMG_LD + (w >> 1) * 40;
    unsigned qv[10];
#pragma unroll
    for (int i = 0; i < 5; i++) { const v2u t = *(LAS v2u*)(qp + i * 8); qv[2 * i] = t.x; qv[2 * i + 1] = t.y; }
    float lg[8];
    static_for<8>([&](auto K) {
      constexpr int k = decltype(K)::value;
      if constexpr (k < 5) B[3][k] = ldB1<k>(c.rsT, lane * 16, (k0 + 3) * 5120);
      LAS char* kp = qp + 160 - 2 * k * IMG_LD;
      float a0 = 0, a1 = 0;
#pragma unroll
      for (int i = 0; i < 5; i++) { const v2u t = *(LAS v2u*)(kp + i * 8); a0 = dot2(qv[2 * i], t.x, a0); a1 = dot2(qv[2 * i + 1], t.y, a1); }
      lg[k] = pos - 2 * k >= 0 ? a0 + a1 : -4e9f;
    });
    float mx = lg[0];
#pragma unroll
    for (int k = 1; k < 8; k++) mx = fmaxf(mx, lg[k]);
    float sum = 0;
    constexpr float C = 0.22360679774997896f * 1.4426950408889634f;
#pragma unroll
    for (int k = 0; k < 8; k++) { lg[k] = __builtin_amdgcn_exp2f((lg[k] - mx) * C); sum += lg[k]; }
    const float inv = __builtin_amdgcn_rcpf(sum);
    float o[20];
#pragma unroll
    for (int d = 0; d < 20; d++) o[d] = 0;
    static_for<8>([&](auto K) {
      constexpr int k = decltype(K)::value;
      if constexpr (k < 5) B[4][k] = ldB1<k>(c.rsT, lane * 16, (k0 + 4) * 5120);
      if constexpr (k == 5) fl = ld32_sc1(c.rsC, w >= 4 ? foff : OOB);
      const unsigned plo = pk2(lg[k] * inv, 0.f), phi = plo << 16;
      LAS char* vp = qp + 320 - 2 * k * IMG_LD;
#pragma unroll
      for (int i = 0; i < 5; i++) {
        const v2u t = *(LAS v2u*)(vp + i * 8);
        o[4 * i] = dot2(t.x, plo, o[4 * i]); o[4 * i + 1] = dot2(t.x, phi, o[4 * i + 1]);
        o[4 * i + 2] = dot2(t.y, plo, o[4 * i + 2]); o[4 * i + 3] = dot2(t.y, phi, o[4 * i + 3]);
      }
    });
#pragma unroll
    for (int i = 0; i < 5; i++) { v2u t; t.x = pk2(o[4 * i], o[4 * i + 1]); t.y = pk2(o[4 * i + 2], o[4 * i + 3]); *(LAS v2u*)(qp + i * 8) = t; }
    ldsbar();
#pragma unroll
    for (int i = 0; i < 3; i++) {
      const int b0 = w + 8 * i, b = b0 < 20 ? b0 : 19, rp = b / 5, t = b - 5 * rp;
      LAS char* sp = smem + (16 + rp * 32 + r) * IMG_LD + t * 32 + g * 8;
      const v2u lo = *(LAS v2u*)sp, hi = *(LAS v2u*)(sp + 16 * IMG_LD);
      v4u v; v.x = lo.x; v.y = lo.y; v.z = hi.x; v.w = hi.y;
      st128_sc1(c.rsH, lane * 16, b0 < 20 ? ((grp * 4 + rp) * NCT + sub * 5 + t) * 1024 : OOB, v);
    }
    if (w >= 4) {
      asm volatile("s_waitcnt vmcnt(3)" : "+v"(fl)::"memory");
      wait_flags(c, foff, fl);
      ldA4(E[0], c.rsH, lane * 16, hso); ldA4(E[1], c.rsH, lane * 16, hso + 2048);
    }
  }
  if (!isffn && w >= 4) waitbar<8>(); else waitbar<0>();
  st32_sc1(c.rsC, tid == 0 ? FLAG_OFF + grp * 256 + mem * 4 : OOB, c.tag);
  if (isffn && w < 4) {
    __builtin_amdgcn_s_sleep(FSLEEP);
    ldB(0); ldB(1); ldB(2); ldB(3); ldB(4);
    __builtin_amdgcn_s_sleep(FSLEEP2);
  }
  if (w < 4 && !isffn) __builtin_amdgcn_s_sleep(OSLEEP);
  if (isffn || w < 4) wait_flags(c, foff, ~c.tag);
  if (isffn && w >= 4) { ldA4(E[0], c.rsH, lane * 16, hso); ldA4(E[1], c.rsH, lane * 16, hso + 2048); }
  v2u xr[3];
  auto ldx = [&]() {
#pragma unroll
    for (int i = 0; i < 3; i++) {
      const int tl = w + 8 * i, tt = tl < 20 ? tl : 19, m = tt / 5, n = tt - 5 * m;
      xr[i] = ld64(c.rsA, (cg * 128 + rh * 64 + m * 16 + r) * (DM * 2) + (c3 * 80 + n * 16 + g * 4) * 2, 0);
    }
  };
  auto put = [&](const f32x4 (&acc)[4][5], int p0, int p1, int stride) {
    asm volatile("s_nop 7\n\ts_nop 3\n\ts_waitcnt vmcnt(0) lgkmcnt(0)" : "+v"(xr[0]), "+v"(xr[1]), "+v"(xr[2])::"memory");
#pragma unroll
    for (int m = 0; m < 4; m++)
#pragma unroll
      for (int n = 0; n < 5; n++) *(LAS f32x4*)(smem + (m < 2 ? p0 : p1) + ((m & 1) * 5 + n) * stride + lane * 16) = acc[m][n];
  };
  const int vo = lane * 16;
  if (w < 4) {
    v4u A[5][4];
    auto ldA = [&](int s) { ldA4(A[s], c.rsH, vo, hso + s * 2048); };
    ldA(0); ldA(1); ldA(2);
    f32x4 acc[4][5];
#pragma unroll
    for (int m = 0; m < 4; m++)
#pragma unroll
      for (int n = 0; n < 5; n++) acc[m][n] = f32x4{0, 0, 0, 0};
#pragma unroll
    for (int m = 0; m < 4; m++) TIE5(acc[m]);
#pragma unroll
    for (int s = 0; s < 5; s++) TIE5(B[s]);
    WAITA(A[0], 8); swap_pair(A[0][0], A[0][1]); swap_pair(A[0][2], A[0][3]);
    asm volatile("s_nop 1");
    static_for<5>([&](auto S) {
      constexpr int s = decltype(S)::value;
      static_for<20>([&](auto I) {
        constexpr int i = decltype(I)::value;
        mfma_ip(acc[i / 5][i % 5], B[s][i % 5], A[s][i / 5]);
        if constexpr ((s == 1 || s == 2) && i < 8 && (i & 1)) A[s + 2][i >> 1] = ldA1<(i >> 1)>(c.rsH, vo, hso + (s + 2) * 2048);
        if constexpr (s == 2 && i == 9) ldx();
        if constexpr (s < 4 && i == 13) WAITA(A[s + 1], (s == 0 ? 4 : s == 1 ? 4 : s == 2 ? 7 : 3));
        if constexpr (s < 4 && i == 14) swap_pair(A[s + 1][0], A[s + 1][1]);
        if constexpr (s < 4 && i == 15) swap_pair(A[s + 1][2], A[s + 1][3]);
      });
    });
    put(acc, w * 20480, w < 3 ? w * 20480 + 10240 : CS_OFF, 1024);
#pragma unroll
    for (int s = 0; s < 5; s++)
#pragma unroll
      for (int n = 0; n < 5; n++) asm volatile("" : "=v"(B[s][n]));
#pragma unroll
    for (int i = 0; i < 8; i++) asm volatile("" : "=v"(E[i >> 2][i & 3]));
  }
  int w_h = w;
  asm volatile("" : "+s"(w_h));
  if (w_h >= 4) {
    const int hw = w & 3;
    v4u A[14][4], Bs[14][5];
    const int last = hw < 2 ? 0 : OOB;
#pragma unroll
    for (int i = 0; i < 8; i++) A[i >> 2][i & 3] = E[i >> 2][i & 3];
    f32x4 acc[4][5];
#pragma unroll
    for (int m = 0; m < 4; m++)
#pragma unroll
      for (int n = 0; n < 5; n++) acc[m][n] = f32x4{0, 0, 0, 0};
#pragma unroll
    for (int m = 0; m < 4; m++) TIE5(acc[m]);
    LAS char* eb = smem + RING_OFF + hw * 1024 + lane * 16;
#pragma unroll
    for (int s = 0; s < 5; s++) {
      TIE5(B[s]);
#pragma unroll
      for (int n = 0; n < 5; n++) Bs[s][n] = B[s][n];
    }
    WAITA(A[0], 4); swap_pair(A[0][0], A[0][1]); swap_pair(A[0][2], A[0][3]);
    asm volatile("s_nop 1");
    static_for<14>([&](auto S) {
      constexpr int s = decltype(S)::value;
      static_for<20>([&](auto I) {
        constexpr int i = decltype(I)::value;
        mfma_ip(acc[i / 5][i % 5], Bs[s][i % 5], A[s][i / 5]);
        if constexpr (i < 4 * h_na(s)) {
          constexpr int k = h_a0(s) + (i >> 2);
          A[k][i & 3] = ldA1<(i & 3)>(c.rsH, k == 9 ? (vo | last) : vo, hso + h_k(k) * 2048);
        }
        if constexpr (s >= 1 && s <= 5 && i >= 4 && i < 9) Bs[s + 4][i - 4] = ldB1<i - 4>(c.rsT, s == 5 ? (vo | last) : vo, (k0 + h_k(s + 4)) * 5120);
        if constexpr (s == 8 && i == 9) ldx();
        if constexpr (s >= 9 && s < 13 && i >= 9 && i < 14) {
          SB; Bs[s + 1][i - 9] = *(LAS v4u*)(eb + (((s - 9) & 1) * 5 + i - 9) * 8192 + ((s - 9) >> 1) * 4096); SB;
        }
        if constexpr (s < 13 && i == 14) {
          asm volatile("s_waitcnt vmcnt(%4)" : "+v"(A[s + 1][0]), "+v"(A[s + 1][1]), "+v"(A[s + 1][2]), "+v"(A[s + 1][3]) : "i"(hwait(s + 1)) : "memory");
          if constexpr (s + 1 >= 5 && s + 1 <= 9) TIE5(Bs[s + 1]);
        }
        if constexpr (s < 13 && i == 15) swap_pair(A[s + 1][0], A[s + 1][1]);
        if constexpr (s < 13 && i == 16) swap_pair(A[s + 1][2], A[s + 1][3]);
      });
    });
    put(acc, RING_OFF + hw * 1024, RING_OFF + hw * 1024 + 4096, 8192);
  }
  ldsbar();
  if (w == 0 && bid == 0) { v2u t; t.x = c.tag; t.y = c.taghi; st64_sc1(c.rsC, lane == 0 ? EPOCH_OFF : OOB, t); }
#pragma unroll
  for (int i = 0; i < 3; i++) {
    const int tl = w + 8 * i;
    if (tl < 20) {
      const int m = tl / 5, n = tl - 5 * m;
      LAS char* ps = smem + tl * 1024 + lane * 16;
      LAS char* pf = smem + RING_OFF + (tl < 10 ? tl * 8192 : (tl - 10) * 8192 + 4096) + lane * 16;
      const f32x4 s = (*(LAS f32x4*)ps + *(LAS f32x4*)(ps + 20480)) + (*(LAS f32x4*)(ps + 40960) + *(LAS f32x4*)(ps + (tl < 10 ? 61440 : CS_OFF - 10240)));
      const f32x4 f = (*(LAS f32x4*)pf + *(LAS f32x4*)(pf + 1024)) + (*(LAS f32x4*)(pf + 2048) + *(LAS f32x4*)(pf + 3072));
      float y[4];
#pragma unroll
      for (int j = 0; j < 4; j++) {
        const float xv = (j & 1) ? bfhi(xr[i][j >> 1]) : bflo(xr[i][j >> 1]);
        y[j] = xv + s[j] * silu_f(f[j]);
      }
      v2u o; o.x = pk2(y[0], y[1]); o.y = pk2(y[2], y[3]);
      *(GAS v2u*)((GAS char*)out + (size_t)(cg * 128 + rh * 64 + m * 16 + r) * (DM * 2) + (c3 * 80 + n * 16 + g * 4) * 2) = o;
    }
  }
  asm volatile(".Lcode_end_mark:");
}
}

extern "C" void kernel_launch(void* const* d_in, const int* in_sizes, int n_in, void* d_out, int out_size, void* d_ws, size_t ws_size,
                              hipStream_t stream) {
  (void)hipFuncSetAttribute((const void*)fused_kernel, hipFuncAttributeMaxDynamicSharedMemorySize, LDS_BYTES);
  fused_kernel<<<256, 512, LDS_BYTES, stream>>>((const u16*)d_in[0], (const u16*)d_in[1], (const u16*)d_in[2], (const u16*)d_in[4],
                                                (const u16*)d_in[5], (u16*)d_out, (char*)d_ws);
}
```

```cpp
#include <hip/hip_runtime.h>
#include <stdint.h>
#include <type_traits>
#include <utility>

#define GAS __attribute__((address_space(1)))
#define LAS __attribute__((address_space(3)))
typedef unsigned short u16;
typedef unsigned long long u64;
typedef __bf16 v2bf __attribute__((ext_vector_type(2)));
typedef __bf16 v4bf __attribute__((ext_vector_type(4)));
typedef __bf16 v8bf __attribute__((ext_vector_type(8)));
typedef float f32x2 __attribute__((ext_vector_type(2)));
typedef float f32x4 __attribute__((ext_vector_type(4)));
typedef unsigned v4u __attribute__((ext_vector_type(4)));
typedef unsigned v2u __attribute__((ext_vector_type(2)));
typedef int v4i __attribute__((ext_vector_type(4)));

namespace {
constexpr int SEQ = 2048, DM = 640, FF = 1728;
constexpr int NCT = 148;
constexpr int H_BYTES = 64 * NCT * 1024;
constexpr size_t CTRL_OFF = 16u << 20;
constexpr int EPOCH_OFF = 0, RDY_OFF = 2048, FLAG_OFF = 8192, STAT_OFF = 65536;
constexpr size_t BT_OFF = 12u << 20;
constexpr int BT_SLICE = 74 * 5120;
constexpr int A_BYTES = 144 * 128, B_BYTES = 64 * 512, ST_BYTES = A_BYTES + B_BYTES;
constexpr int CS_OFF = 3 * ST_BYTES, RS_OFF = CS_OFF + 1024;
constexpr int RDYL_OFF = CS_OFF + 6144, EPL_OFF = CS_OFF + 7168;
constexpr int DUMP_OFF = CS_OFF + 4096;
constexpr int IMG_LD = 488;
constexpr int RING_OFF = 71680;
constexpr int LDS_BYTES = 163840;
constexpr int OOB = 0x7FF00000;
constexpr unsigned SPIN_CAP = 1u << 16;
#define NCLS 3
#define PFMASK 0x42
#define OSLEEP 30
#define FSLEEP2 70
#define SSLEEP 16
#define POLL_SLEEP 8
#define SB __builtin_amdgcn_sched_barrier(0)

__device__ __forceinline__ v4i mkrs(const void* p, int bytes) {
  u64 a = (u64)p; v4i r; r.x = (int)(unsigned)a; r.y = (int)((unsigned)(a >> 32) & 0xFFFFu); r.z = bytes; r.w = 0x00020000; return r;
}
__device__ __forceinline__ void dma16(v4i rs, unsigned lds, int voff, int soff) {
  asm volatile("s_mov_b32 m0, %0\n\ts_nop 0\n\tbuffer_load_dwordx4 %1, %2, %3 offen lds" ::"s"(lds), "v"(voff), "s"(rs), "s"(soff));
}
__device__ __forceinline__ void dma16_sc1(v4i rs, unsigned lds, int voff, int soff) {
  asm volatile("s_mov_b32 m0, %0\n\ts_nop 3\n\tbuffer_load_dwordx4 %1, %2, %3 offen sc1 lds" ::"s"(lds), "v"(voff), "s"(rs), "s"(soff));
}
template <int IMM> __device__ __forceinline__ v4u ld128_sc1(v4i rs, int voff, int soff) {
  v4u r; asm volatile("buffer_load_dwordx4 %0, %1, %2, %3 offen offset:%4 sc1" : "=v"(r) : "v"(voff), "s"(rs), "s"(soff), "i"(IMM)); return r;
}
template <int IMM> __device__ __forceinline__ v4u ld128(v4i rs, int voff, int soff) {
  v4u r; asm volatile("buffer_load_dwordx4 %0, %1, %2, %3 offen offset:%4" : "=v"(r) : "v"(voff), "s"(rs), "s"(soff), "i"(IMM)); return r;
}
__device__ __forceinline__ v2u ld64(v4i rs, int voff, int soff) {
  v2u r; asm volatile("buffer_load_dwordx2 %0, %1, %2, %3 offen" : "=v"(r) : "v"(voff), "s"(rs), "s"(soff)); return r;
}
__device__ __forceinline__ unsigned ld32_sc1(v4i rs, int voff) {
  unsigned r; asm volatile("buffer_load_dword %0, %1, %2, 0 offen sc1" : "=v"(r) : "v"(voff), "s"(rs)); return r;
}
__device__ __forceinline__ void st128_sc1(v4i rs, int voff, int soff, v4u v) {
  asm volatile("buffer_store_dwordx4 %0, %1, %2, %3 offen sc1\n\ts_nop 1" ::"v"(v), "v"(voff), "s"(rs), "s"(soff) : "memory");
}
__device__ __forceinline__ void st64_sc1(v4i rs, int voff, v2u v) {
  asm volatile("buffer_store_dwordx2 %0, %1, %2, 0 offen sc1" ::"v"(v), "v"(voff), "s"(rs) : "memory");
}
__device__ __forceinline__ void st32_sc1(v4i rs, int voff, unsigned v) {
  asm volatile("buffer_store_dword %0, %1, %2, 0 offen sc1" ::"v"(v), "v"(voff), "s"(rs) : "memory");
}
template <int N> __device__ __forceinline__ void waitbar() { asm volatile("s_waitcnt vmcnt(%0) lgkmcnt(0)\n\ts_barrier" ::"i"(N) : "memory"); }
__device__ __forceinline__ void ldsbar() { asm volatile("s_waitcnt lgkmcnt(0)\n\ts_barrier" ::: "memory"); }
__device__ __forceinline__ float bflo(unsigned u) { return __builtin_bit_cast(float, u << 16); }
__device__ __forceinline__ float bfhi(unsigned u) { return __builtin_bit_cast(float, u & 0xFFFF0000u); }
__device__ __forceinline__ unsigned pk2(float a, float b) {
  v2bf v; v.x = (__bf16)a; v.y = (__bf16)b; return __builtin_bit_cast(unsigned, v);
}
__device__ __forceinline__ float rbf(float a) { return (float)(__bf16)a; }
__device__ __forceinline__ float dot2(unsigned a, unsigned b, float c) {
  return __builtin_amdgcn_fdot2_f32_bf16(__builtin_bit_cast(v2bf, a), __builtin_bit_cast(v2bf, b), c, false);
}
__device__ __forceinline__ f32x4 mfma(v8bf a, v8bf b, f32x4 c) { return __builtin_amdgcn_mfma_f32_16x16x32_bf16(a, b, c, 0, 0, 0); }
__device__ __forceinline__ v8bf trfrag(LAS char* p, int second) {
  v4bf lo = __builtin_amdgcn_ds_read_tr16_b64_v4bf16((LAS v4bf*)p);
  v4bf hi = __builtin_amdgcn_ds_read_tr16_b64_v4bf16((LAS v4bf*)(p + second));
  return __builtin_shufflevector(lo, hi, 0, 1, 2, 3, 4, 5, 6, 7);
}
__device__ __forceinline__ float row8_sum(float v) {
  v += __builtin_bit_cast(float, __builtin_amdgcn_update_dpp(0, __builtin_bit_cast(int, v), 0xB1, 0xF, 0xF, false));
  v += __builtin_bit_cast(float, __builtin_amdgcn_update_dpp(0, __builtin_bit_cast(int, v), 0x4E, 0xF, 0xF, false));
  v += __builtin_bit_cast(float, __builtin_amdgcn_update_dpp(0, __builtin_bit_cast(int, v), 0x141, 0xF, 0xF, false));
  return v;
}
__device__ __forceinline__ float silu_f(float y) { return y * __builtin_amdgcn_rcpf(1.0f + __builtin_amdgcn_exp2f(y * -1.4426950408889634f)); }
template <class F, int... I> __device__ __forceinline__ void static_for_impl(F&& f, std::integer_sequence<int, I...>) { (f(std::integral_constant<int, I>{}), ...); }
template <int N, class F> __device__ __forceinline__ void static_for(F&& f) { static_for_impl(f, std::make_integer_sequence<int, N>{}); }

struct P1 {
  v4i rsA, rsB, rsH, rsC, rsT;
  int avoff, asvoff, bvoff, ldb2;
  int pfso, bid;
  int w, wm, lane, grp, mem, rot;
  const char* bt;
  int tstart, ftile0;
  int isffn, htile0;
  unsigned tag, taghi;
  int k0, k0t, halo, side;
  unsigned cpf;
};
__device__ __forceinline__ int hk0(int hw) { return 20 + 14 * hw - ((hw + 1) >> 2); }

template <int I, bool FIRST = false> __device__ __forceinline__ void p1_req(const P1& c, unsigned base, int st) {
  if (!FIRST) {
    st += c.rot;
    int t; asm("s_cmp_ge_i32 %0, 10\n\ts_cselect_b32 %1, 9, 0\n\ts_sub_i32 %0, %0, %1" : "+s"(st), "=&s"(t)::"scc");
  }
  if (I == 0 && c.halo) dma16(c.rsA, base + c.w * 1024, c.asvoff, st * 128);
  if (I < 2) dma16(c.rsA, base + (2 + c.w + 8 * I) * 1024, c.avoff, st * 128 + I * 64 * (DM * 2));
  else dma16(c.rsB, base + A_BYTES + (c.w + 8 * (I - 2)) * 1024, c.bvoff, (st * 64 + (I - 2) * 16) * c.ldb2);
}
template <int E> __device__ __forceinline__ void tail_req(const P1& c, unsigned lds0) {
  dma16_sc1(c.rsT, lds0 + RING_OFF + E * 8192 + c.w * 1024, c.lane * 16, c.k0t * 5120 + E * 1024);
}

__device__ __forceinline__ void ldB5(v4u (&B)[5], v4i rsT, int voff, int so) {
  const int so2 = so == OOB ? OOB : so + 4096;
  B[0] = ld128_sc1<0>(rsT, voff, so); B[1] = ld128_sc1<1024>(rsT, voff, so); B[2] = ld128_sc1<2048>(rsT, voff, so);
  B[3] = ld128_sc1<3072>(rsT, voff, so); B[4] = ld128_sc1<0>(rsT, voff, so2);
}
#define TIE5(B) asm volatile("" : "+v"((B)[0]), "+v"((B)[1]), "+v"((B)[2]), "+v"((B)[3]), "+v"((B)[4]))

template <int I> __device__ __forceinline__ v4u ldB1(v4i rsT, int voff, int so) {
  return ld128_sc1<(I & 3) * 1024>(rsT, voff, I < 4 ? so : so + 4096);
}
template <int NT, int NOWN, int NFOR>
__device__ __forceinline__ void phase1(P1& c, LAS char* smem, unsigned lds0, char* ws, v4u (&B)[5][5]) {
  constexpr int NB = NT + NFOR, NX = NOWN + NFOR;
  const int lane = c.lane, r = lane & 15, g = lane >> 4, q = r >> 2, p = lane & 3;
  int aad[2];
#pragma unroll
  for (int ks = 0; ks < 2; ks++) aad[ks] = r * 128 + (((4 * ks + g) ^ (r >> 1)) << 4);
  const int amain = (1 + c.wm * 4) * 2048;
  const int z = q | ((g & 1) << 2);
  int tile[NB], bad[NB];
#pragma unroll
  for (int n = 0; n < NB; n++) {
    if (n < NT) { int t = n + 2 * c.wm; t = t >= NT ? t - NT : t; tile[n] = c.tstart + t; }
    else tile[n] = c.ftile0 + 2 * (n - NT);
    bad[n] = A_BYTES + (8 * g + q) * 512 + (((tile[n] & 7) ^ z) << 5) + (tile[n] >> 3) * 256 + p * 8;
  }
  f32x4 acc[4][NT], accx[NX];
#pragma unroll
  for (int m = 0; m < 4; m++)
#pragma unroll
    for (int n = 0; n < NT; n++) acc[m][n] = f32x4{0, 0, 0, 0};
#pragma unroll
  for (int n = 0; n < NX; n++) accx[n] = f32x4{0, 0, 0, 0};
  v8bf af[2][4], as[2], bf[2][NB];
  const bool ones = (r == 0) || c.isffn;
  auto ldq = [&](int buf, LAS char* base, int ks, int i) {
    af[buf][i] = *(LAS v8bf*)(base + aad[ks] + amain + i * 2048);
#pragma unroll
    for (int n = 0; n < NB; n++) if ((n & 3) == i) bf[buf][n] = trfrag(base + bad[n] + ks * 16384, 2048);
    if (i == 3) as[buf] = *(LAS v8bf*)(base + aad[ks]);
  };
  auto row = [&](int buf, int m) {
#pragma unroll
    for (int n = 0; n < NT; n++) acc[m][n] = mfma(bf[buf][n], af[buf][m], acc[m][n]);
    if (m == 3) {
      v4u s = __builtin_bit_cast(v4u, as[buf]);
#pragma unroll
      for (int e = 0; e < 4; e++) s[e] = ones ? 0x3F803F80u : s[e];
      v8bf sv = __builtin_bit_cast(v8bf, s);
#pragma unroll
      for (int n = 0; n < NX; n++) accx[n] = mfma(bf[buf][n < NOWN ? n : NT + n - NOWN], sv, accx[n]);
    }
#pragma unroll
    for (int i = 0; i < NT; i++) {
      __builtin_amdgcn_sched_group_barrier(0x8, 1, 0); __builtin_amdgcn_sched_group_barrier(0x100, 2, 0); __builtin_amdgcn_sched_group_barrier(0x2, 1, 0);
    }
    SB;
  };
  auto halfstage = [&](int buf, bool rd, LAS char* rbase, int rks, auto&& rq0, auto&& rq1, auto&& rq2) {
    if (rd) { ldq(buf ^ 1, rbase, rks, 0); ldq(buf ^ 1, rbase, rks, 1); }
    row(buf, 0); rq0(); SB;
    if (rd) ldq(buf ^ 1, rbase, rks, 2);
    row(buf, 1); rq1(); SB;
    if (rd) ldq(buf ^ 1, rbase, rks, 3);
    row(buf, 2); rq2(); SB;
    row(buf, 3);
  };
  auto none = [&]() {};
#pragma unroll
  for (int m = 0; m < 4; m++)
#pragma unroll
    for (int n = 0; n < NT; n++) asm volatile("" : "+v"(acc[m][n]));
#pragma unroll
  for (int n = 0; n < NX; n++) asm volatile("" : "+v"(accx[n]));
#pragma unroll
  for (int n = 0; n < NB; n++) asm volatile("" : "+v"(bad[n]));
  asm volatile("" : "+v"(aad[0]), "+v"(aad[1]));
  if (c.side) ldsbar(); else waitbar<9>();
  asm volatile("" ::"v"(c.cpf));

  auto special = [&](int st) {
    if (st == 1) {
      const v2u ep = *(LAS v2u*)(smem + EPL_OFF);
      const u64 e = ((u64)(unsigned)__builtin_amdgcn_readfirstlane(ep.y) << 32) | (unsigned)__builtin_amdgcn_readfirstlane(ep.x);
      const u64 t = e * 0x5851F42D4C957F2Dull + 0x14057B7EF767814Full;
      c.tag = (unsigned)t; c.taghi = (unsigned)(t >> 32);
      if (c.w == 0) st32_sc1(c.rsC, lane == 0 ? RDY_OFF + c.bid * 4 : OOB, c.tag);
    } else if (st == 6) {
      if (c.w == 0) dma16_sc1(c.rsC, lds0 + RDYL_OFF, lane * 16, RDY_OFF);
    } else if (st == 7) {
      v4u rdy = *(LAS v4u*)(smem + RDYL_OFF + lane * 16);
      for (unsigned spins = 0;; spins++) {
        if (__all(rdy.x == c.tag && rdy.y == c.tag && rdy.z == c.tag && rdy.w == c.tag) || spins > SPIN_CAP) break;
        __builtin_amdgcn_s_sleep(POLL_SLEEP);
        asm volatile("buffer_load_dwordx4 %0, %1, %2, 0 offen offset:%3 sc1\n\ts_waitcnt vmcnt(0)" : "=&v"(rdy) : "v"(lane * 16), "s"(c.rsC), "i"(RDY_OFF) : "memory");
      }
      if (c.w < 2) dma16_sc1(c.rsC, lds0 + RS_OFF + c.w * 1024, lane * 16, STAT_OFF + c.grp * 2048 + c.w * 1024);
    }
  };
#pragma unroll
  for (int i = 0; i < 4; i++) ldq(0, smem, 0, i);
  int so = 0, po = 2 * ST_BYTES;
#pragma nounroll
  for (int st = 0; st < 7; st++) {
    const int no = so == 2 * ST_BYTES ? 0 : so + ST_BYTES;
    halfstage(0, true, smem + so, 1, [&]() { p1_req<3>(c, lds0 + po, st + 2); }, [&]() { p1_req<4>(c, lds0 + po, st + 2); }, [&]() { p1_req<5>(c, lds0 + po, st + 2); });
    waitbar<6>();
    SB;
    if ((PFMASK >> st) & 1) special(st);
    SB;
    halfstage(1, true, smem + no, 0, [&]() { p1_req<0>(c, lds0 + so, st + 3); }, [&]() { p1_req<1>(c, lds0 + so, st + 3); }, [&]() { p1_req<2>(c, lds0 + so, st + 3); });
    po = so; so = no;
  }
  halfstage(0, true, smem + ST_BYTES, 1, [&]() { p1_req<3>(c, lds0, 9); }, [&]() { p1_req<4>(c, lds0, 9); }, [&]() { p1_req<5>(c, lds0, 9); });
  waitbar<6>(); SB;
  special(7); SB;
  halfstage(1, true, smem + 2 * ST_BYTES, 0, [&]() { tail_req<0>(c, lds0); }, [&]() { tail_req<1>(c, lds0); }, [&]() { tail_req<2>(c, lds0); });
  halfstage(0, true, smem + 2 * ST_BYTES, 1, none, none, none);
  waitbar<3>(); SB;
  halfstage(1, true, smem, 0, [&]() { tail_req<3>(c, lds0); }, [&]() { tail_req<4>(c, lds0); }, [&]() { tail_req<5>(c, lds0); });
  halfstage(0, true, smem, 1, [&]() { tail_req<6>(c, lds0); }, [&]() { tail_req<7>(c, lds0); }, [&]() { tail_req<8>(c, lds0); });
  halfstage(1, false, smem, 0, [&]() { tail_req<9>(c, lds0); }, none, none);

#pragma unroll
  for (int n = 0; n < NX; n++)
    *(LAS f32x4*)(smem + (r == 0 ? CS_OFF + (tile[n < NOWN ? n : NT + n - NOWN] * 16 + 4 * g) * 4 : DUMP_OFF + lane * 16)) = accx[n];
  ldsbar();
  f32x2 rs[4];
#pragma unroll
  for (int m = 0; m < 4; m++) rs[m] = *(LAS f32x2*)(smem + RS_OFF + (16 + c.wm * 64 + m * 16 + r) * 8);
  auto ldB = [&](int s) { SB; ldB5(B[s], c.rsT, lane * 16, (c.k0 + s) * 5120); SB; };
  if (c.isffn) {
    if (c.w >= 4) { ldB(0); ldB(1); ldB(2); ldB(3); ldB(4); }
    float c1[4];
#pragma unroll
    for (int m = 0; m < 4; m++) c1[m] = __builtin_amdgcn_rcpf(rs[m].y) * -1.4426950408889634f;
#pragma unroll
    for (int n = 0; n < NT; n++) {
      const f32x4 cs = *(LAS f32x4*)(smem + CS_OFF + (tile[n] * 16 + 4 * g) * 4);
#pragma unroll
      for (int mp = 0; mp < 2; mp++) {
        v4u o;
#pragma unroll
        for (int h = 0; h < 2; h++) {
          const int m = 2 * mp + h;
          const f32x4 t = acc[m][n] - rs[m].x * cs;
          const f32x4 zz = t * c1[m];
          f32x4 e;
#pragma unroll
          for (int j = 0; j < 4; j++) e[j] = __builtin_amdgcn_exp2f(zz[j]);
          const f32x4 d = e * rs[m].y + rs[m].y;
          f32x4 rr;
#pragma unroll
          for (int j = 0; j < 4; j++) rr[j] = __builtin_amdgcn_rcpf(d[j]);
          const f32x4 y = t * rr;
          o[2 * h] = pk2(y[0], y[1]); o[2 * h + 1] = pk2(y[2], y[3]);
        }
        st128_sc1(c.rsH, lane * 16, ((c.grp * 4 + c.wm * 2 + mp) * NCT + c.htile0 + tile[n]) * 1024, o);
      }
    }
  } else {
    float ri[4];
#pragma unroll
    for (int m = 0; m < 4; m++) ri[m] = __builtin_amdgcn_rcpf(rs[m].y);
    f32x4 cs[NB];
#pragma unroll
    for (int n = 0; n < NB; n++) cs[n] = *(LAS f32x4*)(smem + CS_OFF + (tile[n] * 16 + 4 * g) * 4);
    const f32x2 rh = *(LAS f32x2*)(smem + RS_OFF + r * 8);
    const float rih = __builtin_amdgcn_rcpf(rh.y);
    SB;
    static_for<4 * NT + NX>([&](auto U) {
      constexpr int u = decltype(U)::value;
      if constexpr (u < 15) B[u / 5][u % 5] = ldB1<u % 5>(c.rsT, lane * 16, (c.k0 + u / 5) * 5120);
      if constexpr (u < 4 * NT) {
        constexpr int n = u / 4, m = u % 4;
        const f32x4 y = (acc[m][n] - rs[m].x * cs[n]) * ri[m];
        v2u o; o.x = pk2(y[0], y[1]); o.y = pk2(y[2], y[3]);
        *(LAS v2u*)(smem + (16 + c.wm * 64 + m * 16 + r) * IMG_LD + tile[n] * 32 + g * 8) = o;
      } else {
        constexpr int x = u - 4 * NT, n = x < NOWN ? x : NT + x - NOWN;
        const f32x4 y = (accx[x] - rh.x * cs[n]) * rih;
        v2u o; o.x = pk2(y[0], y[1]); o.y = pk2(y[2], y[3]);
        *(LAS v2u*)(smem + r * IMG_LD + tile[n] * 32 + g * 8) = o;
      }
      SB;
    });
    ldsbar();
  }
}

__device__ __forceinline__ void wait_flags(const P1& c, int off, unsigned f) {
  for (unsigned spins = 0;; spins++) {
    if (__all(off == OOB || f == c.tag) || spins > SPIN_CAP) break;
    if (spins) __builtin_amdgcn_s_sleep(POLL_SLEEP);
    asm volatile("buffer_load_dword %0, %1, %2, 0 offen sc1\n\ts_waitcnt vmcnt(0)" : "=&v"(f) : "v"(off), "s"(c.rsC) : "memory");
  }
}
__device__ __forceinline__ void mfma_ip(f32x4& acc, const v4u& b, const v4u& a) {
  asm volatile("v_mfma_f32_16x16x32_bf16 %0, %1, %2, %0" : "+v"(acc) : "v"(b), "v"(a));
}
__device__ __forceinline__ void swap_pair(v4u& l0, v4u& l1) {
  unsigned a = l0.z, b = l1.x, c = l0.w, d = l1.y;
  asm volatile("v_swap_b32 %0, %1" : "+v"(a), "+v"(b));
  l0.z = a; l1.x = b;
  asm volatile("v_swap_b32 %0, %1" : "+v"(c), "+v"(d));
  l0.w = c; l1.y = d;
}
template <int I> __device__ __forceinline__ v4u ldA1(v4i rsH, int voff, int so) {
  return ld128_sc1<(I & 1) * 1024>(rsH, voff, I < 2 ? so : so + NCT * 1024);
}
__device__ __forceinline__ void ldA4(v4u (&A)[4], v4i rsH, int voff, int so) {
  const int so2 = so == OOB ? OOB : so + NCT * 1024;
  A[0] = ld128_sc1<0>(rsH, voff, so); A[1] = ld128_sc1<1024>(rsH, voff, so);
  A[2] = ld128_sc1<0>(rsH, voff, so2); A[3] = ld128_sc1<1024>(rsH, voff, so2);
}
#define WAITA(A, N) asm volatile("s_waitcnt vmcnt(%4) lgkmcnt(0)" : "+v"((A)[0]), "+v"((A)[1]), "+v"((A)[2]), "+v"((A)[3]) : "i"(N) : "memory")
#define ADEEP 6
constexpr int h_k(int s) { return s < 9 ? s : (s == 9 ? 13 : s - 1); }
constexpr int h_na(int t) { return t == ADEEP ? 2 : (t < ADEEP ? 1 : (t + 3 < 14 ? 1 : 0)); }
constexpr int h_a0(int t) { int n = 2; for (int i = 0; i < t; i++) n += h_na(i); return n; }
constexpr int hcnt(int t) { return 4 * h_na(t) + ((t >= 1 && t <= 5) ? 5 : 0) + (t == 8 ? 3 : 0); }
constexpr int hwait(int s) {
  int n = 0;
  if (s < 2) { n = 4 * (1 - s); for (int t = 0; t < s; t++) n += hcnt(t); return n; }
  int t0 = 0; while (h_a0(t0) + h_na(t0) <= s) t0++;
  n = hcnt(t0) - 4 * (s - h_a0(t0) + 1);
  for (int t = t0 + 1; t < s; t++) n += hcnt(t);
  return n;
}

typedef unsigned s8u __attribute__((ext_vector_type(8)));
__global__ __launch_bounds__(512, 2) void fused_kernel(const u16* x_, const u16* wqkv_, const u16* wo_, const u16* w1_, const u16* w2_, u16* out_, char* ws_) {
  extern __shared__ __attribute__((aligned(16))) char smem_[];
  LAS char* smem = (LAS char*)smem_;
  const unsigned lds0 = (unsigned)(size_t)smem;
  s8u ka; v4u kb; v2u kc;
  asm volatile("s_load_dwordx8 %0, %3, 0x0\n\ts_load_dwordx4 %1, %3, 0x20\n\ts_load_dwordx2 %2, %3, 0x30"
               : "=&s"(ka), "=&s"(kb), "=&s"(kc) : "s"(__builtin_amdgcn_kernarg_segment_ptr()));
  SB;
  const int tid = threadIdx.x, lane = tid & 63, w = __builtin_amdgcn_readfirstlane(tid >> 6);
  const int bid = blockIdx.x, xcd = bid & 7, idx = bid >> 3, half = xcd & 1, j8 = idx & 7;
  const int grp = (xcd >> 1) * 4 + (idx >> 3), mem = half * 8 + j8;
  const int isffn = j8 >> 2, sub = half * 4 + (j8 & 3);
  unsigned cpf;
  {
    u64 pc0; unsigned codelen;
    asm volatile("s_getpc_b64 %0\n.Lcode_begin_mark:\n\ts_mov_b32 %1, .Lcode_end_mark-.Lcode_begin_mark" : "=s"(pc0), "=s"(codelen));
    const v4i rp = mkrs((const void*)pc0, (int)codelen);
    asm volatile("buffer_load_dword %0, %1, %2, %3 offen" : "=v"(cpf) : "v"(lane < 2 ? lane * 128 : OOB), "s"(rp), "s"((idx * 8 + w) * 256));
  }
  P1 c;
  c.ldb2 = (3 * DM - isffn * (3 * DM - FF)) * 2;
  c.w = w; c.lane = lane; c.grp = grp; c.isffn = isffn; c.mem = mem;
  const int ftile0 = sub * 14 - half * (j8 & 3);
  const int ntile = 15 - isffn * (1 + half);
  {
    const int ar = lane >> 3, sw = ((lane & 7) ^ ((4 * w + (lane >> 4)) & 7)) << 4;
    c.avoff = (grp * 128 + 8 * w + ar) * (DM * 2) + sw;
    int hr = grp * 128 - 16 + 8 * w + ar; hr = hr < 0 ? 0 : hr;
    c.asvoff = hr * (DM * 2) + sw;
    const int kk = 2 * w + (lane >> 5), zz = (kk & 3) | (((kk >> 3) & 1) << 2), pc = lane & 31, seg = (pc >> 1) ^ zz;
    const int part = (seg * 13) >> 6;
    const int col = seg * 16 + (isffn ? ftile0 * 16 : sub * 80) + (isffn ? 0 : DM - 80) * part;
    const int bo = kk * c.ldb2 + col * 2 + (pc & 1) * 16;
    c.bvoff = seg < ntile ? bo : OOB;
  }
  c.halo = !isffn && w < 2;
  { const int cl = (j8 + grp + half) % NCLS; c.rot = cl * 9 / NCLS; }
  asm volatile("" : "+v"(c.avoff), "+v"(c.asvoff), "+v"(c.bvoff));
  asm volatile("" : "+s"(c.rot));
  asm volatile("s_waitcnt lgkmcnt(0)" : "+s"(ka), "+s"(kb), "+s"(kc));
  auto ptr = [](unsigned lo, unsigned hi) { return (char*)(((u64)hi << 32) | lo); };
  const u16* x = (const u16*)ptr(ka[0], ka[1]); const u16* wqkv = (const u16*)ptr(ka[2], ka[3]); const u16* wo = (const u16*)ptr(ka[4], ka[5]);
  const u16* w1 = (const u16*)ptr(ka[6], ka[7]); const u16* w2 = (const u16*)ptr(kb[0], kb[1]); u16* out = (u16*)ptr(kb[2], kb[3]);
  char* ws = ptr(kc[0], kc[1]);
  c.rsA = mkrs(x, SEQ * DM * 2);
  c.rsB = mkrs(isffn ? w1 : wqkv, DM * c.ldb2);
  static_for<6>([&](auto I) { p1_req<decltype(I)::value, true>(c, lds0, 0); });
  c.rsH = mkrs(ws, H_BYTES);
  c.rsC = mkrs(ws + CTRL_OFF, 1 << 20);
  const int cg = (xcd >> 1) * 4 + half * 2 + (idx >> 4), rh = (idx >> 3) & 1, c3 = idx & 7;
  c.bt = ws + BT_OFF; c.bid = bid;
  c.rsT = mkrs(ws + BT_OFF + (size_t)c3 * BT_SLICE, BT_SLICE);
  c.wm = w >> 2;
  c.k0 = w < 4 ? 5 * w : hk0(w & 3);
  c.k0t = hk0(w & 3) + 9 + 2 * (w >> 2);
  c.tag = 0; c.taghi = 0;
  const int wn = w < 4 ? w : 7 - w;
  c.htile0 = 40 + ftile0;
  const int tsc = w == 3 ? CS_OFF : 2 * ST_BYTES + A_BYTES + 8192 + (w - 4) * 5120;
  v4u xs[10];
  const int sslot = w * 8 + (lane >> 3);
  const int kap = (7 - w) * 16 + grp;
  const bool dostat = isffn && w < 3, dotile = isffn && w >= 3 && kap < 74;
  if (dostat) {
    int rho = sub * 18 + (sslot < 18 ? sslot : 17);
    int row = grp * 128 - 16 + rho; row = row < 0 ? 0 : row;
    const int vo = row * (DM * 2) + (lane & 7) * 16;
    static_for<10>([&](auto I) { constexpr int i = decltype(I)::value; xs[i] = ld128<128 * i>(c.rsA, vo, 0); });
  } else if (dotile) {
    const v4i rw = mkrs(kap < 20 ? wo : w2, kap < 20 ? DM * DM * 2 : DM * FF * 2);
    int kq = kap;
    if (kap >= 20) asm volatile("s_sub_u32 %0, %0, 20" : "+s"(kq)::"scc");
    const int so = kq * 32 * (DM * 2);
#pragma unroll
    for (int i = 0; i < 5; i++) {
      const int pc = 64 * i + lane, rr = pc / 10;
      dma16(rw, lds0 + tsc + i * 1024, rr * (DM * 2) + sub * 160 + (pc - 10 * rr) * 16, so);
    }
  }
  __builtin_amdgcn_s_sleep(SSLEEP);
  static_for<6>([&](auto I) { p1_req<decltype(I)::value>(c, lds0 + ST_BYTES, 1); });
  static_for<3>([&](auto I) { p1_req<decltype(I)::value>(c, lds0 + 2 * ST_BYTES, 2); });
  c.side = dostat || dotile; c.cpf = cpf;
  if (c.side) asm volatile("s_waitcnt vmcnt(9)" ::: "memory");
  if (w == 7) dma16_sc1(c.rsC, lds0 + EPL_OFF, lane == 0 ? EPOCH_OFF : OOB, 0);
  if (dostat) {
    asm volatile("" : "+v"(xs[0]), "+v"(xs[1]), "+v"(xs[2]), "+v"(xs[3]), "+v"(xs[4]), "+v"(xs[5]), "+v"(xs[6]), "+v"(xs[7]), "+v"(xs[8]), "+v"(xs[9])::"memory");
    float s0 = 0, s1 = 0;
#pragma unroll
    for (int i = 0; i < 40; i += 2) { s0 = dot2(xs[i >> 2][i & 3], 0x3F803F80u, s0); s1 = dot2(xs[i >> 2][(i & 3) + 1], 0x3F803F80u, s1); }
    const float mean = row8_sum(s0 + s1) * (1.0f / DM);
    f32x2 q = {0.f, 0.f};
#pragma unroll
    for (int i = 0; i < 40; i++) { f32x2 d; d.x = bflo(xs[i >> 2][i & 3]); d.y = bfhi(xs[i >> 2][i & 3]); d -= mean; q += d * d; }
    const float var = row8_sum(q.x + q.y) * (1.0f / DM);
    const float sd = __builtin_sqrtf(rbf(rbf(var) + 1e-5f));
    v2u gr; gr.x = __builtin_bit_cast(unsigned, rbf(mean)); gr.y = __builtin_bit_cast(unsigned, rbf(sd));
    st64_sc1(c.rsC, ((lane & 7) == 0 && sslot < 18) ? STAT_OFF + grp * 2048 + (sub * 18 + sslot) * 8 : OOB, gr);
  } else if (dotile) {
    LAS char* tb = smem + tsc + ((lane >> 4) * 4 + ((lane & 15) >> 2)) * 160 + (lane & 3) * 8;
#pragma unroll
    for (int n = 0; n < 5; n++)
      st128_sc1(mkrs(ws + BT_OFF, 8 * BT_SLICE), lane * 16, sub * BT_SLICE + (kap * 5 + n) * 1024, __builtin_bit_cast(v4u, trfrag(tb + n * 32, 16 * 160)));
  }
  v4u B[5][5];
  {
    const int n4 = ntile - 12;
    c.tstart = wn <= n4 ? 4 * wn : 4 * n4 + 3 * (wn - n4);
    if (!isffn) c.ftile0 = 1 + 6 * c.wm;
    else { const int fi = (wn - n4) * 2 + c.wm; c.ftile0 = 1 + 2 * (n4 == 2 ? fi : (fi & 1)); }
    if (wn < n4) phase1<4, 1, 0>(c, smem, lds0, ws, B);
    else if (!isffn) phase1<3, 2, 3>(c, smem, lds0, ws, B);
    else phase1<3, 2, 1>(c, smem, lds0, ws, B);
  }
  const int r = lane & 15, g = lane >> 4;
  const int k0 = c.k0;
  auto ldB = [&](int s) { SB; ldB5(B[s], c.rsT, lane * 16, (k0 + s) * 5120); SB; };
  int foff;
  {
    int m;
    if (w < 4) m = lane < 2 ? (w >> 1) * 8 + (w & 1) * 2 + lane : -1;
    else {
      const int t0 = 2 * (k0 - 20), t1 = t0 + 27 - (w & 2);
      const int f0 = t0 < 56 ? t0 / 14 : 4 + (t0 - 56) / 13, f1 = t1 < 56 ? t1 / 14 : 4 + (t1 - 56) / 13, f = f0 + lane;
      m = f <= f1 ? (f >> 2) * 8 + 4 + (f & 3) : -1;
    }
    foff = m >= 0 ? FLAG_OFF + cg * 256 + m * 4 : OOB;
  }
  const int hso = ((cg * 4 + rh * 2) * NCT + 2 * k0) * 1024;
  v4u E[2][4];
  if (!isffn) {
    unsigned fl;
    const int row = (w & 1) * 64 + lane;
    const int pos = grp * 128 + row;
    LAS char* qp = smem + (row + 16) * IMG_LD + (w >> 1) * 40;
    unsigned qv[10];
#pragma unroll
    for (int i = 0; i < 5; i++) { const v2u t = *(LAS v2u*)(qp + i * 8); qv[2 * i] = t.x; qv[2 * i + 1] = t.y; }
    float lg[8];
    static_for<8>([&](auto K) {
      constexpr int k = decltype(K)::value;
      if constexpr (k < 5) B[3][k] = ldB1<k>(c.rsT, lane * 16, (k0 + 3) * 5120);
      LAS char* kp = qp + 160 - 2 * k * IMG_LD;
      float a0 = 0, a1 = 0;
#pragma unroll
      for (int i = 0; i < 5; i++) { const v2u t = *(LAS v2u*)(kp + i * 8); a0 = dot2(qv[2 * i], t.x, a0); a1 = dot2(qv[2 * i + 1], t.y, a1); }
      lg[k] = pos - 2 * k >= 0 ? a0 + a1 : -4e9f;
    });
    float mx = lg[0];
#pragma unroll
    for (int k = 1; k < 8; k++) mx = fmaxf(mx, lg[k]);
    float sum = 0;
    constexpr float C = 0.22360679774997896f * 1.4426950408889634f;
#pragma unroll
    for (int k = 0; k < 8; k++) { lg[k] = __builtin_amdgcn_exp2f((lg[k] - mx) * C); sum += lg[k]; }
    const float inv = __builtin_amdgcn_rcpf(sum);
    float o[20];
#pragma unroll
    for (int d = 0; d < 20; d++) o[d] = 0;
    static_for<8>([&](auto K) {
      constexpr int k = decltype(K)::value;
      if constexpr (k < 5) B[4][k] = ldB1<k>(c.rsT, lane * 16, (k0 + 4) * 5120);
      if constexpr (k == 5) fl = ld32_sc1(c.rsC, w >= 4 ? foff : OOB);
      const unsigned plo = pk2(lg[k] * inv, 0.f), phi = plo << 16;
      LAS char* vp = qp + 320 - 2 * k * IMG_LD;
#pragma unroll
      for (int i = 0; i < 5; i++) {
        const v2u t = *(LAS v2u*)(vp + i * 8);
        o[4 * i] = dot2(t.x, plo, o[4 * i]); o[4 * i + 1] = dot2(t.x, phi, o[4 * i + 1]);
        o[4 * i + 2] = dot2(t.y, plo, o[4 * i + 2]); o[4 * i + 3] = dot2(t.y, phi, o[4 * i + 3]);
      }
    });
#pragma unroll
    for (int i = 0; i < 5; i++) { v2u t; t.x = pk2(o[4 * i], o[4 * i + 1]); t.y = pk2(o[4 * i + 2], o[4 * i + 3]); *(LAS v2u*)(qp + i * 8) = t; }
    ldsbar();
#pragma unroll
    for (int i = 0; i < 3; i++) {
      const int b0 = w + 8 * i, b = b0 < 20 ? b0 : 19, rp = b / 5, t = b - 5 * rp;
      LAS char* sp = smem + (16 + rp * 32 + r) * IMG_LD + t * 32 + g * 8;
      const v2u lo = *(LAS v2u*)sp, hi = *(LAS v2u*)(sp + 16 * IMG_LD);
      v4u v; v.x = lo.x; v.y = lo.y; v.z = hi.x; v.w = hi.y;
      st128_sc1(c.rsH, lane * 16, b0 < 20 ? ((grp * 4 + rp) * NCT + sub * 5 + t) * 1024 : OOB, v);
    }
    if (w >= 4) {
      asm volatile("s_waitcnt vmcnt(3)" : "+v"(fl)::"memory");
      wait_flags(c, foff, fl);
      ldA4(E[0], c.rsH, lane * 16, hso); ldA4(E[1], c.rsH, lane * 16, hso + 2048);
    }
  }
  if (!isffn && w >= 4) waitbar<8>(); else waitbar<0>();
  st32_sc1(c.rsC, tid == 0 ? FLAG_OFF + grp * 256 + mem * 4 : OOB, c.tag);
  if (isffn && w < 4) {
    ldB(0); ldB(1); ldB(2); ldB(3); ldB(4);
    __builtin_amdgcn_s_sleep(FSLEEP2);
  }
  if (w < 4 && !isffn) __builtin_amdgcn_s_sleep(OSLEEP);
  if (isffn || w < 4) wait_flags(c, foff, ~c.tag);
  if (isffn && w >= 4) { ldA4(E[0], c.rsH, lane * 16, hso); ldA4(E[1], c.rsH, lane * 16, hso + 2048); }
  v2u xr[3];
  auto ldx = [&]() {
#pragma unroll
    for (int i = 0; i < 3; i++) {
      const int tl = w + 8 * i, tt = tl < 20 ? tl : 19, m = tt / 5, n = tt - 5 * m;
      xr[i] = ld64(c.rsA, (cg * 128 + rh * 64 + m * 16 + r) * (DM * 2) + (c3 * 80 + n * 16 + g * 4) * 2, 0);
    }
  };
  auto put = [&](const f32x4 (&acc)[4][5], int p0, int p1, int stride) {
    asm volatile("s_nop 7\n\ts_nop 3\n\ts_waitcnt vmcnt(0) lgkmcnt(0)" : "+v"(xr[0]), "+v"(xr[1]), "+v"(xr[2])::"memory");
#pragma unroll
    for (int m = 0; m < 4; m++)
#pragma unroll
      for (int n = 0; n < 5; n++) *(LAS f32x4*)(smem + (m < 2 ? p0 : p1) + ((m & 1) * 5 + n) * stride + lane * 16) = acc[m][n];
  };
  const int vo = lane * 16;
  if (w < 4) {
    v4u A[5][4];
    auto ldA = [&](int s) { ldA4(A[s], c.rsH, vo, hso + s * 2048); };
    ldA(0); ldA(1); ldA(2);
    f32x4 acc[4][5];
#pragma unroll
    for (int m = 0; m < 4; m++)
#pragma unroll
      for (int n = 0; n < 5; n++) acc[m][n] = f32x4{0, 0, 0, 0};
#pragma unroll
    for (int m = 0; m < 4; m++) TIE5(acc[m]);
#pragma unroll
    for (int s = 0; s < 5; s++) TIE5(B[s]);
    WAITA(A[0], 8); swap_pair(A[0][0], A[0][1]); swap_pair(A[0][2], A[0][3]);
    asm volatile("s_nop 1");
    static_for<5>([&](auto S) {
      constexpr int s = decltype(S)::value;
      static_for<20>([&](auto I) {
        constexpr int i = decltype(I)::value;
        mfma_ip(acc[i / 5][i % 5], B[s][i % 5], A[s][i / 5]);
        if constexpr ((s == 1 || s == 2) && i < 8 && (i & 1)) A[s + 2][i >> 1] = ldA1<(i >> 1)>(c.rsH, vo, hso + (s + 2) * 2048);
        if constexpr (s == 2 && i == 9) ldx();
        if constexpr (s < 4 && i == 13) WAITA(A[s + 1], (s == 0 ? 4 : s == 1 ? 4 : s == 2 ? 7 : 3));
        if constexpr (s < 4 && i == 14) swap_pair(A[s + 1][0], A[s + 1][1]);
        if constexpr (s < 4 && i == 15) swap_pair(A[s + 1][2], A[s + 1][3]);
      });
    });
    put(acc, w * 20480, w < 3 ? w * 20480 + 10240 : CS_OFF, 1024);
#pragma unroll
    for (int s = 0; s < 5; s++)
#pragma unroll
      for (int n = 0; n < 5; n++) asm volatile("" : "=v"(B[s][n]));
#pragma unroll
    for (int i = 0; i < 8; i++) asm volatile("" : "=v"(E[i >> 2][i & 3]));
  }
  int w_h = w;
  asm volatile("" : "+s"(w_h));
  if (w_h >= 4) {
    const int hw = w & 3;
    v4u A[14][4], Bs[14][5];
    const int last = hw < 2 ? 0 : OOB;
#pragma unroll
    for (int i = 0; i < 8; i++) A[i >> 2][i & 3] = E[i >> 2][i & 3];
    f32x4 acc[4][5];
#pragma unroll
    for (int m = 0; m < 4; m++)
#pragma unroll
      for (int n = 0; n < 5; n++) acc[m][n] = f32x4{0, 0, 0, 0};
#pragma unroll
    for (int m = 0; m < 4; m++) TIE5(acc[m]);
    LAS char* eb = smem + RING_OFF + hw * 1024 + lane * 16;
#pragma unroll
    for (int s = 0; s < 5; s++) {
      TIE5(B[s]);
#pragma unroll
      for (int n = 0; n < 5; n++) Bs[s][n] = B[s][n];
    }
    WAITA(A[0], 4); swap_pair(A[0][0], A[0][1]); swap_pair(A[0][2], A[0][3]);
    asm volatile("s_nop 1");
    static_for<14>([&](auto S) {
      constexpr int s = decltype(S)::value;
      static_for<20>([&](auto I) {
        constexpr int i = decltype(I)::value;
        mfma_ip(acc[i / 5][i % 5], Bs[s][i % 5], A[s][i / 5]);
        if constexpr (i < 4 * h_na(s)) {
          constexpr int k = h_a0(s) + (i >> 2);
          A[k][i & 3] = ldA1<(i & 3)>(c.rsH, k == 9 ? (vo | last) : vo, hso + h_k(k) * 2048);
        }
        if constexpr (s >= 1 && s <= 5 && i >= 4 && i < 9) Bs[s + 4][i - 4] = ldB1<i - 4>(c.rsT, s == 5 ? (vo | last) : vo, (k0 + h_k(s + 4)) * 5120);
        if constexpr (s == 8 && i == 9) ldx();
        if constexpr (s >= 9 && s < 13 && i >= 9 && i < 14) {
          SB; Bs[s + 1][i - 9] = *(LAS v4u*)(eb + (((s - 9) & 1) * 5 + i - 9) * 8192 + ((s - 9) >> 1) * 4096); SB;
        }
        if constexpr (s < 13 && i == 14) {
          asm volatile("s_waitcnt vmcnt(%4)" : "+v"(A[s + 1][0]), "+v"(A[s + 1][1]), "+v"(A[s + 1][2]), "+v"(A[s + 1][3]) : "i"(hwait(s + 1)) : "memory");
          if constexpr (s + 1 >= 5 && s + 1 <= 9) TIE5(Bs[s + 1]);
        }
        if constexpr (s < 13 && i == 15) swap_pair(A[s + 1][0], A[s + 1][1]);
        if constexpr (s < 13 && i == 16) swap_pair(A[s + 1][2], A[s + 1][3]);
      });
    });
    put(acc, RING_OFF + hw * 1024, RING_OFF + hw * 1024 + 4096, 8192);
  }
  ldsbar();
  if (w == 0 && bid == 0) { v2u t; t.x = c.tag; t.y = c.taghi; st64_sc1(c.rsC, lane == 0 ? EPOCH_OFF : OOB, t); }
#pragma unroll
  for (int i = 0; i < 3; i++) {
    const int tl = w + 8 * i;
    if (tl < 20) {
      const int m = tl / 5, n = tl - 5 * m;
      LAS char* ps = smem + tl * 1024 + lane * 16;
      LAS char* pf = smem + RING_OFF + (tl < 10 ? tl * 8192 : (tl - 10) * 8192 + 4096) + lane * 16;
      const f32x4 s = (*(LAS f32x4*)ps + *(LAS f32x4*)(ps + 20480)) + (*(LAS f32x4*)(ps + 40960) + *(LAS f32x4*)(ps + (tl < 10 ? 61440 : CS_OFF - 10240)));
      const f32x4 f = (*(LAS f32x4*)pf + *(LAS f32x4*)(pf + 1024)) + (*(LAS f32x4*)(pf + 2048) + *(LAS f32x4*)(pf + 3072));
      float y[4];
#pragma unroll
      for (int j = 0; j < 4; j++) {
        const float xv = (j & 1) ? bfhi(xr[i][j >> 1]) : bflo(xr[i][j >> 1]);
        y[j] = xv + s[j] * silu_f(f[j]);
      }
      v2u o; o.x = pk2(y[0], y[1]); o.y = pk2(y[2], y[3]);
      *(GAS v2u*)((GAS char*)out + (size_t)(cg * 128 + rh * 64 + m * 16 + r) * (DM * 2) + (c3 * 80 + n * 16 + g * 4) * 2) = o;
    }
  }
  asm volatile(".Lcode_end_mark:");
}
}

extern "C" void kernel_launch(void* const* d_in, const int* in_sizes, int n_in, void* d_out, int out_size, void* d_ws, size_t ws_size,
                              hipStream_t stream) {
  (void)hipFuncSetAttribute((const void*)fused_kernel, hipFuncAttributeMaxDynamicSharedMemorySize, LDS_BYTES);
  fused_kernel<<<256, 512, LDS_BYTES, stream>>>((const u16*)d_in[0], (const u16*)d_in[1], (const u16*)d_in[2], (const u16*)d_in[4],
                                                (const u16*)d_in[5], (u16*)d_out, (char*)d_ws);
}
```
